```python
import torch

def _softmax(x, axis=-1):
    return torch.softmax(x, dim=axis)
def _gelu(x):
    return torch.nn.functional.gelu(x, approximate="tanh")
def _silu(x):
    return torch.nn.functional.silu(x)
def _sigmoid(x):
    return torch.sigmoid(x)
def _layernorm(x, axis=-1, eps=1e-5):
    m = x.mean(dim=axis, keepdim=True)
    v = x.var(dim=axis, keepdim=True, unbiased=False)
    return (x - m) / torch.sqrt(v + eps)
def _scatter_add(tgt, idx, src, axis=0):
    return tgt.clone().index_add_(axis, idx, src)
def _segment_sum(src, seg, n):
    out = torch.zeros((n,) + src.shape[1:], dtype=src.dtype)
    return out.index_add_(0, seg, src)
def _pad(x, pw):
    flat = tuple(v for pair in reversed(pw) for v in pair)
    return torch.nn.functional.pad(x, flat)

kernel_name = 'gated_mix__compressed_kv_cla__moe_arch_mod__prompt'

def setup_inputs():

    _w = (lambda a: (lambda t: t.to(torch.bfloat16) if t.is_floating_point() else t)(torch.as_tensor(a)))
    def _A_setup():
        B, H, Sq, Sk, D, M = 1, 32, 2048, 2048, 128, 512
        Dc = 4 * D
        return {
            "x": _w((lambda *s: torch.randn(*s, dtype=torch.bfloat16) * 0.05)(B, Sq, M)),
            "c_kv": _w((lambda *s: torch.randn(*s, dtype=torch.bfloat16) * 0.8)(B, Sk, Dc)),
            "w_q": _w((lambda *s: torch.randn(*s, dtype=torch.bfloat16) * 6.4)(M, H, D) * (M ** -0.5)),
            "w_uk": _w((lambda *s: torch.randn(*s, dtype=torch.bfloat16) * 6.4)(Dc, H, D) * (Dc ** -0.5)),
            "w_uv": _w((lambda *s: torch.randn(*s, dtype=torch.bfloat16) * 0.8)(Dc, H, D) * (Dc ** -0.5)),
            "w_o": _w((lambda *s: torch.randn(*s, dtype=torch.bfloat16) * 0.8)(H, D, M) * ((H * D) ** -0.5)),
        }
    _A_in = _A_setup()

    def _B_setup():
        B, S, D, F, C = 1, 2048, 512, 1792, 2
        return {
            "x": _w((lambda *s: torch.randn(*s, dtype=torch.bfloat16) * 0.05)(B, S, D)),
            "w_r": _w((lambda *s: torch.randn(*s, dtype=torch.bfloat16) * 3.2)(D)),
            "w_gate": _w((lambda *s: torch.randn(*s, dtype=torch.bfloat16) * 0.2)(D, F)),
            "w_up": _w((lambda *s: torch.randn(*s, dtype=torch.bfloat16) * 0.2)(D, F)),
            "w_down": _w((lambda *s: torch.randn(*s, dtype=torch.bfloat16) * 0.2)(F, D)),
        }
    _B_in = _B_setup()

    return {
        "A_x": _A_in["x"],
        "A_c_kv": _A_in["c_kv"],
        "A_w_q": _A_in["w_q"],
        "A_w_uk": _A_in["w_uk"],
        "A_w_uv": _A_in["w_uv"],
        "A_w_o": _A_in["w_o"],
        "B_x": _B_in["x"],
        "B_w_r": _B_in["w_r"],
        "B_w_gate": _B_in["w_gate"],
        "B_w_up": _B_in["w_up"],
        "B_w_down": _B_in["w_down"],
        "gate_w": _w((lambda *s: torch.randn(*s, dtype=torch.bfloat16) * 0.05)(512) * 16.0),
    }

def reference(A_x, A_c_kv, A_w_q, A_w_uk, A_w_uv, A_w_o, B_x, B_w_r, B_w_gate, B_w_up, B_w_down, gate_w):
    def _A_ref(x, c_kv, w_q, w_uk, w_uv, w_o):
        B, Sq, M = x.shape[0], x.shape[1], x.shape[2]
        Sk = c_kv.shape[1]
        H, D = w_q.shape[1], w_q.shape[2]
        q = torch.einsum("bqm,mhd->bhqd", x, w_q)
        k = torch.einsum("bkc,chd->bhkd", c_kv, w_uk)
        v = torch.einsum("bkc,chd->bhkd", c_kv, w_uv)
        scores = torch.einsum("bhqd,bhkd->bhqk", q, k) * (D ** -0.5)
        off = Sk - Sq
        qi = torch.arange(Sq)[:, None]
        kj = torch.arange(Sk)[None, :]
        cmask = kj <= (qi + off)
        scores = torch.where(cmask[None, None], scores, -1e9)
        p = _softmax(scores, axis=-1)
        ctx = torch.einsum("bhqk,bhkd->bhqd", p, v)
        return torch.einsum("bhqd,hdm->bqm", ctx, w_o)

    def _B_ref(x, w_r, w_gate, w_up, w_down):
        B, S, D = x.shape
        C = 2
        r = x @ w_r
        idx = torch.argsort(-r, axis=-1)[:, :C]
        idx_e = (lambda x, n, axis: torch.repeat_interleave(x, n, dim=axis))(idx.reshape(B, C, 1), D, axis=2)
        x_sel = (lambda x, i, axis: torch.take_along_dim(x, i, dim=axis))(x, idx_e, axis=1)
        g = _silu(x_sel @ w_gate)
        u = x_sel @ w_up
        y = (g * u) @ w_down
        r_sel = _sigmoid((lambda x, i, axis: torch.take_along_dim(x, i, dim=axis))(r, idx, axis=1))
        y = y * r_sel.reshape(B, C, 1)
        b_off = (torch.arange(B) * S).reshape(B, 1)
        flat_idx = (idx + b_off).reshape(B * C)
        zero = (x * 0.0).reshape(B * S, D)
        upd = _scatter_add(zero, flat_idx, y.reshape(B * C, D), axis=0)
        return x + upd.reshape(B, S, D)

    x = A_x
    g = _sigmoid((x * gate_w).sum(-1, keepdims=True))
    a = _A_ref(x, A_c_kv, A_w_q, A_w_uk, A_w_uv, A_w_o)
    b = _B_ref(x, B_w_r, B_w_gate, B_w_up, B_w_down)
    return g * a + (1 - g) * b
```

```cpp
#include <hip/hip_runtime.h>
#include <hip/hip_bf16.h>
#include <stdint.h>

typedef unsigned short u16;
typedef short bf16x8 __attribute__((ext_vector_type(8)));
typedef short s16x4 __attribute__((ext_vector_type(4)));
typedef short v4i16_t __attribute__((ext_vector_type(4)));
typedef float f32x16 __attribute__((ext_vector_type(16)));
typedef float f32x4 __attribute__((ext_vector_type(4)));
typedef unsigned u32x4 __attribute__((ext_vector_type(4)));
typedef unsigned u32x2 __attribute__((ext_vector_type(2)));
#define LAS __attribute__((address_space(3)))
#define DI __device__ __forceinline__
#define MFMA(a, b, c) __builtin_amdgcn_mfma_f32_32x32x16_bf16((a), (b), (c), 0, 0, 0)
#define SBAR() __builtin_amdgcn_sched_barrier(0)

constexpr int SEQ = 2048, DM = 512, NH = 32, HD = 128, NHD = NH * HD;
constexpr int FF = 1792;
constexpr int MOE_WGS = 224, MOE_COLS = 8;

constexpr size_t WS_KF = 0;
constexpr size_t WS_VF = WS_KF + (size_t)SEQ * NHD * 2;
constexpr size_t WS_CTX = WS_VF + (size_t)SEQ * NHD * 2;
constexpr size_t WS_R = WS_CTX + (size_t)SEQ * NHD * 2;
constexpr size_t WS_G = WS_R + SEQ * 4;
constexpr size_t WS_SEL = WS_G + SEQ * 4;
constexpr size_t WS_YP = WS_SEL + 256;

DI unsigned cvtpk(float lo, float hi) {
    typedef float f32x2 __attribute__((ext_vector_type(2)));
    typedef __bf16 bf16x2 __attribute__((ext_vector_type(2)));
    f32x2 v = {lo, hi}; bf16x2 b = __builtin_convertvector(v, bf16x2); return __builtin_bit_cast(unsigned, b);
}
DI float bf2f(u16 v) { return __uint_as_float((unsigned)v << 16); }
DI float bfround(float x) { return __uint_as_float(cvtpk(x, x) << 16); }
DI bf16x8 pack8(const f32x16& x, int s, float sc) {
    u32x4 w = {cvtpk(x[8 * s] * sc, x[8 * s + 1] * sc), cvtpk(x[8 * s + 2] * sc, x[8 * s + 3] * sc),
               cvtpk(x[8 * s + 4] * sc, x[8 * s + 5] * sc), cvtpk(x[8 * s + 6] * sc, x[8 * s + 7] * sc)};
    return __builtin_bit_cast(bf16x8, w);
}
DI bf16x8 pack8(const f32x16& x, int s) {
    u32x4 w = {cvtpk(x[8 * s], x[8 * s + 1]), cvtpk(x[8 * s + 2], x[8 * s + 3]),
               cvtpk(x[8 * s + 4], x[8 * s + 5]), cvtpk(x[8 * s + 6], x[8 * s + 7])};
    return __builtin_bit_cast(bf16x8, w);
}
DI void glds16(const void* sbase, unsigned voff, unsigned lds_addr) {
    asm volatile("s_mov_b32 m0, %2\n\ts_nop 0\n\tglobal_load_lds_dwordx4 %0, %1" :: "v"(voff), "s"(sbase), "s"(lds_addr) : "memory");
}
DI void glds16x4(const void* sbase, unsigned voff, unsigned lds_addr) {
    asm volatile("s_mov_b32 m0, %2\n\ts_nop 0\n\tglobal_load_lds_dwordx4 %0, %1\n\tglobal_load_lds_dwordx4 %0, %1 offset:1024\n\t"
                 "global_load_lds_dwordx4 %0, %1 offset:2048\n\tglobal_load_lds_dwordx4 %0, %1 offset:3072"
                 :: "v"(voff), "s"(sbase), "s"(lds_addr) : "memory");
}
#define WAIT_BAR(N) asm volatile("s_waitcnt vmcnt(" #N ") lgkmcnt(0)\n\ts_barrier" ::: "memory")
typedef LAS const char* lptr;
DI bf16x8 ldsr128(lptr p) { return *(const LAS bf16x8*)p; }
DI s16x4 ldstr(lptr p) { return __builtin_bit_cast(s16x4, __builtin_amdgcn_ds_read_tr16_b64_v4i16((LAS v4i16_t*)p)); }
DI bf16x8 cat(s16x4 a, s16x4 b) { return __builtin_shufflevector(a, b, 0, 1, 2, 3, 4, 5, 6, 7); }
DI float wave_sum(float v) {
#pragma unroll
    for (int o = 32; o >= 1; o >>= 1) v += __shfl_xor(v, o);
    return v;
}
DI float sigmoidf_(float x) { return 1.0f / (1.0f + __expf(-x)); }

constexpr int K1_STAGE = 65536, K1_LDS = 2 * K1_STAGE;
__global__ __launch_bounds__(512, 2)
void kv_proj_kernel(const u16* __restrict__ ckv, const u16* __restrict__ wuk, const u16* __restrict__ wuv,
                    const u16* __restrict__ ax, const u16* __restrict__ gatew, const u16* __restrict__ bx, const u16* __restrict__ wr,
                    char* __restrict__ ws) {
    extern __shared__ __attribute__((aligned(16))) char lds_[];
    const lptr lds = (lptr)lds_;
    const unsigned lds0 = (unsigned)(uintptr_t)lds;
    const int tid = threadIdx.x, lane = tid & 63, l31 = lane & 31, hi = lane >> 5;
    const int wid = __builtin_amdgcn_readfirstlane(tid >> 6);
    const int bid = blockIdx.x, h = (bid & 7) * 4 + ((bid >> 3) & 3), p = bid >> 5;
    const int wr_ = wid >> 1, wc = wid & 1;

    unsigned voffC[4], voffW[2];
#pragma unroll
    for (int j = 0; j < 4; ++j) { const int n = 4 * wid + j, row = 8 * n + (lane >> 3);
        voffC[j] = row * 1024 + (((lane & 7) ^ ((row >> 1) & 7)) << 4); }
#pragma unroll
    for (int j = 0; j < 2; ++j) { const int n = 2 * wid + j, row = 4 * n + (lane >> 4);
        voffW[j] = row * 8192 + (((lane & 15) ^ ((row & 3) << 2)) << 4); }
    const char* cbase = (const char*)ckv + (size_t)p * 256 * 1024;
    const char* kbase = (const char*)wuk + h * 256;
    const char* vbase = (const char*)wuv + h * 256;
    auto stage = [&](int s, int buf) {
        const unsigned b = lds0 + buf * K1_STAGE;
#pragma unroll
        for (int j = 0; j < 4; ++j) glds16(cbase + s * 128, voffC[j], b + (4 * wid + j) * 1024);
#pragma unroll
        for (int j = 0; j < 2; ++j) glds16(kbase + (size_t)s * 64 * 8192, voffW[j], b + 32768 + (2 * wid + j) * 1024);
#pragma unroll
        for (int j = 0; j < 2; ++j) glds16(vbase + (size_t)s * 64 * 8192, voffW[j], b + 49152 + (2 * wid + j) * 1024);
    };
    const int tok = bid * 8 + wid;
    const bf16x8 bxv = *(const bf16x8*)(bx + tok * DM + lane * 8), wrv = *(const bf16x8*)(wr + lane * 8);
    const bf16x8 axv = *(const bf16x8*)(ax + tok * DM + lane * 8), gwv = *(const bf16x8*)(gatew + lane * 8);
    stage(0, 0);
    {
        float sr = 0.f, sg = 0.f;
#pragma unroll
        for (int j = 0; j < 8; ++j) { sr += bf2f((u16)bxv[j]) * bf2f((u16)wrv[j]); sg += bf2f((u16)axv[j]) * bf2f((u16)gwv[j]); }
        sr = wave_sum(sr); sg = wave_sum(sg);
        if (lane == 0) { ((float*)(ws + WS_R))[tok] = bfround(sr); ((float*)(ws + WS_G))[tok] = sigmoidf_(sg); }
    }
    lptr cA[4];
#pragma unroll
    for (int ks = 0; ks < 4; ++ks) cA[ks] = lds + (64 * wr_ + l31) * 128 + (((2 * ks + hi) ^ ((l31 >> 1) & 7)) << 4);
    const int tq = (lane & 15) >> 2, tp = lane & 3, tblk = (lane >> 4) & 1;
    lptr wA[2];
#pragma unroll
    for (int cb = 0; cb < 2; ++cb) wA[cb] = lds + 32768 + (8 * hi + tq) * 256 + (((2 * wc + cb) ^ tq) << 6) + tblk * 32 + tp * 8;

    f32x16 accK[2][2] = {}, accV[2][2] = {};
    WAIT_BAR(0);
#pragma unroll 2
    for (int s = 0; s < 8; ++s) {
        const int buf = s & 1;
        if (s + 1 < 8) stage(s + 1, buf ^ 1);
        const int bo = buf * K1_STAGE;
#pragma unroll
        for (int ks = 0; ks < 4; ++ks) {
            bf16x8 cf[2], wk[2], wv[2];
#pragma unroll
            for (int g = 0; g < 2; ++g) cf[g] = ldsr128(cA[ks] + bo + g * 4096);
#pragma unroll
            for (int cb = 0; cb < 2; ++cb) {
                wk[cb] = cat(ldstr(wA[cb] + bo + ks * 4096), ldstr(wA[cb] + bo + ks * 4096 + 1024));
                wv[cb] = cat(ldstr(wA[cb] + bo + 16384 + ks * 4096), ldstr(wA[cb] + bo + 16384 + ks * 4096 + 1024));
            }
#pragma unroll
            for (int g = 0; g < 2; ++g)
#pragma unroll
                for (int cb = 0; cb < 2; ++cb) {
                    accK[g][cb] = MFMA(wk[cb], cf[g], accK[g][cb]);
                    accV[g][cb] = MFMA(cf[g], wv[cb], accV[g][cb]);
                }
        }
        WAIT_BAR(0);
    }
    char* Kf = ws + WS_KF; char* Vf = ws + WS_VF;
#pragma unroll
    for (int g = 0; g < 2; ++g)
#pragma unroll
        for (int cb = 0; cb < 2; ++cb)
#pragma unroll
            for (int s = 0; s < 2; ++s) {
                const size_t off = ((size_t)((h * 64 + 8 * p + 2 * wr_ + g) * 8 + 2 * (2 * wc + cb) + s) * 64 + lane) * 16;
                *(bf16x8*)(Kf + off) = pack8(accK[g][cb], s);
                *(bf16x8*)(Vf + off) = pack8(accV[g][cb], s);
            }
}

DI unsigned umax_(unsigned a, unsigned b) { return a > b ? a : b; }
DI unsigned umin_(unsigned a, unsigned b) { return a < b ? a : b; }
DI void moe_part(const u16* __restrict__ bx, const u16* __restrict__ wg, const u16* __restrict__ wu, const u16* __restrict__ wd,
                 char* __restrict__ ws, char* lds_, int bid, int tid) {
    const int lane = tid & 63, wid = tid >> 6;
    unsigned* sk = (unsigned*)lds_;
    float* xs = (float*)(lds_ + 256);
    float* red = (float*)(lds_ + 256 + 4096);
    float* hs = (float*)(lds_ + 256 + 4096 + 1024);
    const int cg = (bid & 7) * 28 + (bid >> 3);
    const bool work = (bid >> 3) < 28;
    const int f0 = cg * MOE_COLS;
    const int col = tid & 15, ksl = tid >> 4;
    u16 wv[16]; u16 dv[8];
    if (work) {
        const u16* wsrc = (col < 8 ? wg : wu) + f0 + (col & 7) + (size_t)ksl * 16 * FF;
#pragma unroll
        for (int j = 0; j < 16; ++j) wv[j] = wsrc[(size_t)j * FF];
#pragma unroll
        for (int j = 0; j < 8; ++j) dv[j] = wd[(size_t)(f0 + j) * DM + tid];
    }
    const f32x4 rv = *(const f32x4*)((const float*)(ws + WS_R) + tid * 4);
    unsigned k1 = 0, k2 = 0;
#pragma unroll
    for (int j = 0; j < 4; ++j) {
        const unsigned u = __float_as_uint(rv[j]) >> 16;
        const unsigned o = (u & 0x8000u) ? (~u & 0xFFFFu) : (u | 0x8000u);
        const unsigned key = (o << 16) | (unsigned)(0xFFFF - (tid * 4 + j));
        k2 = umax_(k2, umin_(k1, key)); k1 = umax_(k1, key);
    }
#pragma unroll
    for (int o = 32; o >= 1; o >>= 1) {
        const unsigned b1 = __shfl_xor(k1, o), b2 = __shfl_xor(k2, o);
        k2 = umax_(umin_(k1, b1), umax_(k2, b2)); k1 = umax_(k1, b1);
    }
    if (lane == 0) { sk[wid * 2] = k1; sk[wid * 2 + 1] = k2; }
    __syncthreads();
    k1 = 0; k2 = 0;
#pragma unroll
    for (int j = 0; j < 16; ++j) { const unsigned key = sk[j]; k2 = umax_(k2, umin_(k1, key)); k1 = umax_(k1, key); }
    const int i0 = 0xFFFF - (int)(k1 & 0xFFFFu), i1 = 0xFFFF - (int)(k2 & 0xFFFFu);
    if (bid == 0 && tid == 0) {
        const float* r = (const float*)(ws + WS_R);
        int* sel = (int*)(ws + WS_SEL);
        sel[0] = i0; sel[1] = i1;
        ((float*)sel)[2] = sigmoidf_(r[i0]); ((float*)sel)[3] = sigmoidf_(r[i1]);
    }
    if (work) {
        xs[tid] = bf2f(bx[i0 * DM + tid]); xs[512 + tid] = bf2f(bx[i1 * DM + tid]);
    }
    __syncthreads();
    if (work) {
        float a0 = 0.f, a1 = 0.f;
#pragma unroll
        for (int j = 0; j < 16; ++j) { const float w = bf2f(wv[j]); a0 += xs[ksl * 16 + j] * w; a1 += xs[512 + ksl * 16 + j] * w; }
        a0 += __shfl_xor(a0, 16); a0 += __shfl_xor(a0, 32); a1 += __shfl_xor(a1, 16); a1 += __shfl_xor(a1, 32);
        if (lane < 16) { red[(wid * 16 + lane) * 2] = a0; red[(wid * 16 + lane) * 2 + 1] = a1; }
    }
    __syncthreads();
    if (work && tid < 16) {
        const int c = tid & 7, t = tid >> 3;
        float g = 0.f, u = 0.f;
#pragma unroll
        for (int w = 0; w < 8; ++w) { g += red[(w * 16 + c) * 2 + t]; u += red[(w * 16 + 8 + c) * 2 + t]; }
        g = bfround(g); u = bfround(u);
        const float sg = bfround(g * sigmoidf_(g));
        hs[t * 8 + c] = bfround(sg * u);
    }
    __syncthreads();
    if (work) {
        float y0 = 0.f, y1 = 0.f;
#pragma unroll
        for (int j = 0; j < 8; ++j) { const float w = bf2f(dv[j]); y0 += hs[j] * w; y1 += hs[8 + j] * w; }
        float* yp = (float*)(ws + WS_YP) + (size_t)cg * 1024;
        yp[tid] = y0; yp[512 + tid] = y1;
    }
    __syncthreads();
}

constexpr int K2_QST = 49152, K2_SLOT = 32768, K2_QB = 131072, K2_LDS = 163840;
constexpr float QSCALE = 0.08838834764831845f * 1.4426950408889634f;
constexpr float THR = 8.0f;

struct SMState { float mhat, l; f32x16 negm; };

DI void attn_tile(lptr kp, const bf16x8 (&qf)[8], f32x16 (&o)[4], SMState& st, bool first, bool mask, int qrel  ) {
    f32x16 s0 = st.negm, s1 = st.negm;
#pragma unroll
    for (int ds = 0; ds < 8; ++ds) {
        const bf16x8 k0 = ldsr128(kp + ds * 1024), k1 = ldsr128(kp + 8192 + ds * 1024);
        s0 = MFMA(k0, qf[ds], s0); s1 = MFMA(k1, qf[ds], s1);
    }
    if (mask) {
        const float NEG = -__builtin_inff();
#pragma unroll
        for (int r = 0; r < 16; ++r) { const int c = (r & 3) + 8 * (r >> 2);
            if (c > qrel) s0[r] = NEG;
            if (c + 32 > qrel) s1[r] = NEG; }
    }
    float a = fmaxf(fmaxf(s0[0], s0[1]), s1[0]), b = fmaxf(fmaxf(s0[2], s0[3]), s1[1]);
    a = fmaxf(fmaxf(a, s1[2]), s1[3]);
#pragma unroll
    for (int r = 4; r < 16; r += 4) {
        a = fmaxf(fmaxf(a, s0[r]), s0[r + 1]); b = fmaxf(fmaxf(b, s0[r + 2]), s0[r + 3]);
        a = fmaxf(fmaxf(a, s1[r]), s1[r + 1]); b = fmaxf(fmaxf(b, s1[r + 2]), s1[r + 3]);
    }
    float rm = fmaxf(a, b);
    { auto rr = __builtin_amdgcn_permlane32_swap(__float_as_uint(rm), __float_as_uint(rm), false, false);
      rm = fmaxf(__uint_as_float(rr[0]), __uint_as_float(rr[1])); }
    if (__builtin_expect(first || __any(rm > THR), 0)) {
        const float dl = first ? rm : fmaxf(rm, 0.f), f = first ? 1.0f : __builtin_amdgcn_exp2f(-dl);
        st.mhat += dl; st.l *= f;
#pragma unroll
        for (int r = 0; r < 16; ++r) { s0[r] -= dl; s1[r] -= dl; st.negm[r] = -st.mhat; }
#pragma unroll
        for (int d = 0; d < 4; ++d)
#pragma unroll
            for (int r = 0; r < 16; ++r) o[d][r] *= f;
    }
    float sum = 0.f;
#pragma unroll
    for (int r = 0; r < 16; ++r) { s0[r] = __builtin_amdgcn_exp2f(s0[r]); s1[r] = __builtin_amdgcn_exp2f(s1[r]); sum += s0[r] + s1[r]; }
    st.l += sum;
    bf16x8 pf[4] = {pack8(s0, 0), pack8(s0, 1), pack8(s1, 0), pack8(s1, 1)};
#pragma unroll
    for (int kb = 0; kb < 2; ++kb)
#pragma unroll
        for (int s = 0; s < 2; ++s)
#pragma unroll
            for (int d = 0; d < 4; ++d) {
                const bf16x8 v = ldsr128(kp + 16384 + kb * 8192 + (2 * d + s) * 1024);
                o[d] = MFMA(v, pf[2 * kb + s], o[d]);
            }
}

__global__ __launch_bounds__(512, 2)
void attn_kernel(const u16* __restrict__ x, const u16* __restrict__ wq,
                 const u16* __restrict__ bx, const u16* __restrict__ wg, const u16* __restrict__ wu, const u16* __restrict__ wd,
                 char* __restrict__ ws) {
    extern __shared__ __attribute__((aligned(16))) char lds_[];
    const lptr lds = (lptr)lds_;
    const unsigned lds0 = (unsigned)(uintptr_t)lds;
    const int tid = threadIdx.x, lane = tid & 63, l31 = lane & 31, hi = lane >> 5;
    const int wid = __builtin_amdgcn_readfirstlane(tid >> 6);
    const int bid = blockIdx.x, h = (bid & 7) * 4 + ((bid >> 3) & 3), bi = bid >> 5;
    const int grp = wid >> 2, rg = wid & 3;
    const int blkA = bi, blkB = 15 - bi;

    moe_part(bx, wg, wu, wd, ws, lds_, bid, tid);

    bf16x8 qf[8];
    {
        unsigned voffX[4], voffW[2];
        const int myblk = grp ? blkB : blkA;
#pragma unroll
        for (int j = 0; j < 4; ++j) { const int r = 8 * j + (lane >> 3);
            voffX[j] = r * 1024 + (((lane & 7) ^ (((8 * j + (lane >> 3)) >> 1) & 7)) << 4); }
#pragma unroll
        for (int j = 0; j < 2; ++j) { const int n = 2 * wid + j, row = 4 * n + (lane >> 4);
            voffW[j] = row * 8192 + (((lane & 15) ^ ((row & 3) << 2)) << 4); }
        const char* xbase = (const char*)x + (size_t)(myblk * 128 + rg * 32) * 1024;
        const char* wbase = (const char*)wq + h * 256;
        auto stage = [&](int s, int slot) {
            const unsigned b = lds0 + slot * K2_QST;
#pragma unroll
            for (int j = 0; j < 4; ++j) glds16(xbase + s * 128, voffX[j], b + (4 * wid + j) * 1024);
#pragma unroll
            for (int j = 0; j < 2; ++j) glds16(wbase + (size_t)s * 64 * 8192, voffW[j], b + 32768 + (2 * wid + j) * 1024);
        };
        lptr xA[4];
#pragma unroll
        for (int ks = 0; ks < 4; ++ks) xA[ks] = lds + (32 * wid + l31) * 128 + (((2 * ks + hi) ^ ((l31 >> 1) & 7)) << 4);
        const int tq = (lane & 15) >> 2, tp = lane & 3, tblk = (lane >> 4) & 1;
        lptr wA[4];
#pragma unroll
        for (int c = 0; c < 4; ++c) wA[c] = lds + 32768 + (8 * hi + tq) * 256 + ((c ^ tq) << 6) + tblk * 32 + tp * 8;
        f32x16 acc[4] = {};
        stage(0, 0); stage(1, 1);
#pragma unroll
        for (int s = 0; s < 8; ++s) {
            if (s < 7) { WAIT_BAR(6); } else { WAIT_BAR(0); }
            if (s + 2 < 8) stage(s + 2, (s + 2) % 3);
            const int bo = (s % 3) * K2_QST;
#pragma unroll
            for (int ks = 0; ks < 4; ++ks) {
                const bf16x8 xf = ldsr128(xA[ks] + bo);
#pragma unroll
                for (int c = 0; c < 4; ++c) {
                    const bf16x8 wf = cat(ldstr(wA[c] + bo + ks * 4096), ldstr(wA[c] + bo + ks * 4096 + 1024));
                    acc[c] = MFMA(wf, xf, acc[c]);
                }
            }
        }
#pragma unroll
        for (int c = 0; c < 4; ++c) { qf[2 * c] = pack8(acc[c], 0, QSCALE); qf[2 * c + 1] = pack8(acc[c], 1, QSCALE); }
    }

    const char* Kh = ws + WS_KF + (size_t)h * 64 * 8192;
    const char* Vh = ws + WS_VF + (size_t)h * 64 * 8192;
    const unsigned vlin = lane * 16;
    auto dma_shared = [&](int T, int slot) {
        const char* src = (wid < 4 ? Kh : Vh) + (size_t)T * 16384 + (wid & 3) * 4096;
        glds16x4(src, vlin, lds0 + slot * K2_SLOT + wid * 4096);
    };
    auto dma_pair = [&](int T0, int slot0) {
        const char* srcK = Kh + (size_t)(T0 + grp) * 16384 + rg * 4096;
        const char* srcV = Vh + (size_t)(T0 + grp) * 16384 + rg * 4096;
        glds16x4(srcK, vlin, lds0 + (slot0 + grp) * K2_SLOT + rg * 4096);
        glds16x4(srcV, vlin, lds0 + (slot0 + grp) * K2_SLOT + 16384 + rg * 4096);
    };
    const int n1 = 2 * bi + 2;
    WAIT_BAR(0);
    dma_shared(0, 0);
    if (grp == 1) {
#pragma unroll
        for (int c = 0; c < 8; ++c) *(LAS bf16x8*)((LAS char*)lds_ + K2_QB + rg * 8192 + c * 1024 + lane * 16) = qf[c];
    }
    f32x16 o[4] = {};
    SMState st; st.mhat = 0.f; st.l = 0.f; st.negm = f32x16{};
    const lptr kp0 = lds + lane * 16;
    char* ctx = ws + WS_CTX;
    auto store_ctx = [&](int blk, int d, const f32x16& v, float inv) {
        const size_t off = ((size_t)((4 * blk + rg) * 32 + h) * 8 + 2 * d) * 1024 + lane * 16;
        *(bf16x8*)(ctx + off) = pack8(v, 0, inv);
        *(bf16x8*)(ctx + off + 1024) = pack8(v, 1, inv);
    };
    auto half_sum = [&](float v) { auto rr = __builtin_amdgcn_permlane32_swap(__float_as_uint(v), __float_as_uint(v), false, false);
                                   return __uint_as_float(rr[0]) + __uint_as_float(rr[1]); };
    bool first = true;
    for (int n = 0; n < 17; ++n) {
        WAIT_BAR(0);
        if (n + 1 < n1) dma_shared(n + 1, 2 * ((n + 1) & 1));
        else if (n + 1 < 17) dma_pair(n1 + 2 * (n + 1 - n1), 2 * ((n + 1) & 1));
        const bool ph2 = n >= n1;
        const int T = ph2 ? n1 + 2 * (n - n1) + grp : n, kb0 = 64 * T;
        const int q0 = 128 * ((grp || ph2) ? blkB : blkA) + 32 * rg;
        const lptr kp = kp0 + (2 * (n & 1) + (ph2 ? grp : 0)) * K2_SLOT;
        if (kb0 <= q0 + 31) {
            attn_tile(kp, qf, o, st, first, kb0 + 63 > q0, q0 + l31 - kb0 - 4 * hi);
            first = false;
        }
        if (n == n1 - 1 && grp == 0) {
            const float inv = 1.0f / half_sum(st.l);
#pragma unroll
            for (int d = 0; d < 4; ++d) store_ctx(blkA, d, o[d], inv);
#pragma unroll
            for (int d = 0; d < 4; ++d) o[d] = f32x16{};
#pragma unroll
            for (int c = 0; c < 8; ++c) qf[c] = ldsr128(lds + K2_QB + rg * 8192 + c * 1024 + lane * 16);
            st.mhat = 0.f; st.l = 0.f; st.negm = f32x16{}; first = true;
        }
    }
    WAIT_BAR(0);
    {
        const float lsum = half_sum(st.l);
        LAS char* xch = (LAS char*)lds_;
        LAS char* mine = xch + wid * 8192;
#pragma unroll
        for (int dd = 0; dd < 2; ++dd) {
            const f32x16& v = grp ? o[dd] : o[2 + dd];
#pragma unroll
            for (int q4 = 0; q4 < 4; ++q4)
                *(LAS f32x4*)(mine + (dd * 4 + q4) * 1024 + lane * 16) = (f32x4){v[4 * q4], v[4 * q4 + 1], v[4 * q4 + 2], v[4 * q4 + 3]};
        }
        LAS float* ml = (LAS float*)(xch + 65536);
        ml[wid * 128 + lane] = st.mhat; ml[wid * 128 + 64 + lane] = lsum;
        WAIT_BAR(0);
        const int ow = wid ^ 4;
        const float m2 = ml[ow * 128 + lane], l2 = ml[ow * 128 + 64 + lane];
        const float mm = fmaxf(st.mhat, m2);
        const float f1 = __builtin_amdgcn_exp2f(st.mhat - mm), f2 = __builtin_amdgcn_exp2f(m2 - mm);
        const float inv = 1.0f / (lsum * f1 + l2 * f2);
        const float c1 = f1 * inv, c2 = f2 * inv;
        const LAS char* theirs = xch + ow * 8192;
#pragma unroll
        for (int dd = 0; dd < 2; ++dd) {
            f32x16 v = grp ? o[2 + dd] : o[dd];
#pragma unroll
            for (int q4 = 0; q4 < 4; ++q4) {
                const f32x4 t = *(const LAS f32x4*)(theirs + (dd * 4 + q4) * 1024 + lane * 16);
#pragma unroll
                for (int e = 0; e < 4; ++e) v[4 * q4 + e] = v[4 * q4 + e] * c1 + t[e] * c2;
            }
            store_ctx(blkB, 2 * grp + dd, v, 1.0f);
        }
    }
}

constexpr int K3_SLOT = 32768, K3_LDS = 4 * K3_SLOT;
__global__ __launch_bounds__(512, 2)
void o_proj_kernel(const u16* __restrict__ wo, const u16* __restrict__ bx, const char* __restrict__ ws, u16* __restrict__ out) {
    extern __shared__ __attribute__((aligned(16))) char lds_[];
    const lptr lds = (lptr)lds_;
    const unsigned lds0 = (unsigned)(uintptr_t)lds;
    const int tid = threadIdx.x, lane = tid & 63, l31 = lane & 31, hi = lane >> 5;
    const int wid = __builtin_amdgcn_readfirstlane(tid >> 6);
    const int bid = blockIdx.x, rt = (bid & 7) * 4 + ((bid >> 3) & 3), ct = bid >> 5;
    const int kh = wid >> 2, qg2 = (wid >> 1) & 1, cb = wid & 1;

    unsigned voff[4];
    const char* src; unsigned hstep;
    if (wid < 4) {
        src = ws + WS_CTX + (size_t)(2 * rt + (wid >> 1)) * 32 * 8192 + (wid & 1) * 4096; hstep = 8192;
#pragma unroll
        for (int j = 0; j < 4; ++j) voff[j] = lane * 16 + j * 1024;
    } else {
        src = (const char*)wo + ct * 128; hstep = 128 * 1024;
#pragma unroll
        for (int j = 0; j < 4; ++j) { const int row = ((wid - 4) * 4 + j) * 8 + (lane >> 3);
            voff[j] = row * 1024 + (((lane & 7) ^ (((row >> 1) & 1) << 2)) << 4); }
    }
    auto stage = [&](int hh) {
        const unsigned b = lds0 + (hh & 3) * K3_SLOT + wid * 4096;
#pragma unroll
        for (int j = 0; j < 4; ++j) glds16(src + (size_t)hh * hstep, voff[j], b + j * 1024);
    };
    const int* sel = (const int*)(ws + WS_SEL);
    const int i0 = sel[0], i1 = sel[1];
    const float sg0 = ((const float*)sel)[2], sg1 = ((const float*)sel)[3];
    const bool has0 = (i0 >> 6) == rt, has1 = (i1 >> 6) == rt;
    float yp0 = 0.f, yp1 = 0.f;
    if (has0 || has1) {
        const float* yp = (const float*)(ws + WS_YP) + ct * 64 + lane;
        for (int b = wid * 28; b < wid * 28 + 28; ++b) { yp0 += yp[(size_t)b * 1024]; yp1 += yp[(size_t)b * 1024 + 512]; }
    }
    stage(0); stage(1); stage(2);
    const int tq = (lane & 15) >> 2, tp = lane & 3, tblk = (lane >> 4) & 1;
    const lptr wA = lds + 16384 + (4 * hi + tq) * 128 + ((cb ^ ((tq >> 1) & 1)) << 6) + tblk * 32 + tp * 8;
    const lptr cA = lds + qg2 * 8192 + lane * 16;
    f32x16 acc = {};
    for (int hh = 0; hh < 32; ++hh) {
        if (hh < 30) { WAIT_BAR(8); } else if (hh == 30) { WAIT_BAR(4); } else { WAIT_BAR(0); }
        if (hh + 3 < 32) stage(hh + 3);
        const int bo = (hh & 3) * K3_SLOT;
#pragma unroll
        for (int dd = 0; dd < 2; ++dd)
#pragma unroll
            for (int s = 0; s < 2; ++s) {
                const int f = 2 * (2 * kh + dd) + s;
                const bf16x8 cf = ldsr128(cA + bo + f * 1024);
                const bf16x8 wf = cat(ldstr(wA + bo + f * 2048), ldstr(wA + bo + f * 2048 + 1024));
                acc = MFMA(wf, cf, acc);
            }
    }
    WAIT_BAR(0);
    LAS float* ysum = (LAS float*)((LAS char*)lds_ + 65536);
    ysum[(wid * 2) * 64 + lane] = yp0; ysum[(wid * 2 + 1) * 64 + lane] = yp1;
    if (kh == 1) {
#pragma unroll
        for (int q4 = 0; q4 < 4; ++q4)
            *(LAS f32x4*)((LAS char*)lds_ + (wid & 3) * 4096 + q4 * 1024 + lane * 16) = (f32x4){acc[4 * q4], acc[4 * q4 + 1], acc[4 * q4 + 2], acc[4 * q4 + 3]};
    }
    WAIT_BAR(0);
    if (kh == 0) {
        const int q = rt * 64 + qg2 * 32 + l31;
        const float g = ((const float*)(ws + WS_G))[q];
        const bool r0 = (q == i0), r1 = (q == i1);
#pragma unroll
        for (int q4 = 0; q4 < 4; ++q4) {
            const f32x4 t = *(const LAS f32x4*)((LAS char*)lds_ + wid * 4096 + q4 * 1024 + lane * 16);
            const int ml = cb * 32 + 8 * q4 + 4 * hi, m = ct * 64 + ml;
            const s16x4 xb = *(const s16x4*)(bx + (size_t)q * DM + m);
            float res[4];
#pragma unroll
            for (int e = 0; e < 4; ++e) {
                const float a = acc[4 * q4 + e] + t[e];
                float b = bf2f((u16)xb[e]);
                if (r0 || r1) {
                    float y = 0.f;
#pragma unroll
                    for (int w = 0; w < 8; ++w) y += ysum[(w * 2 + (r1 ? 1 : 0)) * 64 + ml + e];
                    b += y * (r1 ? sg1 : sg0);
                }
                res[e] = g * a + (1.0f - g) * b;
            }
            u32x2 w2 = {cvtpk(res[0], res[1]), cvtpk(res[2], res[3])};
            *(u32x2*)(out + (size_t)q * DM + m) = w2;
        }
    }
}

extern "C" void kernel_launch(void* const* d_in, const int* in_sizes, int n_in, void* d_out, int out_size, void* d_ws, size_t ws_size,
                              hipStream_t stream) {
    const u16* ax = (const u16*)d_in[0]; const u16* ckv = (const u16*)d_in[1]; const u16* wq = (const u16*)d_in[2];
    const u16* wuk = (const u16*)d_in[3]; const u16* wuv = (const u16*)d_in[4]; const u16* wo = (const u16*)d_in[5];
    const u16* bx = ax  ; const u16* wr = (const u16*)d_in[7]; const u16* wg = (const u16*)d_in[8];
    const u16* wu = (const u16*)d_in[9]; const u16* wd = (const u16*)d_in[10]; const u16* gw = (const u16*)d_in[11];
    char* ws = (char*)d_ws;
    (void)hipFuncSetAttribute((const void*)kv_proj_kernel, hipFuncAttributeMaxDynamicSharedMemorySize, K1_LDS);
    (void)hipFuncSetAttribute((const void*)attn_kernel, hipFuncAttributeMaxDynamicSharedMemorySize, K2_LDS);
    (void)hipFuncSetAttribute((const void*)o_proj_kernel, hipFuncAttributeMaxDynamicSharedMemorySize, K3_LDS);
    kv_proj_kernel<<<256, 512, K1_LDS, stream>>>(ckv, wuk, wuv, ax, gw, bx, wr, ws);
    attn_kernel<<<256, 512, K2_LDS, stream>>>(ax, wq, bx, wg, wu, wd, ws);
    o_proj_kernel<<<256, 512, K3_LDS, stream>>>(wo, bx, ws, (u16*)d_out);
}
```

```cpp
#include <hip/hip_runtime.h>
#include <hip/hip_bf16.h>
#include <stdint.h>

typedef unsigned short u16;
typedef short bf16x8 __attribute__((ext_vector_type(8)));
typedef short s16x4 __attribute__((ext_vector_type(4)));
typedef short v4i16_t __attribute__((ext_vector_type(4)));
typedef float f32x16 __attribute__((ext_vector_type(16)));
typedef float f32x4 __attribute__((ext_vector_type(4)));
typedef unsigned u32x4 __attribute__((ext_vector_type(4)));
typedef unsigned u32x2 __attribute__((ext_vector_type(2)));
#define LAS __attribute__((address_space(3)))
#define DI __device__ __forceinline__
#define MFMA(a, b, c) __builtin_amdgcn_mfma_f32_32x32x16_bf16((a), (b), (c), 0, 0, 0)
#define SBAR() __builtin_amdgcn_sched_barrier(0)

constexpr int SEQ = 2048, DM = 512, NH = 32, HD = 128, NHD = NH * HD;
constexpr int FF = 1792;
constexpr int MOE_WGS = 224, MOE_COLS = 8;

constexpr size_t WS_KF = 0;
constexpr size_t WS_VF = WS_KF + (size_t)SEQ * NHD * 2;
constexpr size_t WS_CTX = WS_VF + (size_t)SEQ * NHD * 2;
constexpr size_t WS_R = WS_CTX + (size_t)SEQ * NHD * 2;
constexpr size_t WS_G = WS_R + SEQ * 4;
constexpr size_t WS_SEL = WS_G + SEQ * 4;
constexpr size_t WS_YP = WS_SEL + 256;

constexpr size_t WS_DBG = 128u << 20;
#define STAMP(K, i) do {} while (0)
#define SEG_DECL
#define SEG_PARAMS
#define SEG_ARGS
#define SEG_START() do {} while (0)
#define SEG(i) do {} while (0)
#define SEG_OUT(K) do {} while (0)
DI unsigned cvtpk(float lo, float hi) {
    typedef float f32x2 __attribute__((ext_vector_type(2)));
    typedef __bf16 bf16x2 __attribute__((ext_vector_type(2)));
    f32x2 v = {lo, hi}; bf16x2 b = __builtin_convertvector(v, bf16x2); return __builtin_bit_cast(unsigned, b);
}
DI float bf2f(u16 v) { return __uint_as_float((unsigned)v << 16); }
DI float bfround(float x) { return __uint_as_float(cvtpk(x, x) << 16); }
DI bf16x8 pack8(const f32x16& x, int s, float sc) {
    u32x4 w = {cvtpk(x[8 * s] * sc, x[8 * s + 1] * sc), cvtpk(x[8 * s + 2] * sc, x[8 * s + 3] * sc),
               cvtpk(x[8 * s + 4] * sc, x[8 * s + 5] * sc), cvtpk(x[8 * s + 6] * sc, x[8 * s + 7] * sc)};
    return __builtin_bit_cast(bf16x8, w);
}
DI bf16x8 pack8(const f32x16& x, int s) {
    u32x4 w = {cvtpk(x[8 * s], x[8 * s + 1]), cvtpk(x[8 * s + 2], x[8 * s + 3]),
               cvtpk(x[8 * s + 4], x[8 * s + 5]), cvtpk(x[8 * s + 6], x[8 * s + 7])};
    return __builtin_bit_cast(bf16x8, w);
}
DI void glds16(const void* sbase, unsigned voff, unsigned lds_addr) {
    asm volatile("s_mov_b32 m0, %2\n\ts_nop 0\n\tglobal_load_lds_dwordx4 %0, %1" :: "v"(voff), "s"(sbase), "s"(lds_addr) : "memory");
}
DI void glds16x4(const void* sbase, unsigned voff, unsigned lds_addr) {
    asm volatile("s_mov_b32 m0, %2\n\ts_nop 0\n\tglobal_load_lds_dwordx4 %0, %1\n\tglobal_load_lds_dwordx4 %0, %1 offset:1024\n\t"
                 "global_load_lds_dwordx4 %0, %1 offset:2048\n\tglobal_load_lds_dwordx4 %0, %1 offset:3072"
                 :: "v"(voff), "s"(sbase), "s"(lds_addr) : "memory");
}
#define WAIT_BAR(N) asm volatile("s_waitcnt vmcnt(" #N ") lgkmcnt(0)\n\ts_barrier" ::: "memory")
#define WAIT_CNT(c) do { if ((c) == 8) { WAIT_BAR(8); } else if ((c) == 4) { WAIT_BAR(4); } else { WAIT_BAR(0); } } while (0)
typedef LAS const char* lptr;
DI bf16x8 ldsr128(lptr p) { return *(const LAS bf16x8*)p; }
DI bf16x8 ldsr128a(lptr p) { return *(const LAS bf16x8*)p; }
DI s16x4 ldstr(lptr p) { return __builtin_bit_cast(s16x4, __builtin_amdgcn_ds_read_tr16_b64_v4i16((LAS v4i16_t*)p)); }
DI bf16x8 cat(s16x4 a, s16x4 b) { return __builtin_shufflevector(a, b, 0, 1, 2, 3, 4, 5, 6, 7); }
DI float wave_sum(float v) {
#pragma unroll
    for (int o = 32; o >= 1; o >>= 1) v += __shfl_xor(v, o);
    return v;
}
DI float sigmoidf_(float x) { return 1.0f / (1.0f + __expf(-x)); }

constexpr int K1_STAGE = 49152, K1_LDS = 163840;
typedef __amdgpu_buffer_rsrc_t rsrc_t;
DI rsrc_t make_rsrc(const void* p, unsigned bytes) { return __builtin_amdgcn_make_buffer_rsrc((void*)p, (short)0, (int)bytes, 0x00020000); }
DI void store_wt(rsrc_t r, unsigned off, bf16x8 v) {
    __builtin_amdgcn_raw_buffer_store_b128(__builtin_bit_cast(u32x4, v), r, (int)off, 0, 16);
}
#define WAIT_VM(N) asm volatile("s_waitcnt vmcnt(" #N ")" ::: "memory")

template <bool ISV, class Pre>
DI void kv_body(const char* cbase, const char* kbase, const char* vbase, rsrc_t outr, unsigned outoff, lptr lds, unsigned lds0, int wid, int lane, Pre&& pre) {
    const int l31 = lane & 31, hi = lane >> 5;
    unsigned voffW[2], voffC[2], voffG[2];
#pragma unroll
    for (int j = 0; j < 2; ++j) {
        const int n = 2 * wid + j;
        { const int row = 4 * n + (lane >> 4); voffW[j] = row * 8192 + (((lane & 15) ^ ((row & 3) << 2)) << 4); }
        { const int row = 8 * n + (lane >> 3); voffC[j] = row * 1024 + (((lane & 7) ^ ((row >> 1) & 7)) << 4); }
        voffG[j] = (lane ^ ((wid + 8 * j) & 15)) << 4;
    }
    auto stage = [&](int s) {
        const unsigned b = lds0 + (s % 3) * K1_STAGE + 2 * wid * 1024;
#pragma unroll
        for (int j = 0; j < 2; ++j) glds16(kbase + (size_t)s * 64 * 8192, voffW[j], b + j * 1024);
#pragma unroll
        for (int j = 0; j < 2; ++j) glds16(vbase + (size_t)s * 64 * 8192, voffW[j], b + 16384 + j * 1024);
#pragma unroll
        for (int j = 0; j < 2; ++j) glds16(cbase + s * 128, voffC[j], b + 32768 + j * 1024);
    };
    auto gbuf = [&](int k) { return k == 4 ? 98304 : k == 5 ? 131072 : k == 6 ? 0 : 49152; };
    auto stageG = [&](int k) {
#pragma unroll
        for (int j = 0; j < 4; ++j) glds16(cbase + (size_t)(32 * k + wid + 8 * j) * 1024, voffG[j & 1], lds0 + gbuf(k) + (wid + 8 * j) * 1024);
    };
    const int tq = (lane & 15) >> 2, tp = lane & 3, tblk = (lane >> 4) & 1;
    const lptr wA = lds + (ISV ? 16384 : 0) + (8 * hi + tq) * 256 + (((wid & 3) ^ tq) << 6) + tblk * 32 + tp * 8;
    lptr cA[4];
#pragma unroll
    for (int ks = 0; ks < 4; ++ks) cA[ks] = lds + 32768 + l31 * 128 + (((2 * ks + hi) ^ ((l31 >> 1) & 7)) << 4);
    auto mm = [&](const bf16x8& w, const bf16x8& c, const f32x16& acc) { return ISV ? MFMA(c, w, acc) : MFMA(w, c, acc); };
    auto out_tile = [&](int kg, const f32x16& acc) {
        store_wt(outr, outoff + kg * 8192, pack8(acc, 0));
        store_wt(outr, outoff + kg * 8192 + 1024, pack8(acc, 1));
    };

    bf16x8 wf[32];
    f32x16 acc[4] = {};
    lptr gA[8];
    stage(0); stage(1); stage(2);
    pre();
    auto rdW = [&](int s, int ks) { const int bo = (s % 3) * K1_STAGE; wf[4 * s + ks] = cat(ldstr(wA + bo + ks * 4096), ldstr(wA + bo + ks * 4096 + 1024)); };
    auto rdC = [&](int s, int i) { return ldsr128(cA[i >> 2] + (s % 3) * K1_STAGE + (i & 3) * 4096); };
    auto rdG = [&](int k, int ks) { return ldsr128(gA[ks & 7] + gbuf(k) + (ks >> 3) * 256); };
    auto dmaS = [&](int s, int j) {
        const unsigned b = lds0 + (s % 3) * K1_STAGE + 2 * wid * 1024;
        if (j < 2) glds16(kbase + (size_t)s * 64 * 8192, voffW[j], b + j * 1024);
        else if (j < 4) glds16(vbase + (size_t)s * 64 * 8192, voffW[j - 2], b + 16384 + (j - 2) * 1024);
        else glds16(cbase + s * 128, voffC[j - 4], b + 32768 + (j - 4) * 1024);
    };
    auto dmaG = [&](int k, int j) { glds16(cbase + (size_t)(32 * k + wid + 8 * j) * 1024, voffG[j & 1], lds0 + gbuf(k) + (wid + 8 * j) * 1024); };
    bf16x8 cf[8];
    WAIT_BAR(12);
    rdW(0, 0);
#pragma unroll
    for (int i = 0; i < 4; ++i) cf[i] = rdC(0, i);
#pragma unroll
    for (int s = 0; s < 8; ++s) {
#pragma unroll
        for (int i = 0; i < 16; ++i) {
            if (i == 12) {
                if (s < 6) { WAIT_BAR(6); } else if (s == 6) { WAIT_BAR(8); } else { WAIT_BAR(8); }
            }
            if (s == 7 && i == 11) {
                int ln = lane; asm volatile("" : "+v"(ln));
#pragma unroll
                for (int j = 0; j < 8; ++j) gA[j] = lds + (ln & 31) * 1024 + (((2 * j + (ln >> 5)) ^ (ln & 15)) << 4);
            }
            const int nx = i + 4;
            if (nx < 16) { if ((nx & 3) == 0) rdW(s, nx >> 2); cf[nx & 7] = rdC(s, nx); }
            else if (s < 7) { if (nx == 16) rdW(s + 1, 0); cf[nx & 7] = rdC(s + 1, nx - 16); }
            else cf[nx & 7] = rdG(4, nx - 16);
            acc[i & 3] = mm(wf[4 * s + (i >> 2)], cf[i & 7], acc[i & 3]);
            if (i >= 12) {
                const int j = i - 12;
                if (s + 3 < 8) dmaS(s + 3, j);
                else if (s == 5) dmaG(4, j);
                else if (s == 6) dmaG(6, j);
                else dmaG(7, j);
            } else if (i < 2) {
                if (s >= 1 && s + 2 < 8) dmaS(s + 2, 4 + i);
            } else if (i < 6) {
                if (s == 6) dmaG(5, i - 2);
            }
            SBAR();
        }
    }
    f32x16 a2[2];
#pragma unroll
    for (int k = 4; k < 8; ++k) {
#pragma unroll
        for (int ks = 0; ks < 32; ++ks) {
            if (ks == 28 && k < 7) {
                if (k == 4) { WAIT_BAR(16); } else if (k == 5) { WAIT_BAR(14); } else { WAIT_BAR(12); }
            }
            const int nx = ks + 4;
            if (nx < 32) cf[nx & 7] = rdG(k, nx); else if (k < 7) cf[nx & 7] = rdG(k + 1, nx - 32);
            if (ks == 0) a2[k & 1] = mm(wf[0], cf[0], f32x16{}); else a2[k & 1] = mm(wf[ks], cf[ks & 7], a2[k & 1]);
            if (k == 4) { if (ks >= 2 && ks < 18 && (ks & 3) == 2) out_tile((ks - 2) >> 2, acc[(ks - 2) >> 2]); }
            else if (ks == 3) out_tile(k - 1, a2[(k - 1) & 1]);
            SBAR();
        }
    }
    out_tile(7, a2[1]);
}

__global__ __launch_bounds__(512, 2)
void kv_proj_kernel(const u16* __restrict__ ckv, const u16* __restrict__ wuk, const u16* __restrict__ wuv,
                    const u16* __restrict__ ax, const u16* __restrict__ gatew, const u16* __restrict__ bx, const u16* __restrict__ wr,
                    char* __restrict__ ws) {
    extern __shared__ __attribute__((aligned(16))) char lds_[];
    const lptr lds = (lptr)lds_;
    const unsigned lds0 = (unsigned)(uintptr_t)lds;
    const int tid = threadIdx.x, lane = tid & 63;
    const int wid = __builtin_amdgcn_readfirstlane(tid >> 6);
    const int bid = blockIdx.x, h = (bid & 7) * 4 + ((bid >> 3) & 3), p = bid >> 5;
    STAMP(0, 0);
    const int tok = bid * 8 + wid;
    const bf16x8 bxv = *(const bf16x8*)(bx + tok * DM + lane * 8), wrv = *(const bf16x8*)(wr + lane * 8);
    const bf16x8 gwv = *(const bf16x8*)(gatew + lane * 8);
    auto pre = [&]() {
        float sr = 0.f, sg = 0.f;
#pragma unroll
        for (int j = 0; j < 8; ++j) { sr += bf2f((u16)bxv[j]) * bf2f((u16)wrv[j]); sg += bf2f((u16)bxv[j]) * bf2f((u16)gwv[j]); }
        sr = wave_sum(sr); sg = wave_sum(sg);
        if (lane == 0) { ((float*)(ws + WS_R))[tok] = bfround(sr); ((float*)(ws + WS_G))[tok] = sigmoidf_(sg); }
        STAMP(0, 1);
    };
    const char* cbase = (const char*)ckv + (size_t)p * 256 * 1024;
    const char* kbase = (const char*)wuk + h * 256;
    const char* vbase = (const char*)wuv + h * 256;
    const unsigned outoff = (unsigned)((h * 64 + 8 * p) * 8 + 2 * (wid & 3)) * 1024 + lane * 16;
    const unsigned kvbytes = (unsigned)((size_t)SEQ * NHD * 2);
    if (wid < 4) kv_body<false>(cbase, kbase, vbase, make_rsrc(ws + WS_KF, kvbytes), outoff, lds, lds0, wid, lane, pre);
    else         kv_body<true>(cbase, kbase, vbase, make_rsrc(ws + WS_VF, kvbytes), outoff, lds, lds0, wid, lane, pre);
    STAMP(0, 3);
    asm volatile("s_waitcnt vmcnt(0)" ::: "memory");
    STAMP(0, 4);
}

DI unsigned umax_(unsigned a, unsigned b) { return a > b ? a : b; }
DI unsigned umin_(unsigned a, unsigned b) { return a < b ? a : b; }
DI void moe_part(const u16* __restrict__ bx, const u16* __restrict__ wg, const u16* __restrict__ wu, const u16* __restrict__ wd,
                 char* __restrict__ ws, char* lds_, int bid, int tid) {
    const int lane = tid & 63, wid = tid >> 6;
    unsigned* sk = (unsigned*)lds_;
    float* xs = (float*)(lds_ + 256);
    float* red = (float*)(lds_ + 256 + 4096);
    float* hs = (float*)(lds_ + 256 + 4096 + 1024);
    const int cg = (bid & 7) * 28 + (bid >> 3);
    const bool work = (bid >> 3) < 28;
    const int f0 = cg * MOE_COLS;
    const int col = tid & 15, ksl = tid >> 4;
    u16 wv[16]; u16 dv[8];
    if (work) {
        const u16* wsrc = (col < 8 ? wg : wu) + f0 + (col & 7) + (size_t)ksl * 16 * FF;
#pragma unroll
        for (int j = 0; j < 16; ++j) wv[j] = wsrc[(size_t)j * FF];
#pragma unroll
        for (int j = 0; j < 8; ++j) dv[j] = wd[(size_t)(f0 + j) * DM + tid];
    }
    const f32x4 rv = *(const f32x4*)((const float*)(ws + WS_R) + tid * 4);
    unsigned k1 = 0, k2 = 0;
#pragma unroll
    for (int j = 0; j < 4; ++j) {
        const unsigned u = __float_as_uint(rv[j]) >> 16;
        const unsigned o = (u & 0x8000u) ? (~u & 0xFFFFu) : (u | 0x8000u);
        const unsigned key = (o << 16) | (unsigned)(0xFFFF - (tid * 4 + j));
        k2 = umax_(k2, umin_(k1, key)); k1 = umax_(k1, key);
    }
#pragma unroll
    for (int o = 32; o >= 1; o >>= 1) {
        const unsigned b1 = __shfl_xor(k1, o), b2 = __shfl_xor(k2, o);
        k2 = umax_(umin_(k1, b1), umax_(k2, b2)); k1 = umax_(k1, b1);
    }
    if (lane == 0) { sk[wid * 2] = k1; sk[wid * 2 + 1] = k2; }
    __syncthreads();
    k1 = 0; k2 = 0;
#pragma unroll
    for (int j = 0; j < 16; ++j) { const unsigned key = sk[j]; k2 = umax_(k2, umin_(k1, key)); k1 = umax_(k1, key); }
    const int i0 = 0xFFFF - (int)(k1 & 0xFFFFu), i1 = 0xFFFF - (int)(k2 & 0xFFFFu);
    if (bid == 0 && tid == 0) {
        const float* r = (const float*)(ws + WS_R);
        int* sel = (int*)(ws + WS_SEL);
        sel[0] = i0; sel[1] = i1;
        ((float*)sel)[2] = sigmoidf_(r[i0]); ((float*)sel)[3] = sigmoidf_(r[i1]);
    }
    if (work) {
        xs[tid] = bf2f(bx[i0 * DM + tid]); xs[512 + tid] = bf2f(bx[i1 * DM + tid]);
    }
    __syncthreads();
    if (work) {
        float a0 = 0.f, a1 = 0.f;
#pragma unroll
        for (int j = 0; j < 16; ++j) { const float w = bf2f(wv[j]); a0 += xs[ksl * 16 + j] * w; a1 += xs[512 + ksl * 16 + j] * w; }
        a0 += __shfl_xor(a0, 16); a0 += __shfl_xor(a0, 32); a1 += __shfl_xor(a1, 16); a1 += __shfl_xor(a1, 32);
        if (lane < 16) { red[(wid * 16 + lane) * 2] = a0; red[(wid * 16 + lane) * 2 + 1] = a1; }
    }
    __syncthreads();
    if (work && tid < 16) {
        const int c = tid & 7, t = tid >> 3;
        float g = 0.f, u = 0.f;
#pragma unroll
        for (int w = 0; w < 8; ++w) { g += red[(w * 16 + c) * 2 + t]; u += red[(w * 16 + 8 + c) * 2 + t]; }
        g = bfround(g); u = bfround(u);
        const float sg = bfround(g * sigmoidf_(g));
        hs[t * 8 + c] = bfround(sg * u);
    }
    __syncthreads();
    if (work) {
        float y0 = 0.f, y1 = 0.f;
#pragma unroll
        for (int j = 0; j < 8; ++j) { const float w = bf2f(dv[j]); y0 += hs[j] * w; y1 += hs[8 + j] * w; }
        float* yp = (float*)(ws + WS_YP) + (size_t)cg * 1024;
        yp[tid] = y0; yp[512 + tid] = y1;
    }
    __syncthreads();
}

constexpr int K2_QST = 49152, K2_SLOT = 32768, K2_QB = 131072, K2_LDS = 163840;
constexpr float QSCALE = 0.08838834764831845f * 1.4426950408889634f;
constexpr float THR = 8.0f;

#define SETPRIO(x) __builtin_amdgcn_s_setprio(x)
struct SMState { float mhat, l; f32x16 negm; };
#define PIN(x) asm volatile("" : "+v"(x))
DI float max3(float a, float b, float c) { return fmaxf(fmaxf(a, b), c); }

#define EXP2(x) __builtin_amdgcn_exp2f(x)
#define VOFF(i) (((i) >> 3) * 8192 + (2 * ((i) & 3) + (((i) >> 2) & 1)) * 1024)
#define KOFF(i) (((i) & 1) * 8192 + ((i) >> 1) * 1024)
DI float fadd_s(float a, float b) { float r; asm("v_add_f32_e32 %0, %1, %2" : "=v"(r) : "v"(a), "v"(b)); return r; }
DI float max3_s(float a, float b, float c) { float r; asm("v_max3_f32 %0, %1, %2, %3" : "=v"(r) : "v"(a), "v"(b), "v"(c)); return r; }
DI float max2_s(float a, float b) { float r; asm("v_max_f32_e32 %0, %1, %2" : "=v"(r) : "v"(a), "v"(b)); return r; }
template <int OFF> DI void glds_step(const void* sbase, unsigned voff, unsigned lds_addr) {
    if (OFF == 0) asm volatile("s_mov_b32 m0, %2\n\ts_nop 0\n\tglobal_load_lds_dwordx4 %0, %1" :: "v"(voff), "s"(sbase), "s"(lds_addr));
    else asm volatile("global_load_lds_dwordx4 %0, %1 offset:%c2" :: "v"(voff), "s"(sbase), "i"(OFF));
}
DI f32x16 mfma_c(bf16x8 a, bf16x8 b, const f32x16& c) {
    f32x16 d; asm volatile("v_mfma_f32_32x32x16_bf16 %0, %1, %2, %3" : "=&v"(d) : "v"(a), "v"(b), "v"(c)); return d;
}
struct DmaJob { const char* src0; const char* src1; unsigned dst0, dst1; bool has0, has1; };

DI void attn_qk(lptr kp, lptr vpn, const bf16x8 (&qf)[8], const f32x16& negm, const bf16x8 (&kpre)[4], bf16x8 (&vpre)[4], f32x16& s0, f32x16& s1, const DmaJob& dj, unsigned vlin) {
    bf16x8 fr[16];
#pragma unroll
    for (int i = 0; i < 4; ++i) fr[i] = kpre[i];
#pragma unroll
    for (int i = 0; i < 16; ++i) {
        if (i + 4 < 16) fr[i + 4] = ldsr128a(kp + KOFF(i + 4));
        else vpre[i + 4 - 16] = ldsr128a(vpn + VOFF(i + 4 - 16));
        if (i == 0) s0 = mfma_c(fr[0], qf[0], negm);
        else if (i == 1) s1 = mfma_c(fr[1], qf[0], negm);
        else if (i & 1) s1 = MFMA(fr[i], qf[i >> 1], s1);
        else s0 = MFMA(fr[i], qf[i >> 1], s0);
        if (i == 1 && dj.has0) { glds_step<0>(dj.src0, vlin, dj.dst0); glds_step<1024>(dj.src0, vlin, dj.dst0); }
        if (i == 5 && dj.has0) { glds_step<2048>(dj.src0, vlin, dj.dst0); glds_step<3072>(dj.src0, vlin, dj.dst0); }
        if (i == 9 && dj.has1) { glds_step<0>(dj.src1, vlin, dj.dst1); glds_step<1024>(dj.src1, vlin, dj.dst1); }
        if (i == 13 && dj.has1) { glds_step<2048>(dj.src1, vlin, dj.dst1); glds_step<3072>(dj.src1, vlin, dj.dst1); }
        SBAR();
    }
}
DI void dma_plain(const DmaJob& dj, unsigned vlin) {
    if (dj.has0) glds16x4(dj.src0, vlin, dj.dst0);
    if (dj.has1) glds16x4(dj.src1, vlin, dj.dst1);
}
DI void attn_max(f32x16& s0, f32x16& s1, SMState& st, bool first, bool mask, int qrel  , bool& pend, float& fpend) {
    if (mask) {
        const float NEG = -__builtin_inff();
#pragma unroll
        for (int r = 0; r < 16; ++r) { const int c = (r & 3) + 8 * (r >> 2);
            if (c > qrel) s0[r] = NEG;
            if (c + 32 > qrel) s1[r] = NEG; }
    }
    float a = max3_s(s0[0], s0[1], s0[2]), b = max3_s(s0[3], s0[4], s0[5]), c = max3_s(s1[0], s1[1], s1[2]), d = max3_s(s1[3], s1[4], s1[5]);
    a = max3_s(a, s0[6], s0[7]); b = max3_s(b, s0[8], s0[9]); c = max3_s(c, s1[6], s1[7]); d = max3_s(d, s1[8], s1[9]);
    a = max3_s(a, s0[10], s0[11]); b = max3_s(b, s0[12], s0[13]); c = max3_s(c, s1[10], s1[11]); d = max3_s(d, s1[12], s1[13]);
    a = max3_s(a, s0[14], s0[15]); c = max3_s(c, s1[14], s1[15]);
    float rm = max2_s(max3_s(a, b, c), d);
    { auto rr = __builtin_amdgcn_permlane32_swap(__float_as_uint(rm), __float_as_uint(rm), false, false);
      rm = max2_s(__uint_as_float(rr[0]), __uint_as_float(rr[1])); }
    if (__builtin_expect(first || __any(rm > THR), 0)) {
        const float dl = first ? rm : fmaxf(rm, 0.f), f = first ? 1.0f : __builtin_amdgcn_exp2f(-dl);
        st.mhat += dl; st.l *= f;
#pragma unroll
        for (int r = 0; r < 16; ++r) { s0[r] -= dl; s1[r] -= dl; st.negm[r] = -st.mhat; }
        pend = !first; fpend = f;
    }
}
template <bool PV, bool EXPS>
DI void attn_pv(lptr vp, lptr kpn, f32x16 (&o)[4], const bf16x8 (&pin)[4], bf16x8 (&pout)[4], const bf16x8 (&vpre)[4], bf16x8 (&kpre)[4],
                const f32x16& s0, const f32x16& s1, float& l) {
    bf16x8 fr[16];
    unsigned pw[16];
    float sa = 0.f, sb = 0.f;
#pragma unroll
    for (int i = 0; i < 4; ++i) fr[i] = vpre[i];
    SETPRIO(1);
#pragma unroll
    for (int i = 0; i < 16; ++i) {
        if (PV && i + 4 < 16) fr[i + 4] = ldsr128a(vp + VOFF(i + 4));
        if (i + 4 >= 16) kpre[i + 4 - 16] = ldsr128a(kpn + KOFF(i + 4 - 16));
        if (PV) o[i & 3] = MFMA(fr[i], pin[i >> 2], o[i & 3]);
        if (EXPS) {
            const float x0 = i < 8 ? s0[2 * i] : s1[2 * i - 16], x1 = i < 8 ? s0[2 * i + 1] : s1[2 * i - 15];
            const float e0 = EXP2(x0), e1 = EXP2(x1);
            if (i == 0) { sa = e0; sb = e1; } else { sa = fadd_s(sa, e0); sb = fadd_s(sb, e1); }
            pw[i] = cvtpk(e0, e1);
            PIN(pw[i]); PIN(sa); PIN(sb);
        }
        SBAR();
    }
    SETPRIO(0);
    if (EXPS) {
        l += sa + sb;
#pragma unroll
        for (int i = 0; i < 4; ++i) { u32x4 w = {pw[4 * i], pw[4 * i + 1], pw[4 * i + 2], pw[4 * i + 3]}; pout[i] = __builtin_bit_cast(bf16x8, w); }
    }
}

__global__ __launch_bounds__(512, 2)
void attn_kernel(const u16* __restrict__ x, const u16* __restrict__ wq,
                 const u16* __restrict__ bx, const u16* __restrict__ wg, const u16* __restrict__ wu, const u16* __restrict__ wd,
                 char* __restrict__ ws) {
    extern __shared__ __attribute__((aligned(16))) char lds_[];
    const lptr lds = (lptr)lds_;
    const unsigned lds0 = (unsigned)(uintptr_t)lds;
    const int tid = threadIdx.x, lane = tid & 63, l31 = lane & 31, hi = lane >> 5;
    const int wid = __builtin_amdgcn_readfirstlane(tid >> 6);
    const int bid = blockIdx.x, h = (bid & 7) * 4 + ((bid >> 3) & 3), bi = bid >> 5;
    const int grp = wid >> 2, rg = wid & 3;
    const int blkA = bi, blkB = 15 - bi;

    STAMP(1, 0);
    moe_part(bx, wg, wu, wd, ws, lds_, bid, tid);
    STAMP(1, 1);

    bf16x8 qf[8];
    {
        unsigned voffX[4], voffW[2];
        const int myblk = grp ? blkB : blkA;
#pragma unroll
        for (int j = 0; j < 4; ++j) { const int r = 8 * j + (lane >> 3);
            voffX[j] = r * 1024 + (((lane & 7) ^ (((8 * j + (lane >> 3)) >> 1) & 7)) << 4); }
#pragma unroll
        for (int j = 0; j < 2; ++j) { const int n = 2 * wid + j, row = 4 * n + (lane >> 4);
            voffW[j] = row * 8192 + (((lane & 15) ^ ((row & 3) << 2)) << 4); }
        const char* xbase = (const char*)x + (size_t)(myblk * 128 + rg * 32) * 1024;
        const char* wbase = (const char*)wq + h * 256;
        auto stage = [&](int s, int slot) {
            const unsigned b = lds0 + slot * K2_QST;
#pragma unroll
            for (int j = 0; j < 4; ++j) glds16(xbase + s * 128, voffX[j], b + (4 * wid + j) * 1024);
#pragma unroll
            for (int j = 0; j < 2; ++j) glds16(wbase + (size_t)s * 64 * 8192, voffW[j], b + 32768 + (2 * wid + j) * 1024);
        };
        lptr xA[4];
#pragma unroll
        for (int ks = 0; ks < 4; ++ks) xA[ks] = lds + (32 * wid + l31) * 128 + (((2 * ks + hi) ^ ((l31 >> 1) & 7)) << 4);
        const int tq = (lane & 15) >> 2, tp = lane & 3, tblk = (lane >> 4) & 1;
        lptr wA[4];
#pragma unroll
        for (int c = 0; c < 4; ++c) wA[c] = lds + 32768 + (8 * hi + tq) * 256 + ((c ^ tq) << 6) + tblk * 32 + tp * 8;
        f32x16 acc[4] = {};
        stage(0, 0); stage(1, 1);
#pragma unroll
        for (int s = 0; s < 8; ++s) {
            if (s < 7) { WAIT_BAR(6); } else { WAIT_BAR(0); }
            if (s + 2 < 8) stage(s + 2, (s + 2) % 3);
            const int bo = (s % 3) * K2_QST;
#pragma unroll
            for (int ks = 0; ks < 4; ++ks) {
                const bf16x8 xf = ldsr128(xA[ks] + bo);
#pragma unroll
                for (int c = 0; c < 4; ++c) {
                    const bf16x8 wf = cat(ldstr(wA[c] + bo + ks * 4096), ldstr(wA[c] + bo + ks * 4096 + 1024));
                    acc[c] = MFMA(wf, xf, acc[c]);
                }
            }
        }
#pragma unroll
        for (int c = 0; c < 4; ++c) { qf[2 * c] = pack8(acc[c], 0, QSCALE); qf[2 * c + 1] = pack8(acc[c], 1, QSCALE); }
    }

    STAMP(1, 2);
    const char* Kh = ws + WS_KF + (size_t)h * 64 * 8192;
    const char* Vh = ws + WS_VF + (size_t)h * 64 * 8192;
    const unsigned vlin = lane * 16;
    const int n1 = 2 * bi + 2;
    auto tile_of = [&](int n, int g) { return n < n1 ? n : n1 + 2 * (n - n1) + g; };
    auto jstar = [&](int tn) { return tn <= 2 * n1 ? tn : tn <= 2 * n1 + 2 ? tn + 1 : 2 * tn - 2 * n1 - 2; };
    auto jg1 = [&](int tn) { return tn == 2 * n1 + 1 ? tn : 2 * tn - 2 * n1 - 1; };
    int tb_d0, tb_d1, tb_kv, tb_rel;
    auto make_job = [&](int tn, int& d0, int& d1) {
        const int n = tn >> 1;
        int a_off, b_off; bool a_ok, b_ok;
        const int kb = (int)WS_KF + h * 64 * 8192 + rg * 4096, vb = (int)WS_VF + h * 64 * 8192 + rg * 4096;
        if (tn & 1) { a_ok = n >= 1 && n <= 17; a_off = vb + tile_of(n - 1, 0) * 16384; b_ok = n >= n1 && n <= 16; b_off = kb + tile_of(n, 1) * 16384; }
        else        { a_ok = n <= 16;           a_off = kb + tile_of(n, 0) * 16384;     b_ok = n - 2 >= n1 && n <= 18; b_off = vb + tile_of(n - 2, 1) * 16384; }
        const int da = a_off | (jstar(tn) & 7) | 8, db = b_off | (jg1(tn) & 7) | 8;
        d0 = a_ok ? da : b_ok ? db : 0; d1 = (a_ok && b_ok) ? db : 0;
    };
    {
        const int n = lane < 19 ? lane : 18;
        make_job(2 * n + grp + 3, tb_d0, tb_d1);
        const int ks = (n < n1 || grp == 0 ? jstar(2 * n) : jg1(2 * n + 1)) & 7;
        const int vs = (n < n1 || grp == 0 ? jstar(2 * n + 3) : jg1(2 * n + 4)) & 7;
        tb_kv = ks | vs << 8;
        tb_rel = 128 * ((grp || n >= n1) ? blkB : blkA) + 32 * rg - 64 * tile_of(n, grp);
    }
    auto job_from = [&](int d0, int d1) -> DmaJob {
        DmaJob d;
        d.has0 = d0 & 8; d.has1 = d1 & 8;
        d.src0 = ws + (unsigned)(d0 & ~4095); d.src1 = ws + (unsigned)(d1 & ~4095);
        d.dst0 = lds0 + (d0 & 7) * 16384 + rg * 4096; d.dst1 = lds0 + (d1 & 7) * 16384 + rg * 4096;
        return d;
    };
    auto job = [&](int n) { return job_from(__builtin_amdgcn_readlane(tb_d0, n), __builtin_amdgcn_readlane(tb_d1, n)); };
    WAIT_BAR(0);
    if (grp == 0) {
        int d0, d1;
        make_job(0, d0, d1); dma_plain(job_from(__builtin_amdgcn_readfirstlane(d0), 0), vlin);
        make_job(2, d0, d1); dma_plain(job_from(__builtin_amdgcn_readfirstlane(d0), 0), vlin);
    } else {
#pragma unroll
        for (int c = 0; c < 8; ++c) *(LAS bf16x8*)((LAS char*)lds_ + K2_QB + rg * 8192 + c * 1024 + lane * 16) = qf[c];
    }
    f32x16 o[4] = {};
    SMState st; st.mhat = 0.f; st.l = 0.f; st.negm = f32x16{};
    const lptr kp0 = lds + lane * 16;
    char* ctx = ws + WS_CTX;
    auto store_ctx = [&](int blk, int d, const f32x16& v, float inv) {
        const size_t off = ((size_t)((4 * blk + rg) * 32 + h) * 8 + 2 * d) * 1024 + lane * 16;
        *(bf16x8*)(ctx + off) = pack8(v, 0, inv);
        *(bf16x8*)(ctx + off + 1024) = pack8(v, 1, inv);
    };
    auto half_sum = [&](float v) { auto rr = __builtin_amdgcn_permlane32_swap(__float_as_uint(v), __float_as_uint(v), false, false);
                                   return __uint_as_float(rr[0]) + __uint_as_float(rr[1]); };
    auto kslot = [&](int n) { return kp0 + (__builtin_amdgcn_readlane(tb_kv, n) & 7) * 16384; };
    auto vslot = [&](int n) { return kp0 + ((__builtin_amdgcn_readlane(tb_kv, n) >> 8) & 7) * 16384; };
    WAIT_BAR(0);
    bool first = true, pend = false;
    float invA = 0.f, fpend = 1.f;
    f32x16 s0, s1; bf16x8 pfA[4], pfB[4], vpre[4], kpre[4];
#pragma unroll
    for (int i = 0; i < 4; ++i) kpre[i] = ldsr128a(kslot(0) + KOFF(i));
    if (grp == 1) { WAIT_BAR(0); }
    SEG_DECL; SEG_START();
    auto phaseA = [&](int n) {
        const int rel = __builtin_amdgcn_readlane(tb_rel, n);
        attn_qk(kslot(n), vslot(n > 0 ? n - 1 : 0), qf, st.negm, kpre, vpre, s0, s1, job(n), vlin);
        SEG(0);
        attn_max(s0, s1, st, first, rel < 63, rel + l31 - 4 * hi, pend, fpend);
        first = false;
    };
    auto tailB = [&](int n) {
        if (__builtin_expect(pend, 0)) {
#pragma unroll
            for (int d = 0; d < 4; ++d)
#pragma unroll
                for (int r = 0; r < 16; ++r) o[d][r] *= fpend;
            pend = false;
        }
        if (n == n1 && grp == 0) {
#pragma unroll
            for (int d = 0; d < 4; ++d) store_ctx(blkA, d, o[d], invA);
#pragma unroll
            for (int d = 0; d < 4; ++d) o[d] = f32x16{};
        }
        if (n == n1 - 1 && grp == 0) {
            invA = 1.0f / half_sum(st.l);
#pragma unroll
            for (int c = 0; c < 8; ++c) qf[c] = ldsr128(lds + K2_QB + rg * 8192 + c * 1024 + lane * 16);
            st.mhat = 0.f; st.l = 0.f; st.negm = f32x16{}; first = true;
        }
    };
    phaseA(0);
    asm volatile("s_waitcnt lgkmcnt(0)\n\ts_barrier" ::: "memory");
    attn_pv<false, true>(kp0, kslot(1), o, pfB, pfA, vpre, kpre, s0, s1, st.l);
    WAIT_BAR(0);
#define TAILB(n) tailB(n)
#define STEP(n, PIN_, POUT_) do { \
        phaseA(n); \
        SEG(1); \
        asm volatile("s_waitcnt lgkmcnt(0)\n\ts_barrier" ::: "memory"); \
        SEG(2); \
        attn_pv<true, true>(vslot((n) - 1), kslot((n) + 1), o, PIN_, POUT_, vpre, kpre, s0, s1, st.l); \
        TAILB(n); \
        SEG(3); \
        WAIT_BAR(0);                                    \
        SEG(4); \
    } while (0)
    for (int n = 1; n < 17; n += 2) {
        STEP(n, pfA, pfB);
        STEP(n + 1, pfB, pfA);
    }
#undef STEP
#pragma unroll
    for (int i = 0; i < 4; ++i) vpre[i] = ldsr128a(vslot(16) + VOFF(i));
    asm volatile("s_waitcnt lgkmcnt(0)\n\ts_barrier" ::: "memory");
    attn_pv<true, false>(vslot(16), kp0, o, pfA, pfB, vpre, kpre, s0, s1, st.l);
    WAIT_BAR(0);
    SEG_OUT(1);
    if (grp == 0) { WAIT_BAR(0); }
    STAMP(1, 3);
    WAIT_BAR(0);
    {
        const float lsum = half_sum(st.l);
        LAS char* xch = (LAS char*)lds_;
        LAS char* mine = xch + wid * 8192;
        auto put = [&](int dd, const f32x16& v) {
#pragma unroll
            for (int q4 = 0; q4 < 4; ++q4)
                *(LAS f32x4*)(mine + (dd * 4 + q4) * 1024 + lane * 16) = (f32x4){v[4 * q4], v[4 * q4 + 1], v[4 * q4 + 2], v[4 * q4 + 3]};
        };
        if (grp) { put(0, o[0]); put(1, o[1]); } else { put(0, o[2]); put(1, o[3]); }
        LAS float* ml = (LAS float*)(xch + 65536);
        ml[wid * 128 + lane] = st.mhat; ml[wid * 128 + 64 + lane] = lsum;
        WAIT_BAR(0);
        const int ow = wid ^ 4;
        const float m2 = ml[ow * 128 + lane], l2 = ml[ow * 128 + 64 + lane];
        const float mm = fmaxf(st.mhat, m2);
        const float f1 = __builtin_amdgcn_exp2f(st.mhat - mm), f2 = __builtin_amdgcn_exp2f(m2 - mm);
        const float inv = 1.0f / (lsum * f1 + l2 * f2);
        const float c1 = f1 * inv, c2 = f2 * inv;
        const LAS char* theirs = xch + ow * 8192;
        auto fin = [&](int dd, f32x16 v) {
#pragma unroll
            for (int q4 = 0; q4 < 4; ++q4) {
                const f32x4 t = *(const LAS f32x4*)(theirs + (dd * 4 + q4) * 1024 + lane * 16);
#pragma unroll
                for (int e = 0; e < 4; ++e) v[4 * q4 + e] = v[4 * q4 + e] * c1 + t[e] * c2;
            }
            store_ctx(blkB, 2 * grp + dd, v, 1.0f);
        };
        if (grp) { fin(0, o[2]); fin(1, o[3]); } else { fin(0, o[0]); fin(1, o[1]); }
    }
    STAMP(1, 4);
    asm volatile("s_waitcnt vmcnt(0)" ::: "memory");
    STAMP(1, 5);
}

constexpr int K3_SLOT = 32768, K3_NS = 5, K3_LDS = K3_NS * K3_SLOT;
__global__ __launch_bounds__(512, 2)
void o_proj_kernel(const u16* __restrict__ wo, const u16* __restrict__ bx, char* __restrict__ ws, u16* __restrict__ out) {
    extern __shared__ __attribute__((aligned(16))) char lds_[];
    const lptr lds = (lptr)lds_;
    const unsigned lds0 = (unsigned)(uintptr_t)lds;
    const int tid = threadIdx.x, lane = tid & 63, l31 = lane & 31, hi = lane >> 5;
    const int wid = __builtin_amdgcn_readfirstlane(tid >> 6);
    const int bid = blockIdx.x, rt = (bid & 7) * 4 + ((bid >> 3) & 3), ct = bid >> 5;
    STAMP(2, 0);
    if (wid >= 4) {
        const int lw = wid - 4;
        const char* csrc = ws + WS_CTX + (size_t)(2 * rt + (lw >> 1)) * 32 * 8192 + (lw & 1) * 4096;
        const char* wsrc = (const char*)wo + ct * 128;
        unsigned voffW[4];
#pragma unroll
        for (int j = 0; j < 4; ++j) { const int row = (lw * 4 + j) * 8 + (lane >> 3);
            voffW[j] = row * 1024 + (((lane & 7) ^ (((row >> 1) & 1) << 2)) << 4); }
        const unsigned vlin = lane * 16;
        auto stage = [&](int hh, int slot) {
            const unsigned b = lds0 + slot * K3_SLOT + lw * 4096;
            glds16x4(csrc + (size_t)hh * 8192, vlin, b);
#pragma unroll
            for (int j = 0; j < 4; ++j) glds16(wsrc + (size_t)hh * 131072, voffW[j], b + 16384 + j * 1024);
        };
        stage(0, 0); stage(1, 1); stage(2, 2); stage(3, 3);
        int slot = 4;
        for (int hh = 0; hh < 32; ++hh) {
            if (hh < 29) { WAIT_BAR(24); } else if (hh == 29) { WAIT_BAR(16); } else if (hh == 30) { WAIT_BAR(8); } else { WAIT_BAR(0); }
            if (hh + 4 < 32) stage(hh + 4, slot);
            slot = slot == K3_NS - 1 ? 0 : slot + 1;
        }
        WAIT_BAR(0);
        WAIT_BAR(0);
        return;
    }
    const int qg2 = wid >> 1, cb = wid & 1;
    const int* sel = (const int*)(ws + WS_SEL);
    const int i0 = sel[0], i1 = sel[1];
    const float sg0 = ((const float*)sel)[2], sg1 = ((const float*)sel)[3];
    const bool has0 = (i0 >> 6) == rt, has1 = (i1 >> 6) == rt;
    const int q = rt * 64 + qg2 * 32 + l31;
    const float g = ((const float*)(ws + WS_G))[q];
    s16x4 xb[4];
#pragma unroll
    for (int q4 = 0; q4 < 4; ++q4) xb[q4] = *(const s16x4*)(bx + (size_t)q * DM + ct * 64 + cb * 32 + 8 * q4 + 4 * hi);
    float yp0 = 0.f, yp1 = 0.f;
    if (has0 || has1) {
        const float* yp = (const float*)(ws + WS_YP) + ct * 64 + lane;
#pragma unroll 8
        for (int b = wid * 56; b < wid * 56 + 56; ++b) { yp0 += yp[(size_t)b * 1024]; yp1 += yp[(size_t)b * 1024 + 512]; }
    }
    const int tq = (lane & 15) >> 2, tp = lane & 3, tblk = (lane >> 4) & 1;
    const lptr wA = lds + 16384 + (4 * hi + tq) * 128 + ((cb ^ ((tq >> 1) & 1)) << 6) + tblk * 32 + tp * 8;
    const lptr cA = lds + qg2 * 8192 + lane * 16;
    f32x16 acc = {};
    int bo = 0;
    for (int hh = 0; hh < 32; ++hh) {
        WAIT_BAR(0);
        if (hh == 0) STAMP(2, 2);
#pragma unroll
        for (int f = 0; f < 8; ++f) {
            const bf16x8 cf = ldsr128(cA + bo + f * 1024);
            const bf16x8 wf = cat(ldstr(wA + bo + f * 2048), ldstr(wA + bo + f * 2048 + 1024));
            acc = MFMA(wf, cf, acc);
        }
        bo = bo == (K3_NS - 1) * K3_SLOT ? 0 : bo + K3_SLOT;
    }
    WAIT_BAR(0);
    STAMP(2, 3);
    LAS float* ysum = (LAS float*)lds_;
    ysum[(wid * 2) * 64 + lane] = yp0; ysum[(wid * 2 + 1) * 64 + lane] = yp1;
    WAIT_BAR(0);
    {
        const bool r0 = (q == i0), r1 = (q == i1);
#pragma unroll
        for (int q4 = 0; q4 < 4; ++q4) {
            const int ml = cb * 32 + 8 * q4 + 4 * hi, m = ct * 64 + ml;
            float res[4];
#pragma unroll
            for (int e = 0; e < 4; ++e) {
                float b = bf2f((u16)xb[q4][e]);
                if (r0 || r1) {
                    float y = 0.f;
#pragma unroll
                    for (int w = 0; w < 4; ++w) y += ysum[(w * 2 + (r1 ? 1 : 0)) * 64 + ml + e];
                    b += y * (r1 ? sg1 : sg0);
                }
                res[e] = g * acc[4 * q4 + e] + (1.0f - g) * b;
            }
            u32x2 w2 = {cvtpk(res[0], res[1]), cvtpk(res[2], res[3])};
            *(u32x2*)(out + (size_t)q * DM + m) = w2;
        }
    }
    STAMP(2, 4);
    asm volatile("s_waitcnt vmcnt(0)" ::: "memory");
    STAMP(2, 5);
}

extern "C" void kernel_launch(void* const* d_in, const int* in_sizes, int n_in, void* d_out, int out_size, void* d_ws, size_t ws_size,
                              hipStream_t stream) {
    const u16* ax = (const u16*)d_in[0]; const u16* ckv = (const u16*)d_in[1]; const u16* wq = (const u16*)d_in[2];
    const u16* wuk = (const u16*)d_in[3]; const u16* wuv = (const u16*)d_in[4]; const u16* wo = (const u16*)d_in[5];
    const u16* bx = ax  ; const u16* wr = (const u16*)d_in[7]; const u16* wg = (const u16*)d_in[8];
    const u16* wu = (const u16*)d_in[9]; const u16* wd = (const u16*)d_in[10]; const u16* gw = (const u16*)d_in[11];
    char* ws = (char*)d_ws;
    (void)hipFuncSetAttribute((const void*)kv_proj_kernel, hipFuncAttributeMaxDynamicSharedMemorySize, K1_LDS);
    (void)hipFuncSetAttribute((const void*)attn_kernel, hipFuncAttributeMaxDynamicSharedMemorySize, K2_LDS);
    (void)hipFuncSetAttribute((const void*)o_proj_kernel, hipFuncAttributeMaxDynamicSharedMemorySize, K3_LDS);
    kv_proj_kernel<<<256, 512, K1_LDS, stream>>>(ckv, wuk, wuv, ax, gw, bx, wr, ws);
    attn_kernel<<<256, 512, K2_LDS, stream>>>(ax, wq, bx, wg, wu, wd, ws);
    o_proj_kernel<<<256, 512, K3_LDS, stream>>>(wo, bx, ws, (u16*)d_out);
}
```

```cpp
#include <hip/hip_runtime.h>
#include <hip/hip_bf16.h>
#include <stdint.h>
#include <utility>

typedef unsigned short u16;
typedef short bf16x8 __attribute__((ext_vector_type(8)));
typedef short s16x4 __attribute__((ext_vector_type(4)));
typedef short v4i16_t __attribute__((ext_vector_type(4)));
typedef float f32x16 __attribute__((ext_vector_type(16)));
typedef float f32x4 __attribute__((ext_vector_type(4)));
typedef unsigned u32x4 __attribute__((ext_vector_type(4)));
typedef unsigned u32x2 __attribute__((ext_vector_type(2)));
#define LAS __attribute__((address_space(3)))
#define DI __device__ __forceinline__
#define MFMA(a, b, c) __builtin_amdgcn_mfma_f32_32x32x16_bf16((a), (b), (c), 0, 0, 0)
#define SBAR() __builtin_amdgcn_sched_barrier(0)

constexpr int SEQ = 2048, DM = 512, NH = 32, HD = 128, NHD = NH * HD;
constexpr int FF = 1792;
constexpr int MOE_WGS = 224, MOE_COLS = 8;

constexpr size_t WS_KF = 0;
constexpr size_t WS_VF = WS_KF + (size_t)SEQ * NHD * 2;
constexpr size_t WS_CTX = WS_VF + (size_t)SEQ * NHD * 2;
constexpr size_t WS_R = WS_CTX + (size_t)SEQ * NHD * 2;
constexpr size_t WS_G = WS_R + SEQ * 4;
constexpr size_t WS_SEL = WS_G + SEQ * 4;
constexpr size_t WS_YP = WS_SEL + 256;

constexpr size_t WS_DBG = 128u << 20;
#define STAMP(K, i) do {} while (0)
#define SEG_DECL
#define SEG_PARAMS
#define SEG_ARGS
#define SEG_START() do {} while (0)
#define SEG(i) do {} while (0)
#define SEG_OUT(K) do {} while (0)
template <int... Is, class F> DI void static_for_(std::integer_sequence<int, Is...>, F&& f) { (f(std::integral_constant<int, Is>{}), ...); }
template <int N, class F> DI void static_for(F&& f) { static_for_(std::make_integer_sequence<int, N>{}, f); }
DI unsigned cvtpk(float lo, float hi) {
    typedef float f32x2 __attribute__((ext_vector_type(2)));
    typedef __bf16 bf16x2 __attribute__((ext_vector_type(2)));
    f32x2 v = {lo, hi}; bf16x2 b = __builtin_convertvector(v, bf16x2); return __builtin_bit_cast(unsigned, b);
}
DI float bf2f(u16 v) { return __uint_as_float((unsigned)v << 16); }
DI float bfround(float x) { return __uint_as_float(cvtpk(x, x) << 16); }
DI bf16x8 pack8(const f32x16& x, int s, float sc) {
    u32x4 w = {cvtpk(x[8 * s] * sc, x[8 * s + 1] * sc), cvtpk(x[8 * s + 2] * sc, x[8 * s + 3] * sc),
               cvtpk(x[8 * s + 4] * sc, x[8 * s + 5] * sc), cvtpk(x[8 * s + 6] * sc, x[8 * s + 7] * sc)};
    return __builtin_bit_cast(bf16x8, w);
}
DI bf16x8 pack8(const f32x16& x, int s) {
    u32x4 w = {cvtpk(x[8 * s], x[8 * s + 1]), cvtpk(x[8 * s + 2], x[8 * s + 3]),
               cvtpk(x[8 * s + 4], x[8 * s + 5]), cvtpk(x[8 * s + 6], x[8 * s + 7])};
    return __builtin_bit_cast(bf16x8, w);
}
DI void glds16(const void* sbase, unsigned voff, unsigned lds_addr) {
    asm volatile("s_mov_b32 m0, %2\n\ts_nop 0\n\tglobal_load_lds_dwordx4 %0, %1" :: "v"(voff), "s"(sbase), "s"(lds_addr) : "memory");
}
DI void glds16x4(const void* sbase, unsigned voff, unsigned lds_addr) {
    asm volatile("s_mov_b32 m0, %2\n\ts_nop 0\n\tglobal_load_lds_dwordx4 %0, %1\n\tglobal_load_lds_dwordx4 %0, %1 offset:1024\n\t"
                 "global_load_lds_dwordx4 %0, %1 offset:2048\n\tglobal_load_lds_dwordx4 %0, %1 offset:3072"
                 :: "v"(voff), "s"(sbase), "s"(lds_addr) : "memory");
}
#define WAIT_BAR(N) asm volatile("s_waitcnt vmcnt(" #N ") lgkmcnt(0)\n\ts_barrier" ::: "memory")
#define WAIT_CNT(c) do { if ((c) == 8) { WAIT_BAR(8); } else if ((c) == 4) { WAIT_BAR(4); } else { WAIT_BAR(0); } } while (0)
typedef LAS const char* lptr;
DI bf16x8 ldsr128(lptr p) { return *(const LAS bf16x8*)p; }
DI bf16x8 ldsr128a(lptr p) { return *(const LAS bf16x8*)p; }
DI s16x4 ldstr(lptr p) { return __builtin_bit_cast(s16x4, __builtin_amdgcn_ds_read_tr16_b64_v4i16((LAS v4i16_t*)p)); }
DI bf16x8 cat(s16x4 a, s16x4 b) { return __builtin_shufflevector(a, b, 0, 1, 2, 3, 4, 5, 6, 7); }
DI float wave_sum(float v) {
#pragma unroll
    for (int o = 32; o >= 1; o >>= 1) v += __shfl_xor(v, o);
    return v;
}
DI float sigmoidf_(float x) { return 1.0f / (1.0f + __expf(-x)); }

constexpr int K1_STAGE = 49152, K1_LDS = 163840;
typedef __amdgpu_buffer_rsrc_t rsrc_t;
DI rsrc_t make_rsrc(const void* p, unsigned bytes) { return __builtin_amdgcn_make_buffer_rsrc((void*)p, (short)0, (int)bytes, 0x00020000); }
DI void store_wt(rsrc_t r, unsigned off, bf16x8 v) {
    __builtin_amdgcn_raw_buffer_store_b128(__builtin_bit_cast(u32x4, v), r, (int)off, 0, 16);
}
#define WAIT_VM(N) asm volatile("s_waitcnt vmcnt(" #N ")" ::: "memory")

#define PF_STRIDE 128
DI void prefetch8k(const char* chunk, unsigned lds_scratch, unsigned lane16  ) {
    unsigned t;
#pragma unroll
    for (int j = 0; j < 128 / PF_STRIDE; ++j)
        asm volatile("v_lshlrev_b32 %0, 3, %1\n\ts_mov_b32 m0, %3\n\ts_nop 0\n\tglobal_load_lds_dword %0, %2 offset:0"
                     : "=&v"(t) : "v"(lane16), "s"(chunk + j * PF_STRIDE), "s"(lds_scratch) : "memory");
}
template <bool ISV, class Pre>
DI void kv_body(const char* cbase, const char* kbase, const char* vbase, rsrc_t outr, unsigned outoff, lptr lds, unsigned lds0, int wid, int lane, const char* pfchunk, Pre&& pre) {
    const int l31 = lane & 31, hi = lane >> 5;
    unsigned voffW[2], voffC[2], voffG[2];
#pragma unroll
    for (int j = 0; j < 2; ++j) {
        const int n = 2 * wid + j;
        { const int row = 4 * n + (lane >> 4); voffW[j] = row * 8192 + (((lane & 15) ^ ((row & 3) << 2)) << 4); }
        { const int row = 8 * n + (lane >> 3); voffC[j] = row * 1024 + (((lane & 7) ^ ((row >> 1) & 7)) << 4); }
        voffG[j] = (lane ^ ((wid + 8 * j) & 15)) << 4;
    }
    auto stage = [&](int s) {
        const unsigned b = lds0 + (s % 3) * K1_STAGE + 2 * wid * 1024;
#pragma unroll
        for (int j = 0; j < 2; ++j) glds16(kbase + (size_t)s * 64 * 8192, voffW[j], b + j * 1024);
#pragma unroll
        for (int j = 0; j < 2; ++j) glds16(vbase + (size_t)s * 64 * 8192, voffW[j], b + 16384 + j * 1024);
#pragma unroll
        for (int j = 0; j < 2; ++j) glds16(cbase + s * 128, voffC[j], b + 32768 + j * 1024);
    };
    auto gbuf = [&](int k) { return k == 4 ? 98304 : k == 5 ? 131072 : k == 6 ? 0 : 49152; };
    auto stageG = [&](int k) {
#pragma unroll
        for (int j = 0; j < 4; ++j) glds16(cbase + (size_t)(32 * k + wid + 8 * j) * 1024, voffG[j & 1], lds0 + gbuf(k) + (wid + 8 * j) * 1024);
    };
    const int tq = (lane & 15) >> 2, tp = lane & 3, tblk = (lane >> 4) & 1;
    const lptr wA = lds + (ISV ? 16384 : 0) + (8 * hi + tq) * 256 + (((wid & 3) ^ tq) << 6) + tblk * 32 + tp * 8;
    lptr cA[4];
#pragma unroll
    for (int ks = 0; ks < 4; ++ks) cA[ks] = lds + 32768 + l31 * 128 + (((2 * ks + hi) ^ ((l31 >> 1) & 7)) << 4);
    auto mm = [&](const bf16x8& w, const bf16x8& c, const f32x16& acc) { return ISV ? MFMA(c, w, acc) : MFMA(w, c, acc); };
    auto out_tile = [&](int kg, const f32x16& acc) {
        store_wt(outr, outoff + kg * 8192, pack8(acc, 0));
        store_wt(outr, outoff + kg * 8192 + 1024, pack8(acc, 1));
    };

    bf16x8 wf[32];
    f32x16 acc[4] = {};
    lptr gA[8];
    stage(0); stage(1); stage(2);
    pre();
    auto rdW = [&](int s, int ks) { const int bo = (s % 3) * K1_STAGE; wf[4 * s + ks] = cat(ldstr(wA + bo + ks * 4096), ldstr(wA + bo + ks * 4096 + 1024)); };
    auto rdC = [&](int s, int i) { return ldsr128(cA[i >> 2] + (s % 3) * K1_STAGE + (i & 3) * 4096); };
    auto rdG = [&](int k, int ks) { return ldsr128(gA[ks & 7] + gbuf(k) + (ks >> 3) * 256); };
    auto dmaS = [&](int s, int j) {
        const unsigned b = lds0 + (s % 3) * K1_STAGE + 2 * wid * 1024;
        if (j < 2) glds16(kbase + (size_t)s * 64 * 8192, voffW[j], b + j * 1024);
        else if (j < 4) glds16(vbase + (size_t)s * 64 * 8192, voffW[j - 2], b + 16384 + (j - 2) * 1024);
        else glds16(cbase + s * 128, voffC[j - 4], b + 32768 + (j - 4) * 1024);
    };
    auto dmaG = [&](int k, int j) { glds16(cbase + (size_t)(32 * k + wid + 8 * j) * 1024, voffG[j & 1], lds0 + gbuf(k) + (wid + 8 * j) * 1024); };
    bf16x8 cf[8];
    WAIT_BAR(12);
    rdW(0, 0);
#pragma unroll
    for (int i = 0; i < 4; ++i) cf[i] = rdC(0, i);
    static_for<128>([&](auto SI) {
        {
            constexpr int s = SI / 16, i = SI % 16;
            if (i == 12) {
                if (s < 6) { WAIT_BAR(6); } else if (s == 6) { WAIT_BAR(8); } else { WAIT_BAR(8); }
            }
            if (s == 7 && i == 11) {
                int ln = lane; asm volatile("" : "+v"(ln));
#pragma unroll
                for (int j = 0; j < 8; ++j) gA[j] = lds + (ln & 31) * 1024 + (((2 * j + (ln >> 5)) ^ (ln & 15)) << 4);
            }
            const int nx = i + 4;
            if (nx < 16) { if ((nx & 3) == 0) rdW(s, nx >> 2); cf[nx & 7] = rdC(s, nx); }
            else if (s < 7) { if (nx == 16) rdW(s + 1, 0); cf[nx & 7] = rdC(s + 1, nx - 16); }
            else cf[nx & 7] = rdG(4, nx - 16);
            acc[i & 3] = mm(wf[4 * s + (i >> 2)], cf[i & 7], acc[i & 3]);
            if (i >= 12) {
                const int j = i - 12;
                if (s + 3 < 8) dmaS(s + 3, j);
                else if (s == 5) dmaG(4, j);
                else if (s == 6) dmaG(6, j);
                else dmaG(7, j);
            } else if (i < 2) {
                if (s >= 1 && s + 2 < 8) dmaS(s + 2, 4 + i);
            } else if (i < 6) {
                if (s == 6) dmaG(5, i - 2);
            }
            SBAR();
        }
    });
    f32x16 a2[2];
    static_for<128>([&](auto KI) {
        {
            constexpr int k = 4 + KI / 32, ks = KI % 32;
            if (ks == 28 && k < 7) {
                if (k == 4) { WAIT_BAR(16); } else if (k == 5) { WAIT_BAR(14); } else { WAIT_BAR(12); }
            }
            const int nx = ks + 4;
            if (nx < 32) cf[nx & 7] = rdG(k, nx); else if (k < 7) cf[nx & 7] = rdG(k + 1, nx - 32);
            if (ks == 0) a2[k & 1] = mm(wf[0], cf[0], f32x16{}); else a2[k & 1] = mm(wf[ks], cf[ks & 7], a2[k & 1]);
            if (k == 4) { if (ks >= 2 && ks < 18 && (ks & 3) == 2) out_tile((ks - 2) >> 2, acc[(ks - 2) >> 2]); }
            else if (ks == 3) out_tile(k - 1, a2[(k - 1) & 1]);
            SBAR();
        }
    });
    out_tile(7, a2[1]);
}

__global__ __launch_bounds__(512, 2)
void kv_proj_kernel(const u16* __restrict__ ckv, const u16* __restrict__ wuk, const u16* __restrict__ wuv,
                    const u16* __restrict__ ax, const u16* __restrict__ gatew, const u16* __restrict__ bx, const u16* __restrict__ wr,
                    const u16* __restrict__ wq, const u16* __restrict__ wg, const u16* __restrict__ wu, const u16* __restrict__ wd,
                    char* __restrict__ ws) {
    extern __shared__ __attribute__((aligned(16))) char lds_[];
    const lptr lds = (lptr)lds_;
    const unsigned lds0 = (unsigned)(uintptr_t)lds;
    const int tid = threadIdx.x, lane = tid & 63;
    const int wid = __builtin_amdgcn_readfirstlane(tid >> 6);
    const int bid = blockIdx.x, h = (bid & 7) * 4 + ((bid >> 3) & 3), p = bid >> 5;
    STAMP(0, 0);
    const int tok = bid * 8 + wid;
    const bf16x8 bxv = *(const bf16x8*)(bx + tok * DM + lane * 8), wrv = *(const bf16x8*)(wr + lane * 8);
    const bf16x8 gwv = *(const bf16x8*)(gatew + lane * 8);
    auto pre = [&]() {
        float sr = 0.f, sg = 0.f;
#pragma unroll
        for (int j = 0; j < 8; ++j) { sr += bf2f((u16)bxv[j]) * bf2f((u16)wrv[j]); sg += bf2f((u16)bxv[j]) * bf2f((u16)gwv[j]); }
        sr = wave_sum(sr); sg = wave_sum(sg);
        if (lane == 0) { ((float*)(ws + WS_R))[tok] = bfround(sr); ((float*)(ws + WS_G))[tok] = sigmoidf_(sg); }
        STAMP(0, 1);
    };
    const char* cbase = (const char*)ckv + (size_t)p * 256 * 1024;
    const char* kbase = (const char*)wuk + h * 256;
    const char* vbase = (const char*)wuv + h * 256;
    const unsigned outoff = (unsigned)((h * 64 + 8 * p) * 8 + 2 * (wid & 3)) * 1024 + lane * 16;
    const unsigned kvbytes = (unsigned)((size_t)SEQ * NHD * 2);
    const int pc = bid * 8 + wid;
    const char* pfchunk = pc < 512 ? (const char*)wq + (size_t)pc * 8192 : pc < 736 ? (const char*)wg + (size_t)(pc - 512) * 8192
                        : pc < 960 ? (const char*)wu + (size_t)(pc - 736) * 8192 : (const char*)wd + (size_t)((pc - 960) % 224) * 8192;
    pfchunk -= (size_t)((unsigned)((h * 64 + 8 * p) * 8 + 2 * (wid & 3)) * 1024) * 8;
    if (wid < 4) kv_body<false>(cbase, kbase, vbase, make_rsrc(ws + WS_KF, kvbytes), outoff, lds, lds0, wid, lane, pfchunk, pre);
    else         kv_body<true>(cbase, kbase, vbase, make_rsrc(ws + WS_VF, kvbytes), outoff, lds, lds0, wid, lane, pfchunk, pre);
    STAMP(0, 3);
    asm volatile("s_waitcnt vmcnt(0)" ::: "memory");
    STAMP(0, 4);
}

DI unsigned umax_(unsigned a, unsigned b) { return a > b ? a : b; }
DI unsigned umin_(unsigned a, unsigned b) { return a < b ? a : b; }
DI void ld_u16(unsigned& d, const void* sbase, unsigned voff) { asm volatile("global_load_ushort %0, %1, %2" : "=v"(d) : "v"(voff), "s"(sbase)); }
DI void ld_x4(u32x4& d, const void* sbase, unsigned voff) { asm volatile("global_load_dwordx4 %0, %1, %2" : "=v"(d) : "v"(voff), "s"(sbase)); }
DI void ld_u16_nop(unsigned& d, const void* sbase, unsigned voff) { asm volatile("s_nop 4\n\tglobal_load_ushort %0, %1, %2" : "=v"(d) : "v"(voff), "s"(sbase)); }
DI void ld_x4_nop(u32x4& d, const void* sbase, unsigned voff) { asm volatile("s_nop 4\n\tglobal_load_dwordx4 %0, %1, %2" : "=v"(d) : "v"(voff), "s"(sbase)); }
DI float lo16(unsigned v) { return __uint_as_float(v << 16); }
DI float hi16(unsigned v) { return __uint_as_float(v & 0xFFFF0000u); }

constexpr int K2_QST = 49152, K2_SLOT = 32768, K2_QB = 131072, K2_LDS = 163840;
constexpr float QSCALE = 0.08838834764831845f * 1.4426950408889634f;
constexpr float THR = 8.0f;

#define SETPRIO(x) __builtin_amdgcn_s_setprio(x)
struct SMState { float mhat, l; f32x16 negm; };
#define PIN(x) asm volatile("" : "+v"(x))
DI float max3(float a, float b, float c) { return fmaxf(fmaxf(a, b), c); }

#define EXP2(x) __builtin_amdgcn_exp2f(x)
#define VOFF(i) (((i) >> 3) * 8192 + (2 * ((i) & 3) + (((i) >> 2) & 1)) * 1024)
#define KOFF(i) (((i) & 1) * 8192 + ((i) >> 1) * 1024)
DI float fadd_s(float a, float b) { float r; asm("v_add_f32_e32 %0, %1, %2" : "=v"(r) : "v"(a), "v"(b)); return r; }
DI float max3_s(float a, float b, float c) { float r; asm("v_max3_f32 %0, %1, %2, %3" : "=v"(r) : "v"(a), "v"(b), "v"(c)); return r; }
DI float max2_s(float a, float b) { float r; asm("v_max_f32_e32 %0, %1, %2" : "=v"(r) : "v"(a), "v"(b)); return r; }
template <int OFF> DI void glds_step(const void* sbase, unsigned voff, unsigned lds_addr) {
    if (OFF == 0) asm volatile("s_mov_b32 m0, %2\n\ts_nop 0\n\tglobal_load_lds_dwordx4 %0, %1" :: "v"(voff), "s"(sbase), "s"(lds_addr));
    else asm volatile("global_load_lds_dwordx4 %0, %1 offset:%c2" :: "v"(voff), "s"(sbase), "i"(OFF));
}
DI f32x16 mfma_c(bf16x8 a, bf16x8 b, const f32x16& c) {
    f32x16 d; asm volatile("v_mfma_f32_32x32x16_bf16 %0, %1, %2, %3" : "=&v"(d) : "v"(a), "v"(b), "v"(c)); return d;
}
struct DmaJob { const char* src0; const char* src1; unsigned dst0, dst1; bool has0, has1; };

#define LA 4
template <bool QK, bool FIN, class Mid>
DI void attn_A(lptr kp, const lptr& vpn, const bf16x8 (&qf)[8], const f32x16& negm, const bf16x8 (&kpre)[LA], bf16x8 (&vpre)[LA], f32x16& s0, f32x16& s1,
               const f32x16& p0, const f32x16& p1, bf16x8 (&pf)[4], float& l, const DmaJob& dj, unsigned vlin, Mid&& mid) {
    bf16x8 fr[16];
    unsigned pw[16];
    float sa = 0.f, sb = 0.f;
#pragma unroll
    for (int i = 0; i < LA; ++i) fr[i] = kpre[i];
#pragma unroll
    for (int i = 0; i < 16; ++i) {
        if (QK && i + LA < 16) fr[i + LA] = ldsr128a(kp + KOFF(i + LA));
        if (i + LA >= 16) vpre[i + LA - 16] = ldsr128a(vpn + VOFF(i + LA - 16));
        if (QK) {
            if (i == 0) s0 = mfma_c(fr[0], qf[0], negm);
            else if (i == 1) s1 = mfma_c(fr[1], qf[0], negm);
            else if (i & 1) s1 = MFMA(fr[i], qf[i >> 1], s1);
            else s0 = MFMA(fr[i], qf[i >> 1], s0);
        }
        if (FIN) {
            const float e0 = i < 8 ? p0[2 * i] : p1[2 * i - 16], e1 = i < 8 ? p0[2 * i + 1] : p1[2 * i - 15];
            if (i == 0) { sa = e0; sb = e1; } else { sa = fadd_s(sa, e0); sb = fadd_s(sb, e1); PIN(sa); PIN(sb); }
            pw[i] = cvtpk(e0, e1);
            PIN(pw[i]);
        }
        if (i == 3) mid();
        if (QK) {
            if (i == 1 && dj.has0) { glds_step<0>(dj.src0, vlin, dj.dst0); glds_step<1024>(dj.src0, vlin, dj.dst0); }
            if (i == 5 && dj.has0) { glds_step<2048>(dj.src0, vlin, dj.dst0); glds_step<3072>(dj.src0, vlin, dj.dst0); }
            if (i == 9 && dj.has1) { glds_step<0>(dj.src1, vlin, dj.dst1); glds_step<1024>(dj.src1, vlin, dj.dst1); }
            if (i == 13 && dj.has1) { glds_step<2048>(dj.src1, vlin, dj.dst1); glds_step<3072>(dj.src1, vlin, dj.dst1); }
        }
        SBAR();
    }
    if (FIN) {
        l += sa + sb;
#pragma unroll
        for (int i = 0; i < 4; ++i) { u32x4 w = {pw[4 * i], pw[4 * i + 1], pw[4 * i + 2], pw[4 * i + 3]}; pf[i] = __builtin_bit_cast(bf16x8, w); }
    }
}
DI void dma_plain(const DmaJob& dj, unsigned vlin) {
    if (dj.has0) glds16x4(dj.src0, vlin, dj.dst0);
    if (dj.has1) glds16x4(dj.src1, vlin, dj.dst1);
}
template <bool PV, bool SM, class Mid>
DI void attn_B(lptr vp, const lptr& kpn, f32x16 (&o)[4], const bf16x8 (&pf)[4], const bf16x8 (&vpre)[LA], bf16x8 (&kpre)[LA],
               f32x16& s0, f32x16& s1, SMState& st, bool first, bool mask, int qrel  , bool& pend, float& fpend, Mid&& mid) {
    bf16x8 fr[16];
    float ma = 0.f, mb = 0.f, mc = 0.f;
#pragma unroll
    for (int i = 0; i < LA; ++i) fr[i] = vpre[i];
    if (SM && __builtin_expect(mask, 0)) {
        const float NEG = -__builtin_inff();
#pragma unroll
        for (int r = 0; r < 16; ++r) { const int c = (r & 3) + 8 * (r >> 2);
            if (c > qrel) s0[r] = NEG;
            if (c + 32 > qrel) s1[r] = NEG; }
    }
    SETPRIO(1);
    static_for<16>([&](auto I) {
        constexpr int i = I;
        if (PV && i + LA < 16) fr[i + LA] = ldsr128a(vp + VOFF(i + LA));
        if (i + LA >= 16) kpre[i + LA - 16] = ldsr128a(kpn + KOFF(i + LA - 16));
        if (PV) o[i & 3] = MFMA(fr[i], pf[i >> 2], o[i & 3]);
        if (SM) {
            if (i == 0) { ma = max3(s0[0], s0[1], s0[2]); mb = max3(s1[0], s1[1], s1[2]); mc = max3(s0[3], s0[4], s1[3]); PIN(ma); PIN(mb); PIN(mc); }
            if (i == 1) { ma = max3_s(ma, s0[5], s0[6]); mb = max3_s(mb, s1[4], s1[5]); mc = max3_s(mc, s0[7], s1[6]); PIN(ma); PIN(mb); PIN(mc); }
            if (i == 2) { ma = max3_s(ma, s0[8], s0[9]); mb = max3_s(mb, s1[7], s1[8]); mc = max3_s(mc, s0[10], s1[9]); PIN(ma); PIN(mb); PIN(mc); }
            if (i == 3) { ma = max3_s(ma, s0[11], s0[12]); mb = max3_s(mb, s1[10], s1[11]); mc = max3_s(mc, s0[13], s1[12]); PIN(ma); PIN(mb); PIN(mc); }
            if (i == 4) { ma = max3_s(ma, s0[14], s0[15]); mb = max3_s(mb, s1[13], s1[14]); mc = max2_s(mc, s1[15]); PIN(ma); PIN(mb); PIN(mc); }
            if (i == 5) {
                float rm = max3_s(ma, mb, mc);
                auto rr = __builtin_amdgcn_permlane32_swap(__float_as_uint(rm), __float_as_uint(rm), false, false);
                rm = max2_s(__uint_as_float(rr[0]), __uint_as_float(rr[1]));
                if (__builtin_expect(first || __any(rm > THR), 0)) {
                    const float dl = first ? rm : fmaxf(rm, 0.f), f = first ? 1.0f : __builtin_amdgcn_exp2f(-dl);
                    st.mhat += dl; st.l *= f;
#pragma unroll
                    for (int r = 0; r < 16; ++r) { s0[r] -= dl; s1[r] -= dl; st.negm[r] = -st.mhat; }
                    pend = !first; fpend = f;
                }
            }
            if (i >= 6) {
                constexpr int e0 = i < 14 ? 3 * (i - 6) : 24 + 4 * (i - 14), e1 = i < 14 ? e0 + 3 : e0 + 4;
#pragma unroll
                for (int e = e0; e < e1; ++e) { if (e < 16) { s0[e] = EXP2(s0[e]); } else { s1[e - 16] = EXP2(s1[e - 16]); } }
                PIN(s0); PIN(s1);
            }
        }
        if (i == 7) mid();
        SBAR();
    });
    SETPRIO(0);
}

__global__ __launch_bounds__(512, 2)
void attn_kernel(const u16* __restrict__ x, const u16* __restrict__ wq, const u16* __restrict__ wo,
                 const u16* __restrict__ bx, const u16* __restrict__ wg, const u16* __restrict__ wu, const u16* __restrict__ wd,
                 char* __restrict__ ws) {
    extern __shared__ __attribute__((aligned(16))) char lds_[];
    const lptr lds = (lptr)lds_;
    const unsigned lds0 = (unsigned)(uintptr_t)lds;
    const int tid = threadIdx.x, lane = tid & 63, l31 = lane & 31, hi = lane >> 5;
    const int wid = __builtin_amdgcn_readfirstlane(tid >> 6);
    const int bid = blockIdx.x, h = (bid & 7) * 4 + ((bid >> 3) & 3), bi = bid >> 5;
    const int grp = wid >> 2, rg = wid & 3;
    const int blkA = bi, blkB = 15 - bi;

    STAMP(1, 0);
    LAS unsigned* sk = (LAS unsigned*)((LAS char*)lds_ + 147456);
    LAS float* hs = (LAS float*)((LAS char*)lds_ + 147456 + 128);
    LAS float* red = (LAS float*)((LAS char*)lds_ + 147456 + 256);
    constexpr int MOE_WD = 147456 + 2048;
    const int cg = (bid & 7) * 28 + ((bid >> 3) < 28 ? (bid >> 3) : 27);
    const int f0 = cg * MOE_COLS;
    u32x4 rv, wgv, wuv;
    unsigned xs0, xs1;
    int sel0 = 0, sel1 = 0;
    auto moe_step = [&](int step) {
        if (step == 0) {
            asm volatile("" : "+v"(rv));
            unsigned k1 = 0, k2 = 0;
#pragma unroll
            for (int j = 0; j < 4; ++j) {
                const unsigned u = rv[j] >> 16;
                const unsigned o = (u & 0x8000u) ? (~u & 0xFFFFu) : (u | 0x8000u);
                const unsigned key = (o << 16) | (unsigned)(0xFFFF - (tid * 4 + j));
                k2 = umax_(k2, umin_(k1, key)); k1 = umax_(k1, key);
            }
#pragma unroll
            for (int o = 32; o >= 1; o >>= 1) {
                const unsigned b1 = __shfl_xor(k1, o), b2 = __shfl_xor(k2, o);
                k2 = umax_(umin_(k1, b1), umax_(k2, b2)); k1 = umax_(k1, b1);
            }
            if (lane == 0) { sk[wid * 2] = k1; sk[wid * 2 + 1] = k2; }
        } else if (step == 1) {
            unsigned k1 = 0, k2 = 0;
#pragma unroll
            for (int j = 0; j < 16; ++j) { const unsigned key = sk[j]; k2 = umax_(k2, umin_(k1, key)); k1 = umax_(k1, key); }
            k1 = __builtin_amdgcn_readfirstlane(k1); k2 = __builtin_amdgcn_readfirstlane(k2);
            sel0 = 0xFFFF - (int)(k1 & 0xFFFFu); sel1 = 0xFFFF - (int)(k2 & 0xFFFFu);
            ld_u16_nop(xs0, (const char*)bx + sel0 * 1024, tid * 2);
            ld_u16_nop(xs1, (const char*)bx + sel1 * 1024, tid * 2);
            if (bid == 0 && tid == 0) {
                const unsigned o0 = k1 >> 16, o1 = k2 >> 16;
                const float r0 = __uint_as_float(((o0 & 0x8000u) ? (o0 & 0x7FFFu) : (~o0 & 0xFFFFu)) << 16);
                const float r1 = __uint_as_float(((o1 & 0x8000u) ? (o1 & 0x7FFFu) : (~o1 & 0xFFFFu)) << 16);
                int* sel = (int*)(ws + WS_SEL);
                sel[0] = sel0; sel[1] = sel1; ((float*)sel)[2] = sigmoidf_(r0); ((float*)sel)[3] = sigmoidf_(r1);
            }
        } else if (step == 2) {
            asm volatile("" : "+v"(xs0), "+v"(xs1), "+v"(wgv), "+v"(wuv));
            const float a0 = lo16(xs0), a1 = lo16(xs1);
            float v[32];
#pragma unroll
            for (int c = 0; c < 8; ++c) {
                const float g = (c & 1) ? hi16(wgv[c >> 1]) : lo16(wgv[c >> 1]), u = (c & 1) ? hi16(wuv[c >> 1]) : lo16(wuv[c >> 1]);
                v[c] = a0 * g; v[8 + c] = a0 * u; v[16 + c] = a1 * g; v[24 + c] = a1 * u;
            }
#pragma unroll
            for (int i = 0; i < 16; ++i) { auto r = __builtin_amdgcn_permlane32_swap(__float_as_uint(v[i]), __float_as_uint(v[i + 16]), false, false);
                v[i] = __uint_as_float(r[0]) + __uint_as_float(r[1]); }
#pragma unroll
            for (int i = 0; i < 8; ++i) { auto r = __builtin_amdgcn_permlane16_swap(__float_as_uint(v[i]), __float_as_uint(v[i + 8]), false, false);
                v[i] = __uint_as_float(r[0]) + __uint_as_float(r[1]); }
#pragma unroll
            for (int hw = 4; hw >= 1; hw >>= 1) {
                const bool up_ = lane & (2 * hw);
#pragma unroll
                for (int i = 0; i < hw; ++i) { const float send = up_ ? v[i] : v[i + hw], keep = up_ ? v[i + hw] : v[i]; v[i] = keep + __shfl_xor(send, 2 * hw); }
            }
            v[0] += __shfl_xor(v[0], 1);
            if (!(lane & 1)) red[wid * 32 + (lane >> 1)] = v[0];
        } else if (step == 3) {
            if (tid < 16) {
                const int c = tid & 7, t = tid >> 3;
                float g = 0.f, u = 0.f;
#pragma unroll
                for (int w = 0; w < 8; ++w) { g += red[w * 32 + 16 * t + c]; u += red[w * 32 + 16 * t + 8 + c]; }
                g = bfround(g); u = bfround(u);
                hs[t * 8 + c] = bfround(bfround(g * sigmoidf_(g)) * u);
            }
        } else {
            float y0 = 0.f, y1 = 0.f;
#pragma unroll
            for (int j = 0; j < 8; ++j) { const float w = bf2f(*(const LAS u16*)((LAS char*)lds_ + MOE_WD + j * 1024 + tid * 2)); y0 += hs[j] * w; y1 += hs[8 + j] * w; }
            float* yp = (float*)(ws + WS_YP) + (size_t)cg * 1024;
            yp[tid] = y0; yp[512 + tid] = y1;
        }
    };
    ld_x4(rv, ws + WS_R, tid * 16);

    const char* Kh = ws + WS_KF + (size_t)h * 64 * 8192;
    const unsigned vlin = lane * 16;
    bf16x8 qf[8];
    {
        unsigned voffX[4], voffW[2];
        const int myblk = grp ? blkB : blkA;
#pragma unroll
        for (int j = 0; j < 4; ++j) { const int r = 8 * j + (lane >> 3);
            voffX[j] = r * 1024 + (((lane & 7) ^ ((r >> 1) & 7)) << 4); }
#pragma unroll
        for (int j = 0; j < 2; ++j) { const int n = 2 * wid + j, row = 4 * n + (lane >> 4);
            voffW[j] = row * 8192 + (((lane & 15) ^ ((row & 3) << 2)) << 4); }
        const char* xbase = (const char*)x + (size_t)(myblk * 128 + rg * 32) * 1024;
        const char* wbase = (const char*)wq + h * 256;
        auto dmaS = [&](int s, int j) {
            const unsigned b = lds0 + (s % 3) * K2_QST;
            if (j < 2) glds16(wbase + (size_t)s * 64 * 8192, voffW[j], b + 32768 + (2 * wid + j) * 1024);
            else glds16(xbase + s * 128, voffX[j - 2], b + (4 * wid + j - 2) * 1024);
        };
        lptr xA[4];
#pragma unroll
        for (int ks = 0; ks < 4; ++ks) xA[ks] = lds + (32 * wid + l31) * 128 + (((2 * ks + hi) ^ ((l31 >> 1) & 7)) << 4);
        const int tq = (lane & 15) >> 2, tp = lane & 3, tblk = (lane >> 4) & 1;
        lptr wA[4];
#pragma unroll
        for (int c = 0; c < 4; ++c) wA[c] = lds + 32768 + (8 * hi + tq) * 256 + ((c ^ tq) << 6) + tblk * 32 + tp * 8;
        auto rdW = [&](int s, int i) { const int o = (s % 3) * K2_QST + (i >> 2) * 4096; return cat(ldstr(wA[i & 3] + o), ldstr(wA[i & 3] + o + 1024)); };
        auto rdX = [&](int s, int ks) { return ldsr128(xA[ks] + (s % 3) * K2_QST); };
        f32x16 acc[4] = {};
        bf16x8 wfr[8], xfr[2];
#pragma unroll
        for (int s = 0; s < 3; ++s)
#pragma unroll
            for (int j = 0; j < 6; ++j) dmaS(s, j);
        WAIT_BAR(12);
        STAMP(1, 1);
        xfr[0] = rdX(0, 0);
#pragma unroll
        for (int i = 0; i < 4; ++i) wfr[i] = rdW(0, i);
        static_for<128>([&](auto SI) {
            {
                constexpr int s = SI / 16, i = SI % 16;
                if (i == 12 && s < 7) {
                    if (s == 0) { WAIT_BAR(9); } else if (s == 1) { WAIT_BAR(11); } else if (s < 6) { WAIT_BAR(6); } else if (s == 6) { WAIT_BAR(0); }
                }
                const int nx = i + 4;
                if (nx < 16) { if ((nx & 3) == 0) xfr[(nx >> 2) & 1] = rdX(s, nx >> 2); wfr[nx & 7] = rdW(s, nx); }
                else if (s < 7) { if (nx == 16) xfr[0] = rdX(s + 1, 0); wfr[nx & 7] = rdW(s + 1, nx - 16); }
                acc[i & 3] = MFMA(wfr[i & 7], xfr[(i >> 2) & 1], acc[i & 3]);
                if (i >= 12) { if (s + 3 < 8) dmaS(s + 3, i - 12); }
                else if (s == 0) {
                    if (i == 6) ld_x4(wgv, (const char*)wg + f0 * 2, tid * (FF * 2));
                    else if (i == 7) ld_x4(wuv, (const char*)wu + f0 * 2, tid * (FF * 2));
                    else if (i == 8) glds16((const char*)wd + (size_t)(f0 + wid) * 1024, vlin, lds0 + MOE_WD + wid * 1024);
                }
                else if (i < 2) { if (s + 2 < 8) dmaS(s + 2, 4 + i); }
                if (s == 6 && grp == 0) {
                    if (i == 12) glds16x4(Kh + rg * 4096, vlin, lds0 + rg * 4096);
                    if (i == 14) glds16x4(Kh + 16384 + rg * 4096, vlin, lds0 + 32768 + rg * 4096);
                }
                if (i == 3 && (s == 0 || s == 1 || s == 3)) moe_step(s == 3 ? 2 : s);
                if (i == 13 && (s == 3 || s == 4)) moe_step(s);
                SBAR();
            }
        });
#pragma unroll
        for (int c = 0; c < 4; ++c) { qf[2 * c] = pack8(acc[c], 0, QSCALE); qf[2 * c + 1] = pack8(acc[c], 1, QSCALE); }
    }

    STAMP(1, 2);
    const int n1 = 2 * bi + 2;
    auto tile_of = [&](int n, int g) { return n < n1 ? n : n1 + 2 * (n - n1) + g; };
    auto jstar = [&](int tn) { return tn <= 2 * n1 ? tn : tn <= 2 * n1 + 2 ? tn + 1 : 2 * tn - 2 * n1 - 2; };
    auto jg1 = [&](int tn) { return tn == 2 * n1 + 1 ? tn : 2 * tn - 2 * n1 - 1; };
    int tb_d0, tb_d1, tb_kv, tb_rel;
    auto make_job = [&](int tn, int& d0, int& d1) {
        const int n = tn >> 1;
        int a_off, b_off; bool a_ok, b_ok;
        const int kb = (int)WS_KF + h * 64 * 8192 + rg * 4096, vb = (int)WS_VF + h * 64 * 8192 + rg * 4096;
        if (tn & 1) { a_ok = n >= 1 && n <= 17; a_off = vb + tile_of(n - 1, 0) * 16384; b_ok = n >= n1 && n <= 16; b_off = kb + tile_of(n, 1) * 16384; }
        else        { a_ok = n <= 16;           a_off = kb + tile_of(n, 0) * 16384;     b_ok = n - 2 >= n1 && n <= 18; b_off = vb + tile_of(n - 2, 1) * 16384; }
        const int da = a_off | (jstar(tn) & 7) | 8, db = b_off | (jg1(tn) & 7) | 8;
        d0 = a_ok ? da : b_ok ? db : 0; d1 = (a_ok && b_ok) ? db : 0;
    };
    {
        const int n = lane < 19 ? lane : 18;
        make_job(2 * n + grp + 3, tb_d0, tb_d1);
        const int ks = (n < n1 || grp == 0 ? jstar(2 * n) : jg1(2 * n + 1)) & 7;
        const int vs = (n < n1 || grp == 0 ? jstar(2 * n + 3) : jg1(2 * n + 4)) & 7;
        tb_kv = ks | vs << 8;
        tb_rel = 128 * ((grp || n >= n1) ? blkB : blkA) + 32 * rg - 64 * tile_of(n, grp);
    }
    auto job_from = [&](int d0, int d1) -> DmaJob {
        DmaJob d;
        d.has0 = d0 & 8; d.has1 = d1 & 8;
        d.src0 = ws + (unsigned)(d0 & ~4095); d.src1 = ws + (unsigned)(d1 & ~4095);
        d.dst0 = lds0 + (d0 & 7) * 16384 + rg * 4096; d.dst1 = lds0 + (d1 & 7) * 16384 + rg * 4096;
        return d;
    };
    auto job = [&](int n) { return job_from(__builtin_amdgcn_readlane(tb_d0, n), __builtin_amdgcn_readlane(tb_d1, n)); };
    if (grp == 1) {
#pragma unroll
        for (int c = 0; c < 8; ++c) *(LAS bf16x8*)((LAS char*)lds_ + K2_QB + rg * 8192 + c * 1024 + lane * 16) = qf[c];
    }
    f32x16 o[4] = {};
    SMState st; st.mhat = 0.f; st.l = 0.f; st.negm = f32x16{};
    const lptr kp0 = lds + lane * 16;
    char* ctx = ws + WS_CTX;
    auto store_ctx = [&](int blk, int d, const f32x16& v, float inv) {
        const size_t off = ((size_t)((4 * blk + rg) * 32 + h) * 8 + 2 * d) * 1024 + lane * 16;
        *(bf16x8*)(ctx + off) = pack8(v, 0, inv);
        *(bf16x8*)(ctx + off + 1024) = pack8(v, 1, inv);
    };
    auto half_sum = [&](float v) { auto rr = __builtin_amdgcn_permlane32_swap(__float_as_uint(v), __float_as_uint(v), false, false);
                                   return __uint_as_float(rr[0]) + __uint_as_float(rr[1]); };
    auto kslot = [&](int n) { return kp0 + (__builtin_amdgcn_readlane(tb_kv, n) & 7) * 16384; };
    auto vslot = [&](int n) { return kp0 + ((__builtin_amdgcn_readlane(tb_kv, n) >> 8) & 7) * 16384; };
    WAIT_BAR(0);
    bool first = true, pend = false;
    float invA = 0.f, fpend = 1.f;
    f32x16 sA0, sA1, sB0, sB1; bf16x8 pf[4], vpre[LA], kpre[LA];
#pragma unroll
    for (int i = 0; i < LA; ++i) kpre[i] = ldsr128a(kslot(0) + KOFF(i));
    if (grp == 1) { WAIT_BAR(0); }
    SEG_DECL; SEG_START();
    const int nsw = grp == 0 ? n1 - 1 : 100;
    auto headB = [&](int n) {
        if (__builtin_expect(n == nsw + 1, 0)) { invA = 1.0f / half_sum(st.l); st.mhat = 0.f; st.l = 0.f; first = true; }
    };
    auto tailB = [&](int n) {
        first = false;
        if (__builtin_expect(pend || (unsigned)(n - nsw) < 2u, 0)) {
            if (pend) {
#pragma unroll
                for (int d = 0; d < 4; ++d)
#pragma unroll
                    for (int r = 0; r < 16; ++r) o[d][r] *= fpend;
                pend = false;
            }
            if (n == nsw + 1) {
#pragma unroll
                for (int d = 0; d < 4; ++d) store_ctx(blkA, d, o[d], invA);
#pragma unroll
                for (int d = 0; d < 4; ++d) o[d] = f32x16{};
            }
            if (n == nsw) {
#pragma unroll
                for (int c = 0; c < 8; ++c) qf[c] = ldsr128(lds + K2_QB + rg * 8192 + c * 1024 + lane * 16);
                st.negm = f32x16{};
            }
        }
    };
#define BARRIER_L() asm volatile("s_waitcnt lgkmcnt(0)\n\ts_barrier" ::: "memory")
    lptr kcur = kslot(0), vcur = kp0;
    DmaJob dj = job(0);
    int rel = 0;
    auto pinjob = [&]() { asm volatile("" : "+s"(dj.src0), "+s"(dj.src1), "+s"(dj.dst0), "+s"(dj.dst1)); };
    auto midA = [&](int n) { vcur = vslot(n > 0 ? n - 1 : 0); rel = __builtin_amdgcn_readlane(tb_rel, n); asm volatile("" : "+v"(vcur), "+s"(rel)); };
    auto midB = [&](int n) { kcur = kslot(n + 1); dj = job(n + 1); asm volatile("" : "+v"(kcur)); pinjob(); };
#define RELARGS first, rel < 63, rel + l31 - 4 * hi, pend, fpend
    attn_A<true, false>(kcur, vcur, qf, st.negm, kpre, vpre, sA0, sA1, sA0, sA1, pf, st.l, dj, vlin, [&]() { midA(0); });
    BARRIER_L();
    attn_B<false, true>(vcur, kcur, o, pf, vpre, kpre, sA0, sA1, st, RELARGS, [&]() { midB(0); });
    tailB(0);
    WAIT_BAR(0);
#define STEP(n, C0, C1, P0, P1) do { \
        attn_A<true, true>(kcur, vcur, qf, st.negm, kpre, vpre, C0, C1, P0, P1, pf, st.l, dj, vlin, [&]() { midA(n); }); \
        SEG(0); \
        BARRIER_L(); \
        SEG(2); \
        headB(n); \
        attn_B<true, true>(vcur, kcur, o, pf, vpre, kpre, C0, C1, st, RELARGS, [&]() { midB(n); }); \
        tailB(n); \
        SEG(3); \
        if ((n) < 16) { WAIT_BAR(0); }                  \
        else {                                          \
            if (wid < 2) asm volatile("global_load_dword %0, %1, %2" : "=v"(pfd) : "v"(vlin * 8), "s"((const char*)wo + (size_t)(bid * 2 + wid) * 8192)); \
            BARRIER_L(); \
        } \
        SEG(4); \
    } while (0)
    unsigned pfd = 0;
    for (int n = 1; n < 17; n += 2) {
        STEP(n, sB0, sB1, sA0, sA1);
        STEP(n + 1, sA0, sA1, sB0, sB1);
    }
#undef STEP
    attn_A<false, true>(kcur, vcur, qf, st.negm, kpre, vpre, sB0, sB1, sA0, sA1, pf, st.l, dj, vlin, [&]() { midA(17); });
    BARRIER_L();
    attn_B<true, false>(vcur, kcur, o, pf, vpre, kpre, sB0, sB1, st, false, false, 0, pend, fpend, [&]() {});
    BARRIER_L();
    SEG_OUT(1);
    if (grp == 0) { BARRIER_L(); }
    STAMP(1, 3);
    BARRIER_L();
    {
        const float lsum = half_sum(st.l);
        LAS char* xch = (LAS char*)lds_;
        LAS char* mine = xch + wid * 8192;
        auto put = [&](int dd, const f32x16& v) {
#pragma unroll
            for (int q4 = 0; q4 < 4; ++q4)
                *(LAS f32x4*)(mine + (dd * 4 + q4) * 1024 + lane * 16) = (f32x4){v[4 * q4], v[4 * q4 + 1], v[4 * q4 + 2], v[4 * q4 + 3]};
        };
        if (grp) { put(0, o[0]); put(1, o[1]); } else { put(0, o[2]); put(1, o[3]); }
        LAS float* ml = (LAS float*)(xch + 65536);
        ml[wid * 128 + lane] = st.mhat; ml[wid * 128 + 64 + lane] = lsum;
        BARRIER_L();
        const int ow = wid ^ 4;
        const float m2 = ml[ow * 128 + lane], l2 = ml[ow * 128 + 64 + lane];
        const float mm = fmaxf(st.mhat, m2);
        const float f1 = __builtin_amdgcn_exp2f(st.mhat - mm), f2 = __builtin_amdgcn_exp2f(m2 - mm);
        const float inv = 1.0f / (lsum * f1 + l2 * f2);
        const float c1 = f1 * inv, c2 = f2 * inv;
        const LAS char* theirs = xch + ow * 8192;
        auto fin = [&](int dd, f32x16 v) {
#pragma unroll
            for (int q4 = 0; q4 < 4; ++q4) {
                const f32x4 t = *(const LAS f32x4*)(theirs + (dd * 4 + q4) * 1024 + lane * 16);
#pragma unroll
                for (int e = 0; e < 4; ++e) v[4 * q4 + e] = v[4 * q4 + e] * c1 + t[e] * c2;
            }
            store_ctx(blkB, 2 * grp + dd, v, 1.0f);
        };
        if (grp) { fin(0, o[2]); fin(1, o[3]); } else { fin(0, o[0]); fin(1, o[1]); }
    }
    asm volatile("s_waitcnt vmcnt(0)" : "+v"(pfd));
    STAMP(1, 4);
    asm volatile("s_waitcnt vmcnt(0)" ::: "memory");
    STAMP(1, 5);
}

constexpr int K3_SLOT = 32768, K3_NS = 5, K3_LDS = K3_NS * K3_SLOT;
__global__ __launch_bounds__(512, 2)
void o_proj_kernel(const u16* __restrict__ wo, const u16* __restrict__ bx, char* __restrict__ ws, u16* __restrict__ out) {
    extern __shared__ __attribute__((aligned(16))) char lds_[];
    const lptr lds = (lptr)lds_;
    const unsigned lds0 = (unsigned)(uintptr_t)lds;
    const int tid = threadIdx.x, lane = tid & 63, l31 = lane & 31, hi = lane >> 5;
    const int wid = __builtin_amdgcn_readfirstlane(tid >> 6);
    const int bid = blockIdx.x, rt = (bid & 7) * 4 + ((bid >> 3) & 3), ct = bid >> 5;
    STAMP(2, 0);
    if (wid >= 4) {
        const int lw = wid - 4;
        const char* csrc = ws + WS_CTX + (size_t)(2 * rt + (lw >> 1)) * 32 * 8192 + (lw & 1) * 4096;
        const char* wsrc = (const char*)wo + ct * 128;
        unsigned voffW[4];
#pragma unroll
        for (int j = 0; j < 4; ++j) { const int row = (lw * 4 + j) * 8 + (lane >> 3);
            voffW[j] = row * 1024 + (((lane & 7) ^ (((row >> 1) & 1) << 2)) << 4); }
        const unsigned vlin = lane * 16;
        auto stage = [&](int hh, int slot) {
            const unsigned b = lds0 + slot * K3_SLOT + lw * 4096;
            glds16x4(csrc + (size_t)hh * 8192, vlin, b);
#pragma unroll
            for (int j = 0; j < 4; ++j) glds16(wsrc + (size_t)hh * 131072, voffW[j], b + 16384 + j * 1024);
        };
        stage(0, 0); stage(1, 1); stage(2, 2); stage(3, 3);
        int slot = 4;
        for (int hh = 0; hh < 32; ++hh) {
            if (hh < 29) { WAIT_BAR(24); } else if (hh == 29) { WAIT_BAR(16); } else if (hh == 30) { WAIT_BAR(8); } else { WAIT_BAR(0); }
            if (hh + 4 < 32) stage(hh + 4, slot);
            slot = slot == K3_NS - 1 ? 0 : slot + 1;
        }
        WAIT_BAR(0);
        WAIT_BAR(0);
        return;
    }
    const int qg2 = wid >> 1, cb = wid & 1;
    const int* sel = (const int*)(ws + WS_SEL);
    const int i0 = sel[0], i1 = sel[1];
    const float sg0 = ((const float*)sel)[2], sg1 = ((const float*)sel)[3];
    const bool has0 = (i0 >> 6) == rt, has1 = (i1 >> 6) == rt;
    const int q = rt * 64 + qg2 * 32 + l31;
    const float g = ((const float*)(ws + WS_G))[q];
    s16x4 xb[4];
#pragma unroll
    for (int q4 = 0; q4 < 4; ++q4) xb[q4] = *(const s16x4*)(bx + (size_t)q * DM + ct * 64 + cb * 32 + 8 * q4 + 4 * hi);
    const bool has = has0 || has1;
    const float* yp = (const float*)(ws + WS_YP) + (size_t)wid * 56 * 1024 + ct * 64 + lane;
    float yp0 = 0.f, yp1 = 0.f, yt[8];
    const int tq = (lane & 15) >> 2, tp = lane & 3, tblk = (lane >> 4) & 1;
    const lptr wA = lds + 16384 + (4 * hi + tq) * 128 + ((cb ^ ((tq >> 1) & 1)) << 6) + tblk * 32 + tp * 8;
    const lptr cA = lds + qg2 * 8192 + lane * 16;
    f32x16 acc = {};
    int bo = 0;
    for (int hh = 0; hh < 32; ++hh) {
        asm volatile("s_waitcnt lgkmcnt(0)\n\ts_barrier" ::: "memory");
        if (hh == 0) STAMP(2, 2);
        if (has && hh < 15) {
            if (hh > 0) { yp0 += (yt[0] + yt[2]) + (yt[4] + yt[6]); yp1 += (yt[1] + yt[3]) + (yt[5] + yt[7]); }
            if (hh < 14) {
#pragma unroll
                for (int j = 0; j < 4; ++j) { yt[2 * j] = yp[(size_t)(4 * hh + j) * 1024]; yt[2 * j + 1] = yp[(size_t)(4 * hh + j) * 1024 + 512]; }
            }
        }
#pragma unroll
        for (int f = 0; f < 8; ++f) {
            const bf16x8 cf = ldsr128(cA + bo + f * 1024);
            const bf16x8 wf = cat(ldstr(wA + bo + f * 2048), ldstr(wA + bo + f * 2048 + 1024));
            acc = MFMA(wf, cf, acc);
        }
        bo = bo == (K3_NS - 1) * K3_SLOT ? 0 : bo + K3_SLOT;
    }
    WAIT_BAR(0);
    STAMP(2, 3);
    LAS float* ysum = (LAS float*)lds_;
    ysum[(wid * 2) * 64 + lane] = yp0; ysum[(wid * 2 + 1) * 64 + lane] = yp1;
    WAIT_BAR(0);
    {
        const bool r0 = (q == i0), r1 = (q == i1);
#pragma unroll
        for (int q4 = 0; q4 < 4; ++q4) {
            const int ml = cb * 32 + 8 * q4 + 4 * hi, m = ct * 64 + ml;
            float res[4];
#pragma unroll
            for (int e = 0; e < 4; ++e) {
                float b = bf2f((u16)xb[q4][e]);
                if (r0 || r1) {
                    float y = 0.f;
#pragma unroll
                    for (int w = 0; w < 4; ++w) y += ysum[(w * 2 + (r1 ? 1 : 0)) * 64 + ml + e];
                    b += y * (r1 ? sg1 : sg0);
                }
                res[e] = g * acc[4 * q4 + e] + (1.0f - g) * b;
            }
            u32x2 w2 = {cvtpk(res[0], res[1]), cvtpk(res[2], res[3])};
            *(u32x2*)(out + (size_t)q * DM + m) = w2;
        }
    }
    STAMP(2, 4);
    asm volatile("s_waitcnt vmcnt(0)" ::: "memory");
    STAMP(2, 5);
}

extern "C" void kernel_launch(void* const* d_in, const int* in_sizes, int n_in, void* d_out, int out_size, void* d_ws, size_t ws_size,
                              hipStream_t stream) {
    const u16* ax = (const u16*)d_in[0]; const u16* ckv = (const u16*)d_in[1]; const u16* wq = (const u16*)d_in[2];
    const u16* wuk = (const u16*)d_in[3]; const u16* wuv = (const u16*)d_in[4]; const u16* wo = (const u16*)d_in[5];
    const u16* bx = ax  ; const u16* wr = (const u16*)d_in[7]; const u16* wg = (const u16*)d_in[8];
    const u16* wu = (const u16*)d_in[9]; const u16* wd = (const u16*)d_in[10]; const u16* gw = (const u16*)d_in[11];
    char* ws = (char*)d_ws;
    (void)hipFuncSetAttribute((const void*)kv_proj_kernel, hipFuncAttributeMaxDynamicSharedMemorySize, K1_LDS);
    (void)hipFuncSetAttribute((const void*)attn_kernel, hipFuncAttributeMaxDynamicSharedMemorySize, K2_LDS);
    (void)hipFuncSetAttribute((const void*)o_proj_kernel, hipFuncAttributeMaxDynamicSharedMemorySize, K3_LDS);
    kv_proj_kernel<<<256, 512, K1_LDS, stream>>>(ckv, wuk, wuv, ax, gw, bx, wr, wq, wg, wu, wd, ws);
    attn_kernel<<<256, 512, K2_LDS, stream>>>(ax, wq, wo, bx, wg, wu, wd, ws);
    o_proj_kernel<<<256, 512, K3_LDS, stream>>>(wo, bx, ws, (u16*)d_out);
}
```

```cpp
#include <hip/hip_runtime.h>
#include <hip/hip_bf16.h>
#include <stdint.h>
#include <utility>

typedef unsigned short u16;
typedef short bf16x8 __attribute__((ext_vector_type(8)));
typedef short s16x4 __attribute__((ext_vector_type(4)));
typedef short v4i16_t __attribute__((ext_vector_type(4)));
typedef float f32x16 __attribute__((ext_vector_type(16)));
typedef float f32x4 __attribute__((ext_vector_type(4)));
typedef unsigned u32x4 __attribute__((ext_vector_type(4)));
typedef unsigned u32x2 __attribute__((ext_vector_type(2)));
#define LAS __attribute__((address_space(3)))
#define DI __device__ __forceinline__
#define MFMA(a, b, c) __builtin_amdgcn_mfma_f32_32x32x16_bf16((a), (b), (c), 0, 0, 0)
#define SBAR() __builtin_amdgcn_sched_barrier(0)

constexpr int SEQ = 2048, DM = 512, NH = 32, HD = 128, NHD = NH * HD;
constexpr int FF = 1792;
constexpr int MOE_WGS = 224, MOE_COLS = 8;

constexpr size_t WS_KF = 0;
constexpr size_t WS_VF = WS_KF + (size_t)SEQ * NHD * 2;
constexpr size_t WS_CTX = WS_VF + (size_t)SEQ * NHD * 2;
constexpr size_t WS_R = WS_CTX + (size_t)SEQ * NHD * 2;
constexpr size_t WS_G = WS_R + SEQ * 4;
constexpr size_t WS_Y = WS_G + SEQ * 4;
constexpr size_t WS_YP = WS_Y + 4096;
constexpr size_t WS_SYNC = WS_YP + 224 * 4096;
constexpr int SY_EPOCH = 0, SY_KV = 256, SY_R = 512, SY_Y = 768, SY_Y2 = 1024, SY_CTX = 1280;

constexpr size_t WS_DBG = 128u << 20;
#define STAMP(K, i) do {} while (0)
#define SEG_DECL
#define SEG_PARAMS
#define SEG_ARGS
#define SEG_START() do {} while (0)
#define SEG(i) do {} while (0)
#define SEG_OUT(K) do {} while (0)
template <int... Is, class F> DI void static_for_(std::integer_sequence<int, Is...>, F&& f) { (f(std::integral_constant<int, Is>{}), ...); }
template <int N, class F> DI void static_for(F&& f) { static_for_(std::make_integer_sequence<int, N>{}, f); }
DI unsigned cvtpk(float lo, float hi) {
    typedef float f32x2 __attribute__((ext_vector_type(2)));
    typedef __bf16 bf16x2 __attribute__((ext_vector_type(2)));
    f32x2 v = {lo, hi}; bf16x2 b = __builtin_convertvector(v, bf16x2); return __builtin_bit_cast(unsigned, b);
}
DI float bf2f(u16 v) { return __uint_as_float((unsigned)v << 16); }
DI float bfround(float x) { return __uint_as_float(cvtpk(x, x) << 16); }
DI bf16x8 pack8(const f32x16& x, int s, float sc) {
    u32x4 w = {cvtpk(x[8 * s] * sc, x[8 * s + 1] * sc), cvtpk(x[8 * s + 2] * sc, x[8 * s + 3] * sc),
               cvtpk(x[8 * s + 4] * sc, x[8 * s + 5] * sc), cvtpk(x[8 * s + 6] * sc, x[8 * s + 7] * sc)};
    return __builtin_bit_cast(bf16x8, w);
}
DI bf16x8 pack8(const f32x16& x, int s) {
    u32x4 w = {cvtpk(x[8 * s], x[8 * s + 1]), cvtpk(x[8 * s + 2], x[8 * s + 3]),
               cvtpk(x[8 * s + 4], x[8 * s + 5]), cvtpk(x[8 * s + 6], x[8 * s + 7])};
    return __builtin_bit_cast(bf16x8, w);
}
DI void glds16(const void* sbase, unsigned voff, unsigned lds_addr) {
    lds_addr = __builtin_amdgcn_readfirstlane(lds_addr);
    asm volatile("s_mov_b32 m0, %2\n\ts_nop 0\n\tglobal_load_lds_dwordx4 %0, %1" :: "v"(voff), "s"(sbase), "s"(lds_addr) : "memory");
}
DI void glds16x4(const void* sbase, unsigned voff, unsigned lds_addr) {
    lds_addr = __builtin_amdgcn_readfirstlane(lds_addr);
    asm volatile("s_mov_b32 m0, %2\n\ts_nop 0\n\tglobal_load_lds_dwordx4 %0, %1\n\tglobal_load_lds_dwordx4 %0, %1 offset:1024\n\t"
                 "global_load_lds_dwordx4 %0, %1 offset:2048\n\tglobal_load_lds_dwordx4 %0, %1 offset:3072"
                 :: "v"(voff), "s"(sbase), "s"(lds_addr) : "memory");
}
DI unsigned flag_load(const unsigned* p) { return __hip_atomic_load(p, __ATOMIC_RELAXED, __HIP_MEMORY_SCOPE_AGENT); }
DI void flag_store(unsigned* p, unsigned v) { __hip_atomic_store(p, v, __ATOMIC_RELAXED, __HIP_MEMORY_SCOPE_AGENT); }
DI void store_f32_wt(float* p, float v) { __hip_atomic_store((unsigned*)p, __float_as_uint(v), __ATOMIC_RELAXED, __HIP_MEMORY_SCOPE_AGENT); }
template <int NW> DI void wait_flags(const unsigned* f, int n, unsigned tag, int lane) {
    const unsigned long long t0 = __builtin_amdgcn_s_memrealtime();
    for (;;) {
        bool ok = true;
#pragma unroll
        for (int j = 0; j < NW; ++j) { const int i = lane + 64 * j; ok &= (i < n ? flag_load(f + i) : tag) == tag; }
        if (__all(ok)) break;
        if (__builtin_amdgcn_s_memrealtime() - t0 > 5000000ull) break;
        __builtin_amdgcn_s_sleep(2);
    }
}
DI void acquire_agent() { __builtin_amdgcn_fence(__ATOMIC_ACQUIRE, "agent"); }
#define WAIT_BAR(N) asm volatile("s_waitcnt vmcnt(" #N ") lgkmcnt(0)\n\ts_barrier" ::: "memory")
#define WAIT_CNT(c) do { if ((c) == 8) { WAIT_BAR(8); } else if ((c) == 4) { WAIT_BAR(4); } else { WAIT_BAR(0); } } while (0)
DI int lane_now() { int l; asm volatile("v_mbcnt_lo_u32_b32 %0, -1, 0\n\tv_mbcnt_hi_u32_b32 %0, -1, %0" : "=v"(l)); return l; }
typedef LAS const char* lptr;
DI bf16x8 ldsr128(lptr p) { return *(const LAS bf16x8*)p; }
DI bf16x8 ldsr128a(lptr p) { return *(const LAS bf16x8*)p; }
DI s16x4 ldstr(lptr p) { return __builtin_bit_cast(s16x4, __builtin_amdgcn_ds_read_tr16_b64_v4i16((LAS v4i16_t*)p)); }
DI bf16x8 cat(s16x4 a, s16x4 b) { return __builtin_shufflevector(a, b, 0, 1, 2, 3, 4, 5, 6, 7); }
DI unsigned shx(unsigned v, int m, int lane) { return (unsigned)__builtin_amdgcn_ds_bpermute((lane ^ m) << 2, (int)v); }
DI float shx(float v, int m, int lane) { return __uint_as_float(shx(__float_as_uint(v), m, lane)); }
DI float wave_sum(float v, int lane) {
#pragma unroll
    for (int o = 32; o >= 1; o >>= 1) v += shx(v, o, lane);
    return v;
}
DI float sigmoidf_(float x) { return 1.0f / (1.0f + __expf(-x)); }

constexpr int K1_STAGE = 49152, K1_LDS = 163840;
typedef __amdgpu_buffer_rsrc_t rsrc_t;
DI rsrc_t make_rsrc(const void* p, unsigned bytes) { return __builtin_amdgcn_make_buffer_rsrc((void*)p, (short)0, (int)bytes, 0x00020000); }
DI void store_wt(rsrc_t r, unsigned off, bf16x8 v) {
    __builtin_amdgcn_raw_buffer_store_b128(__builtin_bit_cast(u32x4, v), r, (int)off, 0, 16);
}
#define WAIT_VM(N) asm volatile("s_waitcnt vmcnt(" #N ")" ::: "memory")

template <bool ISV, class Pre, class Mid, class Tail>
DI void kv_body(const char* cbase, const char* kbase, const char* vbase, rsrc_t outr, unsigned outoff, lptr lds, unsigned lds0, int wid, int lane,
                Pre&& pre, Mid&& mid, Tail&& tail) {
    const int l31 = lane & 31, hi = lane >> 5;
    unsigned voffW[2], voffC[2], voffG[2];
#pragma unroll
    for (int j = 0; j < 2; ++j) {
        const int n = 2 * wid + j;
        { const int row = 4 * n + (lane >> 4); voffW[j] = row * 8192 + (((lane & 15) ^ ((row & 3) << 2)) << 4); }
        { const int row = 8 * n + (lane >> 3); voffC[j] = row * 1024 + (((lane & 7) ^ ((row >> 1) & 7)) << 4); }
        voffG[j] = (lane ^ ((wid + 8 * j) & 15)) << 4;
    }
    auto stage = [&](int s) {
        const unsigned b = lds0 + (s % 3) * K1_STAGE + 2 * wid * 1024;
#pragma unroll
        for (int j = 0; j < 2; ++j) glds16(kbase + (size_t)s * 64 * 8192, voffW[j], b + j * 1024);
#pragma unroll
        for (int j = 0; j < 2; ++j) glds16(vbase + (size_t)s * 64 * 8192, voffW[j], b + 16384 + j * 1024);
#pragma unroll
        for (int j = 0; j < 2; ++j) glds16(cbase + s * 128, voffC[j], b + 32768 + j * 1024);
    };
    auto gbuf = [&](int k) { return k == 4 ? 98304 : k == 5 ? 131072 : k == 6 ? 0 : 49152; };
    auto stageG = [&](int k) {
#pragma unroll
        for (int j = 0; j < 4; ++j) glds16(cbase + (size_t)(32 * k + wid + 8 * j) * 1024, voffG[j & 1], lds0 + gbuf(k) + (wid + 8 * j) * 1024);
    };
    const int tq = (lane & 15) >> 2, tp = lane & 3, tblk = (lane >> 4) & 1;
    const lptr wA = lds + (ISV ? 16384 : 0) + (8 * hi + tq) * 256 + (((wid & 3) ^ tq) << 6) + tblk * 32 + tp * 8;
    lptr cA[4];
#pragma unroll
    for (int ks = 0; ks < 4; ++ks) cA[ks] = lds + 32768 + l31 * 128 + (((2 * ks + hi) ^ ((l31 >> 1) & 7)) << 4);
    auto mm = [&](const bf16x8& w, const bf16x8& c, const f32x16& acc) { return ISV ? MFMA(c, w, acc) : MFMA(w, c, acc); };
    auto out_tile = [&](int kg, const f32x16& acc) {
        store_wt(outr, outoff + kg * 8192, pack8(acc, 0));
        store_wt(outr, outoff + kg * 8192 + 1024, pack8(acc, 1));
    };

    bf16x8 wf[32];
    f32x16 acc[4] = {};
    lptr gA[8];
    stage(0); stage(1); stage(2);
    pre();
    auto rdW = [&](int s, int ks) { const int bo = (s % 3) * K1_STAGE; wf[4 * s + ks] = cat(ldstr(wA + bo + ks * 4096), ldstr(wA + bo + ks * 4096 + 1024)); };
    auto rdC = [&](int s, int i) { return ldsr128(cA[i >> 2] + (s % 3) * K1_STAGE + (i & 3) * 4096); };
    auto rdG = [&](int k, int ks) { return ldsr128(gA[ks & 7] + gbuf(k) + (ks >> 3) * 256); };
    auto dmaS = [&](int s, int j) {
        const unsigned b = lds0 + (s % 3) * K1_STAGE + 2 * wid * 1024;
        if (j < 2) glds16(kbase + (size_t)s * 64 * 8192, voffW[j], b + j * 1024);
        else if (j < 4) glds16(vbase + (size_t)s * 64 * 8192, voffW[j - 2], b + 16384 + (j - 2) * 1024);
        else glds16(cbase + s * 128, voffC[j - 4], b + 32768 + (j - 4) * 1024);
    };
    auto dmaG = [&](int k, int j) { glds16(cbase + (size_t)(32 * k + wid + 8 * j) * 1024, voffG[j & 1], lds0 + gbuf(k) + (wid + 8 * j) * 1024); };
    bf16x8 cf[8];
    WAIT_BAR(12);
    rdW(0, 0);
#pragma unroll
    for (int i = 0; i < 4; ++i) cf[i] = rdC(0, i);
    static_for<128>([&](auto SI) {
        {
            constexpr int s = SI / 16, i = SI % 16;
            if (i == 12) {
                if (s < 6) { WAIT_BAR(6); } else if (s == 6) { WAIT_BAR(8); } else { WAIT_BAR(8); }
            }
            if (s == 7 && i == 11) {
                const int ln = lane_now();
#pragma unroll
                for (int j = 0; j < 8; ++j) gA[j] = lds + (ln & 31) * 1024 + (((2 * j + (ln >> 5)) ^ (ln & 15)) << 4);
            }
            if (s == 1 && i == 13) mid();
            const int nx = i + 4;
            if (nx < 16) { if ((nx & 3) == 0) rdW(s, nx >> 2); cf[nx & 7] = rdC(s, nx); }
            else if (s < 7) { if (nx == 16) rdW(s + 1, 0); cf[nx & 7] = rdC(s + 1, nx - 16); }
            else cf[nx & 7] = rdG(4, nx - 16);
            acc[i & 3] = mm(wf[4 * s + (i >> 2)], cf[i & 7], acc[i & 3]);
            if (i >= 12) {
                const int j = i - 12;
                if (s + 3 < 8) dmaS(s + 3, j);
                else if (s == 5) dmaG(4, j);
                else if (s == 6) dmaG(6, j);
                else dmaG(7, j);
            } else if (i < 2) {
                if (s >= 1 && s + 2 < 8) dmaS(s + 2, 4 + i);
            } else if (i < 6) {
                if (s == 6) dmaG(5, i - 2);
            }
            if (s == 7 && i == 15) out_tile(0, acc[0]);
            SBAR();
        }
    });
    f32x16 a2[2];
    static_for<128>([&](auto KI) {
        {
            constexpr int k = 4 + KI / 32, ks = KI % 32;
            if (ks == 28 && k < 7) {
                if (k == 4) { WAIT_BAR(16); } else if (k == 5) { WAIT_BAR(14); } else { WAIT_BAR(18); }
            }
            const int nx = ks + 4;
            if (nx < 32) cf[nx & 7] = rdG(k, nx); else if (k < 7) cf[nx & 7] = rdG(k + 1, nx - 32);
            if (ks == 0) a2[k & 1] = mm(wf[0], cf[0], f32x16{}); else a2[k & 1] = mm(wf[ks], cf[ks & 7], a2[k & 1]);
            if constexpr (k == 4) { if constexpr (ks < 3) out_tile(ks + 1, acc[(ks + 1) & 3]); }
            else if constexpr (ks == 3) out_tile(k - 1, a2[(k - 1) & 1]);
            tail(KI);
            SBAR();
        }
    });
    out_tile(7, a2[1]);
}

struct Params {
    const u16 *ax, *ckv, *wq, *wuk, *wuv, *wo, *wr, *wg, *wu, *wd, *gw;
    char* ws; u16* out;
};
struct Ctx { lptr lds; char* lds_; unsigned lds0; int tid, lane, wid, bid; unsigned tag; unsigned* sy; };
typedef __attribute__((address_space(4))) const Params* kparams_t;
DI Params load_params() {
    kparams_t pp = (kparams_t)__builtin_amdgcn_kernarg_segment_ptr();
    asm volatile("" : "+s"(pp));
    Params P;
    P.ax = pp->ax; P.ckv = pp->ckv; P.wq = pp->wq; P.wuk = pp->wuk; P.wuv = pp->wuv; P.wo = pp->wo; P.wr = pp->wr; P.wg = pp->wg; P.wu = pp->wu;
    P.wd = pp->wd; P.gw = pp->gw; P.ws = pp->ws; P.out = pp->out;
    return P;
}
DI Ctx make_ctx(char* lds_, char* ws, int wid, unsigned tag) {
    Ctx C;
    C.lds = (lptr)lds_; C.lds_ = lds_; C.lds0 = (unsigned)(uintptr_t)C.lds;
    int lane; asm volatile("v_mbcnt_lo_u32_b32 %0, -1, 0\n\tv_mbcnt_hi_u32_b32 %0, -1, %0" : "=v"(lane));
    asm volatile("" : "+s"(wid), "+s"(tag));
    C.lane = lane; C.wid = wid; C.tid = wid * 64 + lane; C.bid = blockIdx.x; C.tag = tag; C.sy = (unsigned*)(ws + WS_SYNC);
    return C;
}

constexpr int QST = 49152;
struct QPre { unsigned voffX[4], voffW[2]; u32x4 rv; };
DI void q_dma(const QPre& Q, const Params& P, const Ctx& C, int s, int j) {
    const int wid = C.wid, h = (C.bid & 7) * 4 + ((C.bid >> 3) & 3), bi = C.bid >> 5;
    const char* xbase = (const char*)P.ax + (size_t)(((wid >> 2) ? 15 - bi : bi) * 128 + (wid & 3) * 32) * 1024;
    const char* wbase = (const char*)P.wq + h * 256;
    const unsigned b = C.lds0 + ((s + 2) % 3) * QST;
    if (j < 2) glds16(wbase + (size_t)s * 64 * 8192, Q.voffW[j], b + 32768 + (2 * wid + j) * 1024);
    else glds16(xbase + s * 128, Q.voffX[j - 2], b + (4 * wid + j - 2) * 1024);
}
DI void phase_kv(const Params& P, const Ctx& C, QPre& Q) {
    char* ws = P.ws;
    const int lane = C.lane, wid = C.wid, bid = C.bid, h = (bid & 7) * 4 + ((bid >> 3) & 3), p = bid >> 5;
    STAMP(0, 0);
    const int tok = bid * 8 + wid;
    const bf16x8 bxv = *(const bf16x8*)(P.ax + tok * DM + lane * 8), wrv = *(const bf16x8*)(P.wr + lane * 8);
    const bf16x8 gwv = *(const bf16x8*)(P.gw + lane * 8);
    auto pre = [&]() {
        float sr = 0.f, sg = 0.f;
#pragma unroll
        for (int j = 0; j < 8; ++j) { sr += bf2f((u16)bxv[j]) * bf2f((u16)wrv[j]); sg += bf2f((u16)bxv[j]) * bf2f((u16)gwv[j]); }
        sr = wave_sum(sr, lane); sg = wave_sum(sg, lane);
        if (lane == 0) { store_f32_wt((float*)(ws + WS_R) + tok, bfround(sr)); store_f32_wt((float*)(ws + WS_G) + tok, sigmoidf_(sg)); }
        STAMP(0, 1);
    };
    auto mid = [&]() { if (C.tid == 0) flag_store(C.sy + SY_R + bid, C.tag); };
    const char* cbase = (const char*)P.ckv + (size_t)p * 256 * 1024;
    const char* kbase = (const char*)P.wuk + h * 256;
    const char* vbase = (const char*)P.wuv + h * 256;
    const unsigned outoff = (unsigned)((h * 64 + 8 * p) * 8 + 2 * (wid & 3)) * 1024 + lane * 16;
    const unsigned kvbytes = (unsigned)((size_t)SEQ * NHD * 2);
    unsigned rp[4];
    auto tail = [&](auto KI) {
        constexpr int k = 4 + KI / 32, ks = KI % 32;
        if constexpr (k == 4 && ks == 20) {
            const int ln = lane_now();
#pragma unroll
            for (int j = 0; j < 4; ++j) { const int r = 8 * j + (ln >> 3); Q.voffX[j] = r * 1024 + (((ln & 7) ^ ((r >> 1) & 7)) << 4); }
#pragma unroll
            for (int j = 0; j < 2; ++j) { const int row = 4 * (2 * wid + j) + (ln >> 4); Q.voffW[j] = row * 8192 + (((ln & 15) ^ ((row & 3) << 2)) << 4); }
            if (wid == 0) {
#pragma unroll
                for (int j = 0; j < 4; ++j) asm volatile("global_load_dword %0, %1, %2 sc1" : "=v"(rp[j]) : "v"(ln * 4), "s"(C.sy + SY_R + 64 * j));
            }
        }
        if constexpr (k == 5 && ks >= 28) q_dma(Q, P, C, 0, ks - 28);
        if constexpr (k == 6 && ks < 2) q_dma(Q, P, C, 0, 4 + ks);
        if constexpr (k == 6 && ks == 20) {
            if (wid == 0) {
                asm volatile("s_waitcnt vmcnt(10)" : "+v"(rp[0]), "+v"(rp[1]), "+v"(rp[2]), "+v"(rp[3]));
                if (__builtin_expect(!__all(rp[0] == C.tag && rp[1] == C.tag && rp[2] == C.tag && rp[3] == C.tag), 0)) wait_flags<4>(C.sy + SY_R, 256, C.tag, lane_now());
            }
        }
        if constexpr (k == 6 && ks >= 28) q_dma(Q, P, C, 1, ks - 28);
        if constexpr (k == 7 && ks < 2) q_dma(Q, P, C, 1, 4 + ks);
        if constexpr (k == 7 && ks == 5) asm volatile("global_load_dwordx4 %0, %1, %2 sc1" : "=v"(Q.rv) : "v"((wid * 64 + lane_now()) * 16), "s"(P.ws + WS_R));
    };
    if (wid < 4) kv_body<false>(cbase, kbase, vbase, make_rsrc(ws + WS_KF, kvbytes), outoff, C.lds, C.lds0, wid, lane, pre, mid, tail);
    else         kv_body<true>(cbase, kbase, vbase, make_rsrc(ws + WS_VF, kvbytes), outoff, C.lds, C.lds0, wid, lane, pre, mid, tail);
    STAMP(0, 3);
}

DI unsigned umax_(unsigned a, unsigned b) { return a > b ? a : b; }
DI unsigned umin_(unsigned a, unsigned b) { return a < b ? a : b; }
DI void ld_u16(unsigned& d, const void* sbase, unsigned voff) { asm volatile("global_load_ushort %0, %1, %2" : "=v"(d) : "v"(voff), "s"(sbase)); }
DI void ld_x4(u32x4& d, const void* sbase, unsigned voff) { asm volatile("global_load_dwordx4 %0, %1, %2" : "=v"(d) : "v"(voff), "s"(sbase)); }
DI void ld_u16_nop(unsigned& d, const void* sbase, unsigned voff) { asm volatile("s_nop 4\n\tglobal_load_ushort %0, %1, %2" : "=v"(d) : "v"(voff), "s"(sbase)); }
DI void ld_x4_nop(u32x4& d, const void* sbase, unsigned voff) { asm volatile("s_nop 4\n\tglobal_load_dwordx4 %0, %1, %2" : "=v"(d) : "v"(voff), "s"(sbase)); }
DI float lo16(unsigned v) { return __uint_as_float(v << 16); }
DI float hi16(unsigned v) { return __uint_as_float(v & 0xFFFF0000u); }

constexpr int K2_QST = 49152, K2_SLOT = 32768, K2_QB = 131072, K2_LDS = 163840;
constexpr float QSCALE = 0.08838834764831845f * 1.4426950408889634f;
constexpr float THR = 8.0f;
constexpr int RING0 = 3;

#define SETPRIO(x) __builtin_amdgcn_s_setprio(x)
struct SMState { float mhat, l; f32x16 negm; };
#define PIN(x) asm volatile("" : "+v"(x))
DI float max3(float a, float b, float c) { return fmaxf(fmaxf(a, b), c); }

#define EXP2(x) __builtin_amdgcn_exp2f(x)
#define VOFF(i) (((i) >> 3) * 8192 + (2 * ((i) & 3) + (((i) >> 2) & 1)) * 1024)
#define KOFF(i) (((i) & 1) * 8192 + ((i) >> 1) * 1024)
DI float fadd_s(float a, float b) { float r; asm("v_add_f32_e32 %0, %1, %2" : "=v"(r) : "v"(a), "v"(b)); return r; }
DI float max3_s(float a, float b, float c) { float r; asm("v_max3_f32 %0, %1, %2, %3" : "=v"(r) : "v"(a), "v"(b), "v"(c)); return r; }
DI float max2_s(float a, float b) { float r; asm("v_max_f32_e32 %0, %1, %2" : "=v"(r) : "v"(a), "v"(b)); return r; }
template <int OFF> DI void glds_step(const void* sbase, unsigned voff, unsigned lds_addr) {
    if (OFF == 0) asm volatile("s_mov_b32 m0, %2\n\ts_nop 0\n\tglobal_load_lds_dwordx4 %0, %1" :: "v"(voff), "s"(sbase), "s"(lds_addr));
    else asm volatile("global_load_lds_dwordx4 %0, %1 offset:%c2" :: "v"(voff), "s"(sbase), "i"(OFF));
}
DI f32x16 mfma_c(bf16x8 a, bf16x8 b, const f32x16& c) {
    f32x16 d; asm volatile("v_mfma_f32_32x32x16_bf16 %0, %1, %2, %3" : "=&v"(d) : "v"(a), "v"(b), "v"(c)); return d;
}
struct DmaJob { const char* src0; const char* src1; unsigned dst0, dst1; bool has0, has1; };

#define LA 4
template <bool QK, bool FIN, class Mid>
DI void attn_A(lptr kp, const lptr& vpn, const bf16x8 (&qf)[8], const f32x16& negm, const bf16x8 (&kpre)[LA], bf16x8 (&vpre)[LA], f32x16& s0, f32x16& s1,
               const f32x16& p0, const f32x16& p1, bf16x8 (&pf)[4], float& l, const DmaJob& dj, unsigned vlin, Mid&& mid) {
    bf16x8 fr[16];
    unsigned pw[16];
    float sa = 0.f, sb = 0.f;
#pragma unroll
    for (int i = 0; i < LA; ++i) fr[i] = kpre[i];
#pragma unroll
    for (int i = 0; i < 16; ++i) {
        if (QK && i + LA < 16) fr[i + LA] = ldsr128a(kp + KOFF(i + LA));
        if (i + LA >= 16) vpre[i + LA - 16] = ldsr128a(vpn + VOFF(i + LA - 16));
        if (QK) {
            if (i == 0) s0 = mfma_c(fr[0], qf[0], negm);
            else if (i == 1) s1 = mfma_c(fr[1], qf[0], negm);
            else if (i & 1) s1 = MFMA(fr[i], qf[i >> 1], s1);
            else s0 = MFMA(fr[i], qf[i >> 1], s0);
        }
        if (FIN) {
            const float e0 = i < 8 ? p0[2 * i] : p1[2 * i - 16], e1 = i < 8 ? p0[2 * i + 1] : p1[2 * i - 15];
            if (i == 0) { sa = e0; sb = e1; } else { sa = fadd_s(sa, e0); sb = fadd_s(sb, e1); PIN(sa); PIN(sb); }
            pw[i] = cvtpk(e0, e1);
            PIN(pw[i]);
        }
        if (i == 3) mid();
        if (QK) {
            if (i == 1 && dj.has0) { glds_step<0>(dj.src0, vlin, dj.dst0); glds_step<1024>(dj.src0, vlin, dj.dst0); }
            if (i == 5 && dj.has0) { glds_step<2048>(dj.src0, vlin, dj.dst0); glds_step<3072>(dj.src0, vlin, dj.dst0); }
            if (i == 9 && dj.has1) { glds_step<0>(dj.src1, vlin, dj.dst1); glds_step<1024>(dj.src1, vlin, dj.dst1); }
            if (i == 13 && dj.has1) { glds_step<2048>(dj.src1, vlin, dj.dst1); glds_step<3072>(dj.src1, vlin, dj.dst1); }
        }
        SBAR();
    }
    if (FIN) {
        l += sa + sb;
#pragma unroll
        for (int i = 0; i < 4; ++i) { u32x4 w = {pw[4 * i], pw[4 * i + 1], pw[4 * i + 2], pw[4 * i + 3]}; pf[i] = __builtin_bit_cast(bf16x8, w); }
    }
}
DI void dma_plain(const DmaJob& dj, unsigned vlin) {
    if (dj.has0) glds16x4(dj.src0, vlin, dj.dst0);
    if (dj.has1) glds16x4(dj.src1, vlin, dj.dst1);
}
template <bool PV, bool SM, class Mid>
DI void attn_B(lptr vp, const lptr& kpn, f32x16 (&o)[4], const bf16x8 (&pf)[4], const bf16x8 (&vpre)[LA], bf16x8 (&kpre)[LA],
               f32x16& s0, f32x16& s1, SMState& st, bool first, bool mask, int qrel  , bool& pend, float& fpend, Mid&& mid) {
    bf16x8 fr[16];
    float ma = 0.f, mb = 0.f, mc = 0.f;
#pragma unroll
    for (int i = 0; i < LA; ++i) fr[i] = vpre[i];
    if (SM && __builtin_expect(mask, 0)) {
        const float NEG = -__builtin_inff();
#pragma unroll
        for (int r = 0; r < 16; ++r) { const int c = (r & 3) + 8 * (r >> 2);
            if (c > qrel) s0[r] = NEG;
            if (c + 32 > qrel) s1[r] = NEG; }
    }
    SETPRIO(1);
    static_for<16>([&](auto I) {
        constexpr int i = I;
        if (PV && i + LA < 16) fr[i + LA] = ldsr128a(vp + VOFF(i + LA));
        if (i + LA >= 16) kpre[i + LA - 16] = ldsr128a(kpn + KOFF(i + LA - 16));
        if (PV) o[i & 3] = MFMA(fr[i], pf[i >> 2], o[i & 3]);
        if (SM) {
            if (i == 0) { ma = max3(s0[0], s0[1], s0[2]); mb = max3(s1[0], s1[1], s1[2]); mc = max3(s0[3], s0[4], s1[3]); PIN(ma); PIN(mb); PIN(mc); }
            if (i == 1) { ma = max3_s(ma, s0[5], s0[6]); mb = max3_s(mb, s1[4], s1[5]); mc = max3_s(mc, s0[7], s1[6]); PIN(ma); PIN(mb); PIN(mc); }
            if (i == 2) { ma = max3_s(ma, s0[8], s0[9]); mb = max3_s(mb, s1[7], s1[8]); mc = max3_s(mc, s0[10], s1[9]); PIN(ma); PIN(mb); PIN(mc); }
            if (i == 3) { ma = max3_s(ma, s0[11], s0[12]); mb = max3_s(mb, s1[10], s1[11]); mc = max3_s(mc, s0[13], s1[12]); PIN(ma); PIN(mb); PIN(mc); }
            if (i == 4) { ma = max3_s(ma, s0[14], s0[15]); mb = max3_s(mb, s1[13], s1[14]); mc = max2_s(mc, s1[15]); PIN(ma); PIN(mb); PIN(mc); }
            if (i == 5) {
                float rm = max3_s(ma, mb, mc);
                auto rr = __builtin_amdgcn_permlane32_swap(__float_as_uint(rm), __float_as_uint(rm), false, false);
                rm = max2_s(__uint_as_float(rr[0]), __uint_as_float(rr[1]));
                if (__builtin_expect(first || __any(rm > THR), 0)) {
                    const float dl = first ? rm : fmaxf(rm, 0.f), f = first ? 1.0f : __builtin_amdgcn_exp2f(-dl);
                    st.mhat += dl; st.l *= f;
#pragma unroll
                    for (int r = 0; r < 16; ++r) { s0[r] -= dl; s1[r] -= dl; st.negm[r] = -st.mhat; }
                    pend = !first; fpend = f;
                }
            }
            if (i >= 6) {
                constexpr int e0 = i < 14 ? 3 * (i - 6) : 24 + 4 * (i - 14), e1 = i < 14 ? e0 + 3 : e0 + 4;
#pragma unroll
                for (int e = e0; e < e1; ++e) { if (e < 16) { s0[e] = EXP2(s0[e]); } else { s1[e - 16] = EXP2(s1[e - 16]); } }
                PIN(s0); PIN(s1);
            }
        }
        if (i == 7) mid();
        SBAR();
    });
    SETPRIO(0);
}

struct Routed { int sel0, sel1; float sig0, sig1; };
DI void phase_attn(const Params& P, const Ctx& C, QPre& Q, Routed& R) {
    char* ws = P.ws; char* lds_ = C.lds_;
    const u16 *x = P.ax, *wq = P.wq, *wo = P.wo, *bx = P.ax, *wg = P.wg, *wu = P.wu, *wd = P.wd;
    const lptr lds = C.lds;
    const unsigned lds0 = C.lds0;
    const int tid = C.tid, lane = C.lane, l31 = lane & 31, hi = lane >> 5, wid = C.wid;
    const int bid = C.bid, h = (bid & 7) * 4 + ((bid >> 3) & 3), bi = bid >> 5;
    const int grp = wid >> 2, rg = wid & 3;
    const int blkA = bi, blkB = 15 - bi;

    STAMP(1, 0);
    LAS unsigned* sk = (LAS unsigned*)((LAS char*)lds_ + 147456);
    LAS float* hs = (LAS float*)((LAS char*)lds_ + 147456 + 128);
    LAS float* red = (LAS float*)((LAS char*)lds_ + 147456 + 256);
    constexpr int MOE_WD = 147456 + 2048;
    const int cg = (bid & 7) * 28 + ((bid >> 3) < 28 ? (bid >> 3) : 27);
    const int f0 = cg * MOE_COLS;
    u32x4 wgv, wuv; u32x4& rv = Q.rv;
    unsigned xs0, xs1;
    int sel0 = 0, sel1 = 0;
    auto moe_step = [&](int step) {
        if (step == 0) {
            asm volatile("" : "+v"(rv));
            unsigned k1 = 0, k2 = 0;
#pragma unroll
            for (int j = 0; j < 4; ++j) {
                const unsigned u = rv[j] >> 16;
                const unsigned o = (u & 0x8000u) ? (~u & 0xFFFFu) : (u | 0x8000u);
                const unsigned key = (o << 16) | (unsigned)(0xFFFF - (tid * 4 + j));
                k2 = umax_(k2, umin_(k1, key)); k1 = umax_(k1, key);
            }
#pragma unroll
            for (int o = 32; o >= 1; o >>= 1) {
                const unsigned b1 = shx(k1, o, lane), b2 = shx(k2, o, lane);
                k2 = umax_(umin_(k1, b1), umax_(k2, b2)); k1 = umax_(k1, b1);
            }
            if (lane == 0) { sk[wid * 2] = k1; sk[wid * 2 + 1] = k2; }
        } else if (step == 1) {
            unsigned k1 = 0, k2 = 0;
#pragma unroll
            for (int j = 0; j < 16; ++j) { const unsigned key = sk[j]; k2 = umax_(k2, umin_(k1, key)); k1 = umax_(k1, key); }
            k1 = __builtin_amdgcn_readfirstlane(k1); k2 = __builtin_amdgcn_readfirstlane(k2);
            sel0 = 0xFFFF - (int)(k1 & 0xFFFFu); sel1 = 0xFFFF - (int)(k2 & 0xFFFFu);
            ld_u16_nop(xs0, (const char*)bx + sel0 * 1024, tid * 2);
            ld_u16_nop(xs1, (const char*)bx + sel1 * 1024, tid * 2);
            {
                const unsigned o0 = k1 >> 16, o1 = k2 >> 16;
                const float r0 = __uint_as_float(((o0 & 0x8000u) ? (o0 & 0x7FFFu) : (~o0 & 0xFFFFu)) << 16);
                const float r1 = __uint_as_float(((o1 & 0x8000u) ? (o1 & 0x7FFFu) : (~o1 & 0xFFFFu)) << 16);
                R.sel0 = sel0; R.sel1 = sel1;
                R.sig0 = __uint_as_float(__builtin_amdgcn_readfirstlane(__float_as_uint(sigmoidf_(r0))));
                R.sig1 = __uint_as_float(__builtin_amdgcn_readfirstlane(__float_as_uint(sigmoidf_(r1))));
            }
        } else if (step == 2) {
            asm volatile("" : "+v"(xs0), "+v"(xs1), "+v"(wgv), "+v"(wuv));
            const float a0 = lo16(xs0), a1 = lo16(xs1);
            float v[32];
#pragma unroll
            for (int c = 0; c < 8; ++c) {
                const float g = (c & 1) ? hi16(wgv[c >> 1]) : lo16(wgv[c >> 1]), u = (c & 1) ? hi16(wuv[c >> 1]) : lo16(wuv[c >> 1]);
                v[c] = a0 * g; v[8 + c] = a0 * u; v[16 + c] = a1 * g; v[24 + c] = a1 * u;
            }
#pragma unroll
            for (int i = 0; i < 16; ++i) { auto r = __builtin_amdgcn_permlane32_swap(__float_as_uint(v[i]), __float_as_uint(v[i + 16]), false, false);
                v[i] = __uint_as_float(r[0]) + __uint_as_float(r[1]); }
#pragma unroll
            for (int i = 0; i < 8; ++i) { auto r = __builtin_amdgcn_permlane16_swap(__float_as_uint(v[i]), __float_as_uint(v[i + 8]), false, false);
                v[i] = __uint_as_float(r[0]) + __uint_as_float(r[1]); }
#pragma unroll
            for (int hw = 4; hw >= 1; hw >>= 1) {
                const bool up_ = lane & (2 * hw);
#pragma unroll
                for (int i = 0; i < hw; ++i) { const float send = up_ ? v[i] : v[i + hw], keep = up_ ? v[i + hw] : v[i]; v[i] = keep + shx(send, 2 * hw, lane); }
            }
            v[0] += shx(v[0], 1, lane);
            if (!(lane & 1)) red[wid * 32 + (lane >> 1)] = v[0];
        } else if (step == 3) {
            if (tid < 16) {
                const int c = tid & 7, t = tid >> 3;
                float g = 0.f, u = 0.f;
#pragma unroll
                for (int w = 0; w < 8; ++w) { g += red[w * 32 + 16 * t + c]; u += red[w * 32 + 16 * t + 8 + c]; }
                g = bfround(g); u = bfround(u);
                hs[t * 8 + c] = bfround(bfround(g * sigmoidf_(g)) * u);
            }
        } else {
            float y0 = 0.f, y1 = 0.f;
#pragma unroll
            for (int j = 0; j < 8; ++j) { const float w = bf2f(*(const LAS u16*)((LAS char*)lds_ + MOE_WD + j * 1024 + tid * 2)); y0 += hs[j] * w; y1 += hs[8 + j] * w; }
            float* yp = (float*)(ws + WS_YP) + (size_t)cg * 1024;
            store_f32_wt(yp + tid, y0); store_f32_wt(yp + 512 + tid, y1);
        }
    };

    const char* Kh = ws + WS_KF + (size_t)h * 64 * 8192;
    const unsigned vlin = lane * 16;
    bf16x8 qf[8];
    {
        lptr xA[4];
#pragma unroll
        for (int ks = 0; ks < 4; ++ks) xA[ks] = lds + (32 * wid + l31) * 128 + (((2 * ks + hi) ^ ((l31 >> 1) & 7)) << 4);
        const int tq = (lane & 15) >> 2, tp = lane & 3, tblk = (lane >> 4) & 1;
        lptr wA[4];
#pragma unroll
        for (int c = 0; c < 4; ++c) wA[c] = lds + 32768 + (8 * hi + tq) * 256 + ((c ^ tq) << 6) + tblk * 32 + tp * 8;
        auto dmaS = [&](int s, int j) { q_dma(Q, P, C, s, j); };
        auto rdW = [&](int s, int i) { const int o = ((s + 2) % 3) * QST + (i >> 2) * 4096; return cat(ldstr(wA[i & 3] + o), ldstr(wA[i & 3] + o + 1024)); };
        auto rdX = [&](int s, int ks) { return ldsr128(xA[ks] + ((s + 2) % 3) * QST); };
        f32x16 acc[4] = {};
        bf16x8 wfr[8], xfr[2];
        unsigned kvf = 0;
        WAIT_BAR(13);
        STAMP(1, 1);
        xfr[0] = rdX(0, 0);
#pragma unroll
        for (int i = 0; i < 4; ++i) wfr[i] = rdW(0, i);
        static_for<128>([&](auto SI) {
            {
                constexpr int s = SI / 16, i = SI % 16;
                if (i == 12 && s < 7) {
                    if (s == 0) { WAIT_BAR(11); } else if (s == 1) { WAIT_BAR(9); } else if (s == 2) { WAIT_BAR(8); } else if (s < 6) { WAIT_BAR(6); } else { WAIT_BAR(0); }
                }
                const int nx = i + 4;
                if (nx < 16) { if ((nx & 3) == 0) xfr[(nx >> 2) & 1] = rdX(s, nx >> 2); wfr[nx & 7] = rdW(s, nx); }
                else if (s < 7) { if (nx == 16) xfr[0] = rdX(s + 1, 0); wfr[nx & 7] = rdW(s + 1, nx - 16); }
                acc[i & 3] = MFMA(wfr[i & 7], xfr[(i >> 2) & 1], acc[i & 3]);
                if (i >= 12) { if (s + 3 < 8) dmaS(s + 3, i - 12); }
                else if (s == 0) {
                    if (i < 6) dmaS(2, i);
                    else if (i == 6) ld_x4(wgv, (const char*)wg + f0 * 2, tid * (FF * 2));
                    else if (i == 7) ld_x4(wuv, (const char*)wu + f0 * 2, tid * (FF * 2));
                    else if (i == 8) glds16((const char*)wd + (size_t)(f0 + wid) * 1024, vlin, lds0 + MOE_WD + wid * 1024);
                }
                else if (i < 2) { if (s + 2 < 8) dmaS(s + 2, 4 + i); }
                if (s == 6 && grp == 0) {
                    if (i == 12) glds16x4(Kh + rg * 4096, vlin, lds0 + RING0 * 16384 + rg * 4096);
                    if (i == 14) glds16x4(Kh + 16384 + rg * 4096, vlin, lds0 + (RING0 + 2) * 16384 + rg * 4096);
                }
                if (i == 13 && (s == 0 || s == 1 || (s >= 3 && s <= 5))) moe_step(s < 2 ? s : s - 1);
                if (s == 1 && i == 13 && tid == 0) flag_store(C.sy + SY_KV + h * 8 + bi, C.tag);
                if (s == 4 && i == 3 && wid == 0)
                    asm volatile("global_load_dword %0, %1, %2 sc1" : "=v"(kvf) : "v"((lane & 7) * 4), "s"(C.sy + SY_KV + h * 8));
                if (s == 5 && i == 13 && wid == 0) {
                    asm volatile("" : "+v"(kvf));
                    if (__builtin_expect(!__all(kvf == C.tag), 0)) wait_flags<1>(C.sy + SY_KV + h * 8, 8, C.tag, lane);
                    acquire_agent();
                }
                if (s == 6 && i == 13 && tid == 0) flag_store(C.sy + SY_Y + bid, C.tag);
                SBAR();
            }
        });
#pragma unroll
        for (int c = 0; c < 4; ++c) { qf[2 * c] = pack8(acc[c], 0, QSCALE); qf[2 * c + 1] = pack8(acc[c], 1, QSCALE); }
    }

    STAMP(1, 2);
    const int n1 = 2 * bi + 2;
    auto tile_of = [&](int n, int g) { return n < n1 ? n : n1 + 2 * (n - n1) + g; };
    auto jstar = [&](int tn) { return tn <= 2 * n1 ? tn : tn <= 2 * n1 + 2 ? tn + 1 : 2 * tn - 2 * n1 - 2; };
    auto jg1 = [&](int tn) { return tn == 2 * n1 + 1 ? tn : 2 * tn - 2 * n1 - 1; };
    int tb_d0, tb_d1, tb_kv, tb_rel;
    auto make_job = [&](int tn, int& d0, int& d1) {
        const int n = tn >> 1;
        int a_off, b_off; bool a_ok, b_ok;
        const int kb = (int)WS_KF + h * 64 * 8192 + rg * 4096, vb = (int)WS_VF + h * 64 * 8192 + rg * 4096;
        if (tn & 1) { a_ok = n >= 1 && n <= 17; a_off = vb + tile_of(n - 1, 0) * 16384; b_ok = n >= n1 && n <= 16; b_off = kb + tile_of(n, 1) * 16384; }
        else        { a_ok = n <= 16;           a_off = kb + tile_of(n, 0) * 16384;     b_ok = n - 2 >= n1 && n <= 18; b_off = vb + tile_of(n - 2, 1) * 16384; }
        const int da = a_off | ((jstar(tn) + RING0) & 7) | 8, db = b_off | ((jg1(tn) + RING0) & 7) | 8;
        d0 = a_ok ? da : b_ok ? db : 0; d1 = (a_ok && b_ok) ? db : 0;
    };
    {
        const int n = lane < 19 ? lane : 18;
        make_job(2 * n + grp + 3, tb_d0, tb_d1);
        const int ks = ((n < n1 || grp == 0 ? jstar(2 * n) : jg1(2 * n + 1)) + RING0) & 7;
        const int vs = ((n < n1 || grp == 0 ? jstar(2 * n + 3) : jg1(2 * n + 4)) + RING0) & 7;
        tb_kv = ks | vs << 8;
        tb_rel = 128 * ((grp || n >= n1) ? blkB : blkA) + 32 * rg - 64 * tile_of(n, grp);
    }
    auto job_from = [&](int d0, int d1) -> DmaJob {
        DmaJob d;
        d.has0 = d0 & 8; d.has1 = d1 & 8;
        d.src0 = ws + (unsigned)(d0 & ~4095); d.src1 = ws + (unsigned)(d1 & ~4095);
        d.dst0 = lds0 + (d0 & 7) * 16384 + rg * 4096; d.dst1 = lds0 + (d1 & 7) * 16384 + rg * 4096;
        return d;
    };
    auto job = [&](int n) { return job_from(__builtin_amdgcn_readlane(tb_d0, n), __builtin_amdgcn_readlane(tb_d1, n)); };
    if (grp == 1) {
#pragma unroll
        for (int c = 0; c < 8; ++c) *(LAS bf16x8*)((LAS char*)lds_ + K2_QB + rg * 8192 + c * 1024 + lane * 16) = qf[c];
    }
    f32x16 o[4] = {};
    SMState st; st.mhat = 0.f; st.l = 0.f; st.negm = f32x16{};
    const lptr kp0 = lds + lane * 16;
    char* ctx = ws + WS_CTX;
    const rsrc_t ctxr = make_rsrc(ctx, (unsigned)((size_t)SEQ * NHD * 2));
    auto store_ctx = [&](int blk, int d, const f32x16& v, float inv) {
        const unsigned off = (unsigned)(((4 * blk + rg) * 32 + h) * 8 + 2 * d) * 1024 + lane * 16;
        store_wt(ctxr, off, pack8(v, 0, inv));
        store_wt(ctxr, off + 1024, pack8(v, 1, inv));
    };
    auto half_sum = [&](float v) { auto rr = __builtin_amdgcn_permlane32_swap(__float_as_uint(v), __float_as_uint(v), false, false);
                                   return __uint_as_float(rr[0]) + __uint_as_float(rr[1]); };
    auto kslot = [&](int n) { return kp0 + (__builtin_amdgcn_readlane(tb_kv, n) & 7) * 16384; };
    auto vslot = [&](int n) { return kp0 + ((__builtin_amdgcn_readlane(tb_kv, n) >> 8) & 7) * 16384; };
    WAIT_BAR(0);
    bool first = true, pend = false;
    float invA = 0.f, fpend = 1.f;
    f32x16 sA0, sA1, sB0, sB1; bf16x8 pf[4], vpre[LA], kpre[LA];
#pragma unroll
    for (int i = 0; i < LA; ++i) kpre[i] = ldsr128a(kslot(0) + KOFF(i));
    if (grp == 1) { WAIT_BAR(0); }
    SEG_DECL; SEG_START();
    const int nsw = grp == 0 ? n1 - 1 : 100;
    auto headB = [&](int n) {
        if (__builtin_expect(n == nsw + 1, 0)) { invA = 1.0f / half_sum(st.l); st.mhat = 0.f; st.l = 0.f; first = true; }
    };
    auto tailB = [&](int n) {
        first = false;
        if (__builtin_expect(pend || (unsigned)(n - nsw) < 3u, 0)) {
            if (n == nsw + 2 && tid == 0) flag_store(C.sy + SY_CTX + blkA * 32 + h, C.tag);
            if (pend) {
#pragma unroll
                for (int d = 0; d < 4; ++d)
#pragma unroll
                    for (int r = 0; r < 16; ++r) o[d][r] *= fpend;
                pend = false;
            }
            if (n == nsw + 1) {
#pragma unroll
                for (int d = 0; d < 4; ++d) store_ctx(blkA, d, o[d], invA);
#pragma unroll
                for (int d = 0; d < 4; ++d) o[d] = f32x16{};
            }
            if (n == nsw) {
#pragma unroll
                for (int c = 0; c < 8; ++c) qf[c] = ldsr128(lds + K2_QB + rg * 8192 + c * 1024 + lane * 16);
                st.negm = f32x16{};
            }
        }
    };
#define BARRIER_L() asm volatile("s_waitcnt lgkmcnt(0)\n\ts_barrier" ::: "memory")
    lptr kcur = kslot(0), vcur = kp0;
    DmaJob dj = job(0);
    int rel = 0;
    auto pinjob = [&]() { asm volatile("" : "+s"(dj.src0), "+s"(dj.src1), "+s"(dj.dst0), "+s"(dj.dst1)); };
    auto midA = [&](int n) { vcur = vslot(n > 0 ? n - 1 : 0); rel = __builtin_amdgcn_readlane(tb_rel, n); asm volatile("" : "+v"(vcur), "+s"(rel)); };
    auto midB = [&](int n) { kcur = kslot(n + 1); dj = job(n + 1); asm volatile("" : "+v"(kcur)); pinjob(); };
#define RELARGS first, rel < 63, rel + l31 - 4 * hi, pend, fpend
    attn_A<true, false>(kcur, vcur, qf, st.negm, kpre, vpre, sA0, sA1, sA0, sA1, pf, st.l, dj, vlin, [&]() { midA(0); });
    BARRIER_L();
    attn_B<false, true>(vcur, kcur, o, pf, vpre, kpre, sA0, sA1, st, RELARGS, [&]() { midB(0); });
    tailB(0);
    WAIT_BAR(0);
#define STEP(n, C0, C1, P0, P1) do { \
        attn_A<true, true>(kcur, vcur, qf, st.negm, kpre, vpre, C0, C1, P0, P1, pf, st.l, dj, vlin, [&]() { midA(n); }); \
        SEG(0); \
        BARRIER_L(); \
        SEG(2); \
        headB(n); \
        attn_B<true, true>(vcur, kcur, o, pf, vpre, kpre, C0, C1, st, RELARGS, [&]() { midB(n); }); \
        tailB(n); \
        SEG(3); \
        if ((n) < 16) { WAIT_BAR(0); }                  \
        else {                                          \
            if (wid < 2) asm volatile("global_load_dword %0, %1, %2" : "=v"(pfd) : "v"(vlin * 8), "s"((const char*)wo + (size_t)(bid * 2 + wid) * 8192)); \
            BARRIER_L(); \
        } \
        SEG(4); \
    } while (0)
    unsigned pfd = 0;
    for (int n = 1; n < 17; n += 2) {
        STEP(n, sB0, sB1, sA0, sA1);
        STEP(n + 1, sA0, sA1, sB0, sB1);
    }
#undef STEP
    attn_A<false, true>(kcur, vcur, qf, st.negm, kpre, vpre, sB0, sB1, sA0, sA1, pf, st.l, dj, vlin, [&]() { midA(17); });
    BARRIER_L();
    attn_B<true, false>(vcur, kcur, o, pf, vpre, kpre, sB0, sB1, st, false, false, 0, pend, fpend, [&]() {});
    BARRIER_L();
    SEG_OUT(1);
    if (grp == 0) { BARRIER_L(); }
    STAMP(1, 3);
    BARRIER_L();
    {
        const float lsum = half_sum(st.l);
        LAS char* xch = (LAS char*)lds_;
        LAS char* mine = xch + wid * 8192;
        auto put = [&](int dd, const f32x16& v) {
#pragma unroll
            for (int q4 = 0; q4 < 4; ++q4)
                *(LAS f32x4*)(mine + (dd * 4 + q4) * 1024 + lane * 16) = (f32x4){v[4 * q4], v[4 * q4 + 1], v[4 * q4 + 2], v[4 * q4 + 3]};
        };
        if (grp) { put(0, o[0]); put(1, o[1]); } else { put(0, o[2]); put(1, o[3]); }
        LAS float* ml = (LAS float*)(xch + 65536);
        ml[wid * 128 + lane] = st.mhat; ml[wid * 128 + 64 + lane] = lsum;
        BARRIER_L();
        const int ow = wid ^ 4;
        const float m2 = ml[ow * 128 + lane], l2 = ml[ow * 128 + 64 + lane];
        const float mm = fmaxf(st.mhat, m2);
        const float f1 = __builtin_amdgcn_exp2f(st.mhat - mm), f2 = __builtin_amdgcn_exp2f(m2 - mm);
        const float inv = 1.0f / (lsum * f1 + l2 * f2);
        const float c1 = f1 * inv, c2 = f2 * inv;
        const LAS char* theirs = xch + ow * 8192;
        auto fin = [&](int dd, f32x16 v) {
#pragma unroll
            for (int q4 = 0; q4 < 4; ++q4) {
                const f32x4 t = *(const LAS f32x4*)(theirs + (dd * 4 + q4) * 1024 + lane * 16);
#pragma unroll
                for (int e = 0; e < 4; ++e) v[4 * q4 + e] = v[4 * q4 + e] * c1 + t[e] * c2;
            }
            store_ctx(blkB, 2 * grp + dd, v, 1.0f);
        };
        if (grp) { fin(0, o[2]); fin(1, o[3]); } else { fin(0, o[0]); fin(1, o[1]); }
    }
    asm volatile("s_waitcnt vmcnt(0)" : "+v"(pfd));
    BARRIER_L();
    if (tid == 0) { flag_store(C.sy + SY_CTX + blkB * 32 + h, C.tag); flag_store(C.sy + SY_CTX + blkA * 32 + h, C.tag); }
    STAMP(1, 4);
}

constexpr int K3_SLOT = 32768, K3_NS = 5, K3_LDS = K3_NS * K3_SLOT;
DI void phase_oproj(const Params& P, const Ctx& C, const Routed& R) {
    char* ws = P.ws; char* lds_ = C.lds_;
    const lptr lds = C.lds;
    const unsigned lds0 = C.lds0;
    const int lane = C.lane, l31 = lane & 31, hi = lane >> 5, wid = C.wid;
    const int bid = C.bid, rt = (bid & 7) * 4 + ((bid >> 3) & 3), ct = bid >> 5;
    STAMP(2, 0);
    if (wid >= 4) {
        const int lw = wid - 4;
        const char* csrc = ws + WS_CTX + (size_t)(2 * rt + (lw >> 1)) * 32 * 8192 + (lw & 1) * 4096;
        const char* wsrc = (const char*)P.wo + ct * 128;
        unsigned voffW[4];
#pragma unroll
        for (int j = 0; j < 4; ++j) { const int row = (lw * 4 + j) * 8 + (lane >> 3);
            voffW[j] = row * 1024 + (((lane & 7) ^ (((row >> 1) & 1) << 2)) << 4); }
        const unsigned vlin = lane * 16;
        auto stageW = [&](int hh, int slot) {
            const unsigned b = lds0 + slot * K3_SLOT + lw * 4096;
#pragma unroll
            for (int j = 0; j < 4; ++j) glds16(wsrc + (size_t)hh * 131072, voffW[j], b + 16384 + j * 1024);
        };
        auto stageC = [&](int hh, int slot) { glds16x4(csrc + (size_t)hh * 8192, vlin, lds0 + slot * K3_SLOT + lw * 4096); };
        stageW(0, 0); stageW(1, 1); stageW(2, 2); stageW(3, 3);
        asm volatile("s_barrier" ::: "memory");
        stageC(0, 0); stageC(1, 1); stageC(2, 2); stageC(3, 3);
        int slot = 4;
        for (int hh = 0; hh < 32; ++hh) {
            if (hh == 0) { WAIT_BAR(12); } else if (hh == 1) { WAIT_BAR(16); } else if (hh == 2) { WAIT_BAR(20); }
            else if (hh < 29) { WAIT_BAR(24); } else if (hh == 29) { WAIT_BAR(16); } else if (hh == 30) { WAIT_BAR(8); } else { WAIT_BAR(0); }
            if (hh + 4 < 32) { stageC(hh + 4, slot); stageW(hh + 4, slot); }
            slot = slot == K3_NS - 1 ? 0 : slot + 1;
        }
        WAIT_BAR(0);
        WAIT_BAR(0);
        return;
    }
    const int qg2 = wid >> 1, cb = wid & 1;
    const int i0 = R.sel0, i1 = R.sel1;
    const bool has0 = (i0 >> 6) == rt, has1 = (i1 >> 6) == rt;
    const int q = rt * 64 + qg2 * 32 + l31;
    if (wid == 0) { wait_flags<1>(C.sy + SY_CTX + (rt >> 1) * 32, 32, C.tag, lane); acquire_agent(); asm volatile("s_waitcnt vmcnt(0)" ::: "memory"); }
    asm volatile("s_barrier" ::: "memory");
    const float g = ((const float*)(ws + WS_G))[q];
    s16x4 xb[4];
#pragma unroll
    for (int q4 = 0; q4 < 4; ++q4) xb[q4] = *(const s16x4*)(P.ax + (size_t)q * DM + ct * 64 + cb * 32 + 8 * q4 + 4 * hi);
    const int tq = (lane & 15) >> 2, tp = lane & 3, tblk = (lane >> 4) & 1;
    const lptr wA = lds + 16384 + (4 * hi + tq) * 128 + ((cb ^ ((tq >> 1) & 1)) << 6) + tblk * 32 + tp * 8;
    const lptr cA = lds + qg2 * 8192 + lane * 16;
    f32x16 acc = {};
    int bo = 0;
    auto run = [&](int n) {
        for (int hh = 0; hh < n; ++hh) {
            asm volatile("s_waitcnt lgkmcnt(0)\n\ts_barrier" ::: "memory");
#pragma unroll
            for (int f = 0; f < 8; ++f) {
                const bf16x8 cf = ldsr128(cA + bo + f * 1024);
                const bf16x8 wf = cat(ldstr(wA + bo + f * 2048), ldstr(wA + bo + f * 2048 + 1024));
                acc = MFMA(wf, cf, acc);
            }
            bo = bo == (K3_NS - 1) * K3_SLOT ? 0 : bo + K3_SLOT;
        }
    };
    unsigned yf[4]; f32x4 yv4[4];
    if (wid == 1) {
#pragma unroll
        for (int j = 0; j < 4; ++j) yf[j] = flag_load(C.sy + SY_Y + lane + 64 * j);
    }
    run(4);
    STAMP(2, 2);
    if (wid == 1) {
        if (!__all(yf[0] == C.tag && yf[1] == C.tag && yf[2] == C.tag && yf[3] == C.tag)) wait_flags<4>(C.sy + SY_Y, 256, C.tag, lane);
        const rsrc_t ypr = make_rsrc(ws + WS_YP, MOE_WGS * 4096);
#pragma unroll
        for (int j = 0; j < 4; ++j)
            yv4[j] = __builtin_bit_cast(f32x4, __builtin_amdgcn_raw_buffer_load_b128(ypr, (lane + 64 * j) * 4096 + bid * 16, 0, 16));
    }
    run(8);
    if (wid == 1) {
        f32x4 y = (yv4[0] + yv4[1]) + (yv4[2] + yv4[3]);
#pragma unroll
        for (int e = 0; e < 4; ++e) y[e] = wave_sum(y[e], lane);
        if (lane < 4) store_f32_wt((float*)(ws + WS_Y) + bid * 4 + lane, lane == 0 ? y[0] : lane == 1 ? y[1] : lane == 2 ? y[2] : y[3]);
    }
    run(10);
    if (wid == 1) {
        asm volatile("s_waitcnt vmcnt(0)" ::: "memory");
        if (lane == 0) flag_store(C.sy + SY_Y2 + bid, C.tag);
    }
    run(10);
    WAIT_BAR(0);
    STAMP(2, 3);
    if (has0 || has1) {
        wait_flags<1>(C.sy + SY_Y2 + ct * 16, 16, C.tag, lane);
        wait_flags<1>(C.sy + SY_Y2 + 128 + ct * 16, 16, C.tag, lane);
    }
    WAIT_BAR(0);
    {
        const bool r0 = (q == i0), r1 = (q == i1);
        const float* yv = (const float*)(ws + WS_Y) + (r1 ? 512 : 0) + ct * 64;
#pragma unroll
        for (int q4 = 0; q4 < 4; ++q4) {
            const int ml = cb * 32 + 8 * q4 + 4 * hi, m = ct * 64 + ml;
            float res[4];
#pragma unroll
            for (int e = 0; e < 4; ++e) {
                float b = bf2f((u16)xb[q4][e]);
                if (r0 || r1) b += __uint_as_float(flag_load((const unsigned*)(yv + ml + e))) * (r1 ? R.sig1 : R.sig0);
                res[e] = g * acc[4 * q4 + e] + (1.0f - g) * b;
            }
            u32x2 w2 = {cvtpk(res[0], res[1]), cvtpk(res[2], res[3])};
            *(u32x2*)(P.out + (size_t)q * DM + m) = w2;
        }
    }
    STAMP(2, 4);
}

__global__ __launch_bounds__(512, 2)
void mega_kernel(Params Parg) {
    extern __shared__ __attribute__((aligned(16))) char lds_[];
    const int wid = __builtin_amdgcn_readfirstlane(threadIdx.x >> 6);
    unsigned tag;
    Routed R;
    QPre Q;
    {
        const Params P = load_params();
        tag = __builtin_amdgcn_readfirstlane(flag_load((const unsigned*)(P.ws + WS_SYNC) + SY_EPOCH)) + 1u;
        phase_kv(P, make_ctx(lds_, P.ws, wid, tag), Q);
    }
    { const Params P = load_params(); phase_attn(P, make_ctx(lds_, P.ws, wid, tag), Q, R); }
    {
        const Params P = load_params();
        const Ctx C = make_ctx(lds_, P.ws, wid, tag);
        phase_oproj(P, C, R);
        if (C.tid == 0) __hip_atomic_fetch_add(C.sy + SY_EPOCH, 1u, __ATOMIC_RELAXED, __HIP_MEMORY_SCOPE_AGENT);
    }
}

extern "C" void kernel_launch(void* const* d_in, const int* in_sizes, int n_in, void* d_out, int out_size, void* d_ws, size_t ws_size,
                              hipStream_t stream) {
    const u16* ax = (const u16*)d_in[0]; const u16* ckv = (const u16*)d_in[1]; const u16* wq = (const u16*)d_in[2];
    const u16* wuk = (const u16*)d_in[3]; const u16* wuv = (const u16*)d_in[4]; const u16* wo = (const u16*)d_in[5];
      const u16* wr = (const u16*)d_in[7]; const u16* wg = (const u16*)d_in[8];
    const u16* wu = (const u16*)d_in[9]; const u16* wd = (const u16*)d_in[10]; const u16* gw = (const u16*)d_in[11];
    char* ws = (char*)d_ws;
    (void)hipFuncSetAttribute((const void*)mega_kernel, hipFuncAttributeMaxDynamicSharedMemorySize, 163840);
    Params P{ax, ckv, wq, wuk, wuv, wo, wr, wg, wu, wd, gw, ws, (u16*)d_out};
    mega_kernel<<<256, 512, 163840, stream>>>(P);
}
```

```cpp
#include <hip/hip_runtime.h>
#include <hip/hip_bf16.h>
#include <stdint.h>
#include <utility>

typedef unsigned short u16;
typedef short bf16x8 __attribute__((ext_vector_type(8)));
typedef short s16x4 __attribute__((ext_vector_type(4)));
typedef short v4i16_t __attribute__((ext_vector_type(4)));
typedef float f32x16 __attribute__((ext_vector_type(16)));
typedef float f32x4 __attribute__((ext_vector_type(4)));
typedef unsigned u32x4 __attribute__((ext_vector_type(4)));
typedef unsigned u32x2 __attribute__((ext_vector_type(2)));
#define LAS __attribute__((address_space(3)))
#define DI __device__ __forceinline__
#define MFMA(a, b, c) __builtin_amdgcn_mfma_f32_32x32x16_bf16((a), (b), (c), 0, 0, 0)
#define SBAR() __builtin_amdgcn_sched_barrier(0)

constexpr int SEQ = 2048, DM = 512, NH = 32, HD = 128, NHD = NH * HD;
constexpr int FF = 1792;
constexpr int MOE_WGS = 224, MOE_COLS = 8;

constexpr size_t WS_KF = 0;
constexpr size_t WS_VF = WS_KF + (size_t)SEQ * NHD * 2;
constexpr size_t WS_CTX = WS_VF + (size_t)SEQ * NHD * 2;
constexpr size_t WS_R = WS_CTX + (size_t)SEQ * NHD * 2;
constexpr size_t WS_G = WS_R + SEQ * 4;
constexpr size_t WS_Y = WS_G + SEQ * 4;
constexpr size_t WS_YP = WS_Y + 4096;
constexpr size_t WS_SYNC = WS_YP + 224 * 4096;
constexpr int SY_EPOCH = 0, SY_KV = 256, SY_R = 512, SY_Y = 768, SY_Y2 = 1024, SY_CTX = 1280;

constexpr size_t WS_DBG = 128u << 20;
#define STAMP(K, i) do {} while (0)
#define SEG_DECL
#define SEG_PARAMS
#define SEG_ARGS
#define SEG_START() do {} while (0)
#define SEG(i) do {} while (0)
#define SEG_OUT(K) do {} while (0)
template <int... Is, class F> DI void static_for_(std::integer_sequence<int, Is...>, F&& f) { (f(std::integral_constant<int, Is>{}), ...); }
template <int N, class F> DI void static_for(F&& f) { static_for_(std::make_integer_sequence<int, N>{}, f); }
DI unsigned cvtpk(float lo, float hi) {
    typedef float f32x2 __attribute__((ext_vector_type(2)));
    typedef __bf16 bf16x2 __attribute__((ext_vector_type(2)));
    f32x2 v = {lo, hi}; bf16x2 b = __builtin_convertvector(v, bf16x2); return __builtin_bit_cast(unsigned, b);
}
DI float bf2f(u16 v) { return __uint_as_float((unsigned)v << 16); }
DI float bfround(float x) { return __uint_as_float(cvtpk(x, x) << 16); }
DI bf16x8 pack8(const f32x16& x, int s, float sc) {
    u32x4 w = {cvtpk(x[8 * s] * sc, x[8 * s + 1] * sc), cvtpk(x[8 * s + 2] * sc, x[8 * s + 3] * sc),
               cvtpk(x[8 * s + 4] * sc, x[8 * s + 5] * sc), cvtpk(x[8 * s + 6] * sc, x[8 * s + 7] * sc)};
    return __builtin_bit_cast(bf16x8, w);
}
DI bf16x8 pack8(const f32x16& x, int s) {
    u32x4 w = {cvtpk(x[8 * s], x[8 * s + 1]), cvtpk(x[8 * s + 2], x[8 * s + 3]),
               cvtpk(x[8 * s + 4], x[8 * s + 5]), cvtpk(x[8 * s + 6], x[8 * s + 7])};
    return __builtin_bit_cast(bf16x8, w);
}
DI void glds16(const void* sbase, unsigned voff, unsigned lds_addr) {
    lds_addr = __builtin_amdgcn_readfirstlane(lds_addr);
    asm volatile("s_mov_b32 m0, %2\n\ts_nop 0\n\tglobal_load_lds_dwordx4 %0, %1" :: "v"(voff), "s"(sbase), "s"(lds_addr) : "memory");
}
DI void glds16x4(const void* sbase, unsigned voff, unsigned lds_addr) {
    lds_addr = __builtin_amdgcn_readfirstlane(lds_addr);
    asm volatile("s_mov_b32 m0, %2\n\ts_nop 0\n\tglobal_load_lds_dwordx4 %0, %1\n\tglobal_load_lds_dwordx4 %0, %1 offset:1024\n\t"
                 "global_load_lds_dwordx4 %0, %1 offset:2048\n\tglobal_load_lds_dwordx4 %0, %1 offset:3072"
                 :: "v"(voff), "s"(sbase), "s"(lds_addr) : "memory");
}
DI unsigned flag_load(const unsigned* p) { return __hip_atomic_load(p, __ATOMIC_RELAXED, __HIP_MEMORY_SCOPE_AGENT); }
DI void flag_store(unsigned* p, unsigned v) { __hip_atomic_store(p, v, __ATOMIC_RELAXED, __HIP_MEMORY_SCOPE_AGENT); }
DI void store_f32_wt(float* p, float v) { __hip_atomic_store((unsigned*)p, __float_as_uint(v), __ATOMIC_RELAXED, __HIP_MEMORY_SCOPE_AGENT); }
template <int NW> DI void wait_flags(const unsigned* f, int n, unsigned tag, int lane) {
    const unsigned long long t0 = __builtin_amdgcn_s_memrealtime();
    for (;;) {
        bool ok = true;
#pragma unroll
        for (int j = 0; j < NW; ++j) { const int i = lane + 64 * j; ok &= (i < n ? flag_load(f + i) : tag) == tag; }
        if (__all(ok)) break;
        if (__builtin_amdgcn_s_memrealtime() - t0 > 5000000ull) break;
        __builtin_amdgcn_s_sleep(2);
    }
}
DI void acquire_agent() { __builtin_amdgcn_fence(__ATOMIC_ACQUIRE, "agent"); }
DI void glds16x4_dev(const void* sbase, unsigned voff, unsigned lds_addr) {
    lds_addr = __builtin_amdgcn_readfirstlane(lds_addr);
    asm volatile("s_mov_b32 m0, %2\n\ts_nop 0\n\tglobal_load_lds_dwordx4 %0, %1 sc1\n\tglobal_load_lds_dwordx4 %0, %1 offset:1024 sc1\n\t"
                 "global_load_lds_dwordx4 %0, %1 offset:2048 sc1\n\tglobal_load_lds_dwordx4 %0, %1 offset:3072 sc1"
                 :: "v"(voff), "s"(sbase), "s"(lds_addr) : "memory");
}
#define WAIT_BAR(N) asm volatile("s_waitcnt vmcnt(" #N ") lgkmcnt(0)\n\ts_barrier" ::: "memory")
#define WAIT_CNT(c) do { if ((c) == 8) { WAIT_BAR(8); } else if ((c) == 4) { WAIT_BAR(4); } else { WAIT_BAR(0); } } while (0)
DI int lane_now() { int l; asm volatile("v_mbcnt_lo_u32_b32 %0, -1, 0\n\tv_mbcnt_hi_u32_b32 %0, -1, %0" : "=v"(l)); return l; }
typedef LAS const char* lptr;
DI bf16x8 ldsr128(lptr p) { return *(const LAS bf16x8*)p; }
DI bf16x8 ldsr128a(lptr p) { return *(const LAS bf16x8*)p; }
DI s16x4 ldstr(lptr p) { return __builtin_bit_cast(s16x4, __builtin_amdgcn_ds_read_tr16_b64_v4i16((LAS v4i16_t*)p)); }
DI bf16x8 cat(s16x4 a, s16x4 b) { return __builtin_shufflevector(a, b, 0, 1, 2, 3, 4, 5, 6, 7); }
DI unsigned shx(unsigned v, int m, int lane) { return (unsigned)__builtin_amdgcn_ds_bpermute((lane ^ m) << 2, (int)v); }
DI float shx(float v, int m, int lane) { return __uint_as_float(shx(__float_as_uint(v), m, lane)); }
template <int CTRL> DI unsigned dppu(unsigned v) { return (unsigned)__builtin_amdgcn_update_dpp(0, (int)v, CTRL, 0xF, 0xF, true); }
template <int CTRL> DI float dppf(float v) { return __uint_as_float(dppu<CTRL>(__float_as_uint(v))); }
DI float wave_sum(float v, int lane) {
#pragma unroll
    for (int o = 32; o >= 1; o >>= 1) v += shx(v, o, lane);
    return v;
}
DI float sigmoidf_(float x) { return 1.0f / (1.0f + __expf(-x)); }

constexpr int K1_STAGE = 49152, K1_LDS = 163840;
typedef __amdgpu_buffer_rsrc_t rsrc_t;
DI rsrc_t make_rsrc(const void* p, unsigned bytes) { return __builtin_amdgcn_make_buffer_rsrc((void*)p, (short)0, (int)bytes, 0x00020000); }
DI void store_wt(rsrc_t r, unsigned off, bf16x8 v) {
    __builtin_amdgcn_raw_buffer_store_b128(__builtin_bit_cast(u32x4, v), r, (int)off, 0, 16);
}
#define WAIT_VM(N) asm volatile("s_waitcnt vmcnt(" #N ")" ::: "memory")

template <bool ISV, class Pre, class Mid, class Tail>
DI void kv_body(const char* cbase, const char* kbase, const char* vbase, rsrc_t outr, unsigned outoff, lptr lds, unsigned lds0, int wid, int lane,
                Pre&& pre, Mid&& mid, Tail&& tail) {
    const int l31 = lane & 31, hi = lane >> 5;
    unsigned voffW[2], voffC[2], voffG[2];
#pragma unroll
    for (int j = 0; j < 2; ++j) {
        const int n = 2 * wid + j;
        { const int row = 4 * n + (lane >> 4); voffW[j] = row * 8192 + (((lane & 15) ^ ((row & 3) << 2)) << 4); }
        { const int row = 8 * n + (lane >> 3); voffC[j] = row * 1024 + (((lane & 7) ^ ((row >> 1) & 7)) << 4); }
        voffG[j] = (lane ^ ((wid + 8 * j) & 15)) << 4;
    }
    auto stage = [&](int s) {
        const unsigned b = lds0 + (s % 3) * K1_STAGE + 2 * wid * 1024;
#pragma unroll
        for (int j = 0; j < 2; ++j) glds16(kbase + (size_t)s * 64 * 8192, voffW[j], b + j * 1024);
#pragma unroll
        for (int j = 0; j < 2; ++j) glds16(vbase + (size_t)s * 64 * 8192, voffW[j], b + 16384 + j * 1024);
#pragma unroll
        for (int j = 0; j < 2; ++j) glds16(cbase + s * 128, voffC[j], b + 32768 + j * 1024);
    };
    auto gbuf = [&](int k) { return k == 4 ? 98304 : k == 5 ? 131072 : k == 6 ? 0 : 49152; };
    auto stageG = [&](int k) {
#pragma unroll
        for (int j = 0; j < 4; ++j) glds16(cbase + (size_t)(32 * k + wid + 8 * j) * 1024, voffG[j & 1], lds0 + gbuf(k) + (wid + 8 * j) * 1024);
    };
    const int tq = (lane & 15) >> 2, tp = lane & 3, tblk = (lane >> 4) & 1;
    const lptr wA = lds + (ISV ? 16384 : 0) + (8 * hi + tq) * 256 + (((wid & 3) ^ tq) << 6) + tblk * 32 + tp * 8;
    lptr cA[4];
#pragma unroll
    for (int ks = 0; ks < 4; ++ks) cA[ks] = lds + 32768 + l31 * 128 + (((2 * ks + hi) ^ ((l31 >> 1) & 7)) << 4);
    auto mm = [&](const bf16x8& w, const bf16x8& c, const f32x16& acc) { return ISV ? MFMA(c, w, acc) : MFMA(w, c, acc); };
    auto out_tile = [&](int kg, const f32x16& acc) {
        store_wt(outr, outoff + kg * 8192, pack8(acc, 0));
        store_wt(outr, outoff + kg * 8192 + 1024, pack8(acc, 1));
    };

    bf16x8 wf[32];
    f32x16 acc[4] = {};
    lptr gA[8];
    stage(0); stage(1); stage(2);
    pre();
    auto rdW = [&](int s, int ks) { const int bo = (s % 3) * K1_STAGE; wf[4 * s + ks] = cat(ldstr(wA + bo + ks * 4096), ldstr(wA + bo + ks * 4096 + 1024)); };
    auto rdC = [&](int s, int i) { return ldsr128(cA[i >> 2] + (s % 3) * K1_STAGE + (i & 3) * 4096); };
    auto rdG = [&](int k, int ks) { return ldsr128(gA[ks & 7] + gbuf(k) + (ks >> 3) * 256); };
    auto dmaS = [&](int s, int j) {
        const unsigned b = lds0 + (s % 3) * K1_STAGE + 2 * wid * 1024;
        if (j < 2) glds16(kbase + (size_t)s * 64 * 8192, voffW[j], b + j * 1024);
        else if (j < 4) glds16(vbase + (size_t)s * 64 * 8192, voffW[j - 2], b + 16384 + (j - 2) * 1024);
        else glds16(cbase + s * 128, voffC[j - 4], b + 32768 + (j - 4) * 1024);
    };
    auto dmaG = [&](int k, int j) { glds16(cbase + (size_t)(32 * k + wid + 8 * j) * 1024, voffG[j & 1], lds0 + gbuf(k) + (wid + 8 * j) * 1024); };
    bf16x8 cf[8];
    WAIT_BAR(12);
    rdW(0, 0);
#pragma unroll
    for (int i = 0; i < 4; ++i) cf[i] = rdC(0, i);
    static_for<128>([&](auto SI) {
        {
            constexpr int s = SI / 16, i = SI % 16;
            if (i == 12) {
                if (s < 6) { WAIT_BAR(6); } else if (s == 6) { WAIT_BAR(8); } else { WAIT_BAR(8); }
            }
            if (s == 7 && i == 11) {
                const int ln = lane_now();
#pragma unroll
                for (int j = 0; j < 8; ++j) gA[j] = lds + (ln & 31) * 1024 + (((2 * j + (ln >> 5)) ^ (ln & 15)) << 4);
            }
            if (s == 1 && i == 13) mid();
            const int nx = i + 4;
            if (nx < 16) { if ((nx & 3) == 0) rdW(s, nx >> 2); cf[nx & 7] = rdC(s, nx); }
            else if (s < 7) { if (nx == 16) rdW(s + 1, 0); cf[nx & 7] = rdC(s + 1, nx - 16); }
            else cf[nx & 7] = rdG(4, nx - 16);
            acc[i & 3] = mm(wf[4 * s + (i >> 2)], cf[i & 7], acc[i & 3]);
            if (s + 3 < 8) { if (i == 12 || i == 14) dmaS(s + 3, (i - 12) / 2); }
            else if (i >= 12) {
                const int j = i - 12;
                if (s == 5) dmaG(4, j);
                else if (s == 6) dmaG(6, j);
                else dmaG(7, j);
            }
            if (i < 12 && i % 3 == 0) { if (s >= 1 && s + 2 < 8) dmaS(s + 2, 2 + i / 3); }
            if (s == 6 && i >= 2 && i < 10 && !(i & 1)) dmaG(5, (i - 2) / 2);
            if (s == 7 && i == 15) out_tile(0, acc[0]);
            SBAR();
        }
    });
    f32x16 a2[2];
    static_for<128>([&](auto KI) {
        {
            constexpr int k = 4 + KI / 32, ks = KI % 32;
            if (ks == 28 && k < 7) {
                if (k == 4) { WAIT_BAR(16); } else if (k == 5) { WAIT_BAR(16); } else { WAIT_BAR(21); }
            }
            const int nx = ks + 4;
            if (nx < 32) cf[nx & 7] = rdG(k, nx); else if (k < 7) cf[nx & 7] = rdG(k + 1, nx - 32);
            if (ks == 0) a2[k & 1] = mm(wf[0], cf[0], f32x16{}); else a2[k & 1] = mm(wf[ks], cf[ks & 7], a2[k & 1]);
            if constexpr (k == 4) { if constexpr (ks < 3) out_tile(ks + 1, acc[(ks + 1) & 3]); }
            else if constexpr (ks == 3) out_tile(k - 1, a2[(k - 1) & 1]);
            tail(KI);
            SBAR();
        }
    });
    out_tile(7, a2[1]);
}

DI void ld_u16(unsigned& d, const void* sbase, unsigned voff) { asm volatile("global_load_ushort %0, %1, %2" : "=v"(d) : "v"(voff), "s"(sbase)); }
DI void ld_x4(u32x4& d, const void* sbase, unsigned voff) { asm volatile("global_load_dwordx4 %0, %1, %2" : "=v"(d) : "v"(voff), "s"(sbase)); }
DI void ld_u16_nop(unsigned& d, const void* sbase, unsigned voff) { asm volatile("s_nop 4\n\tglobal_load_ushort %0, %1, %2" : "=v"(d) : "v"(voff), "s"(sbase)); }
DI void ld_x4_nop(u32x4& d, const void* sbase, unsigned voff) { asm volatile("s_nop 4\n\tglobal_load_dwordx4 %0, %1, %2" : "=v"(d) : "v"(voff), "s"(sbase)); }
struct Params {
    const u16 *ax, *ckv, *wq, *wuk, *wuv, *wo, *wr, *wg, *wu, *wd, *gw;
    char* ws; u16* out;
};
struct Ctx { lptr lds; char* lds_; unsigned lds0; int tid, lane, wid, bid; unsigned tag; unsigned* sy; };
typedef __attribute__((address_space(4))) const Params* kparams_t;
DI Params load_params() {
    kparams_t pp = (kparams_t)__builtin_amdgcn_kernarg_segment_ptr();
    asm volatile("" : "+s"(pp));
    Params P;
    P.ax = pp->ax; P.ckv = pp->ckv; P.wq = pp->wq; P.wuk = pp->wuk; P.wuv = pp->wuv; P.wo = pp->wo; P.wr = pp->wr; P.wg = pp->wg; P.wu = pp->wu;
    P.wd = pp->wd; P.gw = pp->gw; P.ws = pp->ws; P.out = pp->out;
    return P;
}
DI Ctx make_ctx(char* lds_, char* ws, int wid, unsigned tag) {
    Ctx C;
    C.lds = (lptr)lds_; C.lds_ = lds_; C.lds0 = (unsigned)(uintptr_t)C.lds;
    int lane; asm volatile("v_mbcnt_lo_u32_b32 %0, -1, 0\n\tv_mbcnt_hi_u32_b32 %0, -1, %0" : "=v"(lane));
    asm volatile("" : "+s"(wid), "+s"(tag));
    C.lane = lane; C.wid = wid; C.tid = wid * 64 + lane; C.bid = blockIdx.x; C.tag = tag; C.sy = (unsigned*)(ws + WS_SYNC);
    return C;
}

#define GU_K 5
#define GU_KS 8
constexpr int QST = 49152;
struct QPre { unsigned voffX[4], voffW[2]; u32x4 rv, wgv, wuv; };
constexpr int MOE_WD = 147456 + 2048;
DI int moe_f0(int bid) { return ((bid & 7) * 28 + ((bid >> 3) < 28 ? (bid >> 3) : 27)) * MOE_COLS; }
DI void q_dma(const QPre& Q, const Params& P, const Ctx& C, int s, int j) {
    const int wid = C.wid, h = (C.bid & 7) * 4 + ((C.bid >> 3) & 3), bi = C.bid >> 5;
    const char* xbase = (const char*)P.ax + (size_t)(((wid >> 2) ? 15 - bi : bi) * 128 + (wid & 3) * 32) * 1024;
    const char* wbase = (const char*)P.wq + h * 256;
    const unsigned b = C.lds0 + ((s + 2) % 3) * QST;
    if (j < 2) glds16(wbase + (size_t)s * 64 * 8192, Q.voffW[j], b + 32768 + (2 * wid + j) * 1024);
    else glds16(xbase + s * 128, Q.voffX[j - 2], b + (4 * wid + j - 2) * 1024);
}
DI void phase_kv(const Params& P, Ctx& C, QPre& Q, unsigned tagv) {
    char* ws = P.ws;
    const int lane = C.lane, wid = C.wid, bid = C.bid, h = (bid & 7) * 4 + ((bid >> 3) & 3), p = bid >> 5;
    STAMP(0, 0);
    const int tok = bid * 8 + wid;
    const bf16x8 bxv = *(const bf16x8*)(P.ax + tok * DM + lane * 8), wrv = *(const bf16x8*)(P.wr + lane * 8);
    const bf16x8 gwv = *(const bf16x8*)(P.gw + lane * 8);
    auto pre = [&]() {
        float sr = 0.f, sg = 0.f;
#pragma unroll
        for (int j = 0; j < 8; ++j) { sr += bf2f((u16)bxv[j]) * bf2f((u16)wrv[j]); sg += bf2f((u16)bxv[j]) * bf2f((u16)gwv[j]); }
        sr = wave_sum(sr, lane); sg = wave_sum(sg, lane);
        asm volatile("s_waitcnt vmcnt(18)" : "+v"(tagv));
        C.tag = __builtin_amdgcn_readfirstlane(tagv) + 1u;
        if (lane == 0) { store_f32_wt((float*)(ws + WS_R) + tok, bfround(sr)); store_f32_wt((float*)(ws + WS_G) + tok, sigmoidf_(sg)); }
        STAMP(0, 1);
    };
    auto mid = [&]() { if (C.tid == 0) flag_store(C.sy + SY_R + bid, C.tag); };
    const char* cbase = (const char*)P.ckv + (size_t)p * 256 * 1024;
    const char* kbase = (const char*)P.wuk + h * 256;
    const char* vbase = (const char*)P.wuv + h * 256;
    const unsigned outoff = (unsigned)((h * 64 + 8 * p) * 8 + 2 * (wid & 3)) * 1024 + lane * 16;
    const unsigned kvbytes = (unsigned)((size_t)SEQ * NHD * 2);
    unsigned rp[4];
    auto tail = [&](auto KI) {
        constexpr int k = 4 + KI / 32, ks = KI % 32;
        if constexpr (k == 4 && ks == 20) {
            const int ln = lane_now();
#pragma unroll
            for (int j = 0; j < 4; ++j) { const int r = 8 * j + (ln >> 3); Q.voffX[j] = r * 1024 + (((ln & 7) ^ ((r >> 1) & 7)) << 4); }
#pragma unroll
            for (int j = 0; j < 2; ++j) { const int row = 4 * (2 * wid + j) + (ln >> 4); Q.voffW[j] = row * 8192 + (((ln & 15) ^ ((row & 3) << 2)) << 4); }
            if (wid == 0) {
#pragma unroll
                for (int j = 0; j < 4; ++j) asm volatile("global_load_dword %0, %1, %2 sc1" : "=v"(rp[j]) : "v"(ln * 4), "s"(C.sy + SY_R + 64 * j));
            }
        }
        if constexpr (k == GU_K && ks == GU_KS) ld_x4(Q.wgv, (const char*)P.wg + moe_f0(bid) * 2, (wid * 64 + lane_now()) * (FF * 2));
        if constexpr (k == GU_K && ks == GU_KS + 1) ld_x4(Q.wuv, (const char*)P.wu + moe_f0(bid) * 2, (wid * 64 + lane_now()) * (FF * 2));
        if constexpr (k == 6 && ks == 8) glds16((const char*)P.wd + (size_t)(moe_f0(bid) + wid) * 1024, lane_now() * 16, C.lds0 + MOE_WD + wid * 1024);
        if constexpr (k == 5 && ks >= 28) q_dma(Q, P, C, 0, ks - 28);
        if constexpr (k == 6 && ks < 2) q_dma(Q, P, C, 0, 4 + ks);
        if constexpr (k == 6 && ks == 20) {
            if (wid == 0) {
                asm volatile("s_waitcnt vmcnt(13)" : "+v"(rp[0]), "+v"(rp[1]), "+v"(rp[2]), "+v"(rp[3]));
                if (__builtin_expect(!__all(rp[0] == C.tag && rp[1] == C.tag && rp[2] == C.tag && rp[3] == C.tag), 0)) wait_flags<4>(C.sy + SY_R, 256, C.tag, lane_now());
            }
        }
        if constexpr (k == 6 && ks >= 28) q_dma(Q, P, C, 1, ks - 28);
        if constexpr (k == 7 && ks < 2) q_dma(Q, P, C, 1, 4 + ks);
        if constexpr (k == 7 && ks == 5) asm volatile("global_load_dwordx4 %0, %1, %2 sc1" : "=v"(Q.rv) : "v"((wid * 64 + lane_now()) * 16), "s"(P.ws + WS_R));
    };
    if (wid < 4) kv_body<false>(cbase, kbase, vbase, make_rsrc(ws + WS_KF, kvbytes), outoff, C.lds, C.lds0, wid, lane, pre, mid, tail);
    else         kv_body<true>(cbase, kbase, vbase, make_rsrc(ws + WS_VF, kvbytes), outoff, C.lds, C.lds0, wid, lane, pre, mid, tail);
    STAMP(0, 3);
}

DI unsigned umax_(unsigned a, unsigned b) { return a > b ? a : b; }
DI unsigned umin_(unsigned a, unsigned b) { return a < b ? a : b; }
DI float lo16(unsigned v) { return __uint_as_float(v << 16); }
DI float hi16(unsigned v) { return __uint_as_float(v & 0xFFFF0000u); }

constexpr int K2_QST = 49152, K2_SLOT = 32768, K2_QB = 131072, K2_LDS = 163840;
constexpr float QSCALE = 0.08838834764831845f * 1.4426950408889634f;
constexpr float THR = 8.0f;
constexpr int RING0 = 3;

#define SETPRIO(x) __builtin_amdgcn_s_setprio(x)
struct SMState { float mhat, l; f32x16 negm; };
#define PIN(x) asm volatile("" : "+v"(x))
DI float max3(float a, float b, float c) { return fmaxf(fmaxf(a, b), c); }

#define EXP2(x) __builtin_amdgcn_exp2f(x)
#define VOFF(i) (((i) >> 3) * 8192 + (2 * ((i) & 3) + (((i) >> 2) & 1)) * 1024)
#define KOFF(i) (((i) & 1) * 8192 + ((i) >> 1) * 1024)
DI float fadd_s(float a, float b) { float r; asm("v_add_f32_e32 %0, %1, %2" : "=v"(r) : "v"(a), "v"(b)); return r; }
DI float max3_s(float a, float b, float c) { float r; asm("v_max3_f32 %0, %1, %2, %3" : "=v"(r) : "v"(a), "v"(b), "v"(c)); return r; }
DI float max2_s(float a, float b) { float r; asm("v_max_f32_e32 %0, %1, %2" : "=v"(r) : "v"(a), "v"(b)); return r; }
template <int OFF> DI void glds_step(const void* sbase, unsigned voff, unsigned lds_addr) {
    if (OFF == 0) asm volatile("s_mov_b32 m0, %2\n\ts_nop 0\n\tglobal_load_lds_dwordx4 %0, %1 sc1" :: "v"(voff), "s"(sbase), "s"(lds_addr));
    else asm volatile("global_load_lds_dwordx4 %0, %1 offset:%c2 sc1" :: "v"(voff), "s"(sbase), "i"(OFF));
}
DI f32x16 mfma_c(bf16x8 a, bf16x8 b, const f32x16& c) {
    f32x16 d; asm volatile("v_mfma_f32_32x32x16_bf16 %0, %1, %2, %3" : "=&v"(d) : "v"(a), "v"(b), "v"(c)); return d;
}
struct DmaJob { const char* src0; const char* src1; unsigned dst0, dst1; bool has0, has1; };

#define LA 4
template <bool QK, bool FIN, class Mid>
DI void attn_A(lptr kp, const lptr& vpn, const bf16x8 (&qf)[8], const f32x16& negm, const bf16x8 (&kpre)[LA], bf16x8 (&vpre)[LA], f32x16& s0, f32x16& s1,
               const f32x16& p0, const f32x16& p1, bf16x8 (&pf)[4], float& l, const DmaJob& dj, unsigned vlin, Mid&& mid) {
    bf16x8 fr[16];
    unsigned pw[16];
    float sa = 0.f, sb = 0.f;
#pragma unroll
    for (int i = 0; i < LA; ++i) fr[i] = kpre[i];
#pragma unroll
    for (int i = 0; i < 16; ++i) {
        if (QK && i + LA < 16) fr[i + LA] = ldsr128a(kp + KOFF(i + LA));
        if (i + LA >= 16) vpre[i + LA - 16] = ldsr128a(vpn + VOFF(i + LA - 16));
        if (QK) {
            if (i == 0) s0 = mfma_c(fr[0], qf[0], negm);
            else if (i == 1) s1 = mfma_c(fr[1], qf[0], negm);
            else if (i & 1) s1 = MFMA(fr[i], qf[i >> 1], s1);
            else s0 = MFMA(fr[i], qf[i >> 1], s0);
        }
        if (FIN) {
            const float e0 = i < 8 ? p0[2 * i] : p1[2 * i - 16], e1 = i < 8 ? p0[2 * i + 1] : p1[2 * i - 15];
            if (i == 0) { sa = e0; sb = e1; } else { sa = fadd_s(sa, e0); sb = fadd_s(sb, e1); PIN(sa); PIN(sb); }
            pw[i] = cvtpk(e0, e1);
            PIN(pw[i]);
        }
        if (i == 3) mid();
        if (QK) {
            if (i == 1 && dj.has0) { glds_step<0>(dj.src0, vlin, dj.dst0); glds_step<1024>(dj.src0, vlin, dj.dst0); }
            if (i == 5 && dj.has0) { glds_step<2048>(dj.src0, vlin, dj.dst0); glds_step<3072>(dj.src0, vlin, dj.dst0); }
            if (i == 9 && dj.has1) { glds_step<0>(dj.src1, vlin, dj.dst1); glds_step<1024>(dj.src1, vlin, dj.dst1); }
            if (i == 13 && dj.has1) { glds_step<2048>(dj.src1, vlin, dj.dst1); glds_step<3072>(dj.src1, vlin, dj.dst1); }
        }
        SBAR();
    }
    if (FIN) {
        l += sa + sb;
#pragma unroll
        for (int i = 0; i < 4; ++i) { u32x4 w = {pw[4 * i], pw[4 * i + 1], pw[4 * i + 2], pw[4 * i + 3]}; pf[i] = __builtin_bit_cast(bf16x8, w); }
    }
}
DI void dma_plain(const DmaJob& dj, unsigned vlin) {
    if (dj.has0) glds16x4_dev(dj.src0, vlin, dj.dst0);
    if (dj.has1) glds16x4_dev(dj.src1, vlin, dj.dst1);
}
template <bool PV, bool SM, class Mid>
DI void attn_B(lptr vp, const lptr& kpn, f32x16 (&o)[4], const bf16x8 (&pf)[4], const bf16x8 (&vpre)[LA], bf16x8 (&kpre)[LA],
               f32x16& s0, f32x16& s1, SMState& st, bool first, bool mask, int qrel  , bool& pend, float& fpend, Mid&& mid) {
    bf16x8 fr[16];
    float ma = 0.f, mb = 0.f, mc = 0.f;
#pragma unroll
    for (int i = 0; i < LA; ++i) fr[i] = vpre[i];
    if (SM && __builtin_expect(mask, 0)) {
        const float NEG = -__builtin_inff();
#pragma unroll
        for (int r = 0; r < 16; ++r) { const int c = (r & 3) + 8 * (r >> 2);
            if (c > qrel) s0[r] = NEG;
            if (c + 32 > qrel) s1[r] = NEG; }
    }
    SETPRIO(1);
    static_for<16>([&](auto I) {
        constexpr int i = I;
        if (PV && i + LA < 16) fr[i + LA] = ldsr128a(vp + VOFF(i + LA));
        if (i + LA >= 16) kpre[i + LA - 16] = ldsr128a(kpn + KOFF(i + LA - 16));
        if (PV) o[i & 3] = MFMA(fr[i], pf[i >> 2], o[i & 3]);
        if (SM) {
            if (i == 0) { ma = max3(s0[0], s0[1], s0[2]); mb = max3(s1[0], s1[1], s1[2]); mc = max3(s0[3], s0[4], s1[3]); PIN(ma); PIN(mb); PIN(mc); }
            if (i == 1) { ma = max3_s(ma, s0[5], s0[6]); mb = max3_s(mb, s1[4], s1[5]); mc = max3_s(mc, s0[7], s1[6]); PIN(ma); PIN(mb); PIN(mc); }
            if (i == 2) { ma = max3_s(ma, s0[8], s0[9]); mb = max3_s(mb, s1[7], s1[8]); mc = max3_s(mc, s0[10], s1[9]); PIN(ma); PIN(mb); PIN(mc); }
            if (i == 3) { ma = max3_s(ma, s0[11], s0[12]); mb = max3_s(mb, s1[10], s1[11]); mc = max3_s(mc, s0[13], s1[12]); PIN(ma); PIN(mb); PIN(mc); }
            if (i == 4) { ma = max3_s(ma, s0[14], s0[15]); mb = max3_s(mb, s1[13], s1[14]); mc = max2_s(mc, s1[15]); PIN(ma); PIN(mb); PIN(mc); }
            if (i == 5) {
                float rm = max3_s(ma, mb, mc);
                auto rr = __builtin_amdgcn_permlane32_swap(__float_as_uint(rm), __float_as_uint(rm), false, false);
                rm = max2_s(__uint_as_float(rr[0]), __uint_as_float(rr[1]));
                if (__builtin_expect(first || __any(rm > THR), 0)) {
                    const float dl = first ? rm : fmaxf(rm, 0.f), f = first ? 1.0f : __builtin_amdgcn_exp2f(-dl);
                    st.mhat += dl; st.l *= f;
#pragma unroll
                    for (int r = 0; r < 16; ++r) { s0[r] -= dl; s1[r] -= dl; st.negm[r] = -st.mhat; }
                    pend = !first; fpend = f;
                }
            }
            if (i >= 6) {
                constexpr int e0 = i < 14 ? 3 * (i - 6) : 24 + 4 * (i - 14), e1 = i < 14 ? e0 + 3 : e0 + 4;
#pragma unroll
                for (int e = e0; e < e1; ++e) { if (e < 16) { s0[e] = EXP2(s0[e]); } else { s1[e - 16] = EXP2(s1[e - 16]); } }
                PIN(s0); PIN(s1);
            }
        }
        if (i == 7) mid();
        SBAR();
    });
    SETPRIO(0);
}

struct Routed { int sel0, sel1; float sig0, sig1; };
DI void phase_attn(const Params& P, const Ctx& C, QPre& Q, Routed& R) {
    char* ws = P.ws; char* lds_ = C.lds_;
    const u16 *x = P.ax, *wq = P.wq, *wo = P.wo, *bx = P.ax, *wg = P.wg, *wu = P.wu, *wd = P.wd;
    const lptr lds = C.lds;
    const unsigned lds0 = C.lds0;
    const int tid = C.tid, lane = C.lane, l31 = lane & 31, hi = lane >> 5, wid = C.wid;
    const int bid = C.bid, h = (bid & 7) * 4 + ((bid >> 3) & 3), bi = bid >> 5;
    const int grp = wid >> 2, rg = wid & 3;
    const int blkA = bi, blkB = 15 - bi;

    STAMP(1, 0);
    LAS unsigned* sk = (LAS unsigned*)((LAS char*)lds_ + 147456);
    LAS float* hs = (LAS float*)((LAS char*)lds_ + 147456 + 128);
    LAS float* red = (LAS float*)((LAS char*)lds_ + 147456 + 256);
    const int cg = moe_f0(bid) / MOE_COLS;
    unsigned k1 = 0, k2 = 0, xs0, xs1;
    u32x4 skv[4];
    float mv[32], ma0, ma1, hsv[16];
    unsigned wdv[8];
    auto top2 = [&](unsigned b1, unsigned b2) { k2 = umax_(umin_(k1, b1), umax_(k2, b2)); k1 = umax_(k1, b1); };
    auto ins = [&](unsigned key) { k2 = umax_(k2, umin_(k1, key)); k1 = umax_(k1, key); };
    auto moe_gap = [&](auto GI) {
        constexpr int G = GI, s = G / 16, i = G % 16;
        if constexpr (G == 13 || G == 14) {
            if constexpr (G == 13) asm volatile("" : "+v"(Q.rv));
#pragma unroll
            for (int j = 2 * (G - 13); j < 2 * (G - 13) + 2; ++j) {
                const unsigned u = Q.rv[j] >> 16;
                const unsigned o = (u & 0x8000u) ? (~u & 0xFFFFu) : (u | 0x8000u);
                ins((o << 16) | (unsigned)(0xFFFF - (tid * 4 + j)));
            }
            PIN(k1); PIN(k2);
        }
        if constexpr (G == 15) { top2(dppu<0x128>(k1), dppu<0x128>(k2)); PIN(k1); PIN(k2); }
        if constexpr (G == 16) { top2(dppu<0x124>(k1), dppu<0x124>(k2)); PIN(k1); PIN(k2); }
        if constexpr (G == 17) { top2(dppu<0x122>(k1), dppu<0x122>(k2)); PIN(k1); PIN(k2); }
        if constexpr (G == 18) { top2(dppu<0x121>(k1), dppu<0x121>(k2)); PIN(k1); PIN(k2); }
        if constexpr (G == 19) { auto a = __builtin_amdgcn_permlane16_swap(k1, k1, false, false); auto b = __builtin_amdgcn_permlane16_swap(k2, k2, false, false);
            k1 = a[0]; k2 = b[0]; top2(a[1], b[1]); PIN(k1); PIN(k2); }
        if constexpr (G == 20) { auto a = __builtin_amdgcn_permlane32_swap(k1, k1, false, false); auto b = __builtin_amdgcn_permlane32_swap(k2, k2, false, false);
            k1 = a[0]; k2 = b[0]; top2(a[1], b[1]);
            if (lane == 0) { sk[wid * 2] = k1; sk[wid * 2 + 1] = k2; } }
        if constexpr (G == 29) {
#pragma unroll
            for (int j = 0; j < 4; ++j) skv[j] = *(const LAS u32x4*)(sk + 4 * j);
            k1 = 0; k2 = 0;
        }
        if constexpr (G == 30 || G == 31) {
#pragma unroll
            for (int j = 8 * (G - 30); j < 8 * (G - 30) + 8; j += 2) top2(skv[j >> 2][j & 3], skv[j >> 2][(j & 3) + 1]);
            PIN(k1); PIN(k2);
        }
        if constexpr (G == 32) {
            k1 = __builtin_amdgcn_readfirstlane(k1); k2 = __builtin_amdgcn_readfirstlane(k2);
            R.sel0 = 0xFFFF - (int)(k1 & 0xFFFFu); R.sel1 = 0xFFFF - (int)(k2 & 0xFFFFu);
            ld_u16_nop(xs0, (const char*)bx + R.sel0 * 1024, tid * 2);
            ld_u16_nop(xs1, (const char*)bx + R.sel1 * 1024, tid * 2);
        }
        if constexpr (G == 33) {
            const unsigned o0 = k1 >> 16, o1 = k2 >> 16;
            const float r0 = __uint_as_float(((o0 & 0x8000u) ? (o0 & 0x7FFFu) : (~o0 & 0xFFFFu)) << 16);
            const float r1 = __uint_as_float(((o1 & 0x8000u) ? (o1 & 0x7FFFu) : (~o1 & 0xFFFFu)) << 16);
            R.sig0 = __uint_as_float(__builtin_amdgcn_readfirstlane(__float_as_uint(sigmoidf_(r0))));
            R.sig1 = __uint_as_float(__builtin_amdgcn_readfirstlane(__float_as_uint(sigmoidf_(r1))));
        }
        if constexpr (G >= 61 && G <= 64) {
            if constexpr (G == 61) { asm volatile("" : "+v"(xs0), "+v"(xs1), "+v"(Q.wgv), "+v"(Q.wuv)); ma0 = lo16(xs0); ma1 = lo16(xs1); }
            constexpr int c = 2 * (G - 61);
            const float g0 = lo16(Q.wgv[c >> 1]), g1 = hi16(Q.wgv[c >> 1]), u0 = lo16(Q.wuv[c >> 1]), u1 = hi16(Q.wuv[c >> 1]);
            mv[c] = ma0 * g0; mv[c + 1] = ma0 * g1; mv[8 + c] = ma0 * u0; mv[9 + c] = ma0 * u1;
            mv[16 + c] = ma1 * g0; mv[17 + c] = ma1 * g1; mv[24 + c] = ma1 * u0; mv[25 + c] = ma1 * u1;
            PIN(mv[c]); PIN(mv[c + 1]); PIN(mv[8 + c]); PIN(mv[9 + c]); PIN(mv[16 + c]); PIN(mv[17 + c]); PIN(mv[24 + c]); PIN(mv[25 + c]);
        }
        if constexpr (G >= 65 && G <= 68) {
#pragma unroll
            for (int j = 4 * (G - 65); j < 4 * (G - 65) + 4; ++j) { auto r = __builtin_amdgcn_permlane32_swap(__float_as_uint(mv[j]), __float_as_uint(mv[j + 16]), false, false);
                mv[j] = __uint_as_float(r[0]) + __uint_as_float(r[1]); PIN(mv[j]); }
        }
        if constexpr (G == 69 || G == 70) {
#pragma unroll
            for (int j = 4 * (G - 69); j < 4 * (G - 69) + 4; ++j) { auto r = __builtin_amdgcn_permlane16_swap(__float_as_uint(mv[j]), __float_as_uint(mv[j + 8]), false, false);
                mv[j] = __uint_as_float(r[0]) + __uint_as_float(r[1]); PIN(mv[j]); }
        }
        if constexpr (G == 71) { const bool up_ = lane & 8;
#pragma unroll
            for (int j = 0; j < 4; ++j) { const float send = up_ ? mv[j] : mv[j + 4], keep = up_ ? mv[j + 4] : mv[j]; mv[j] = keep + dppf<0x128>(send); PIN(mv[j]); } }
        if constexpr (G == 72) { const bool up_ = lane & 4;
#pragma unroll
            for (int j = 0; j < 2; ++j) { const float send = up_ ? mv[j] : mv[j + 2], keep = up_ ? mv[j + 2] : mv[j]; mv[j] = keep + dppf<0x1B>(dppf<0x141>(send)); PIN(mv[j]); } }
        if constexpr (G == 73) { const bool up_ = lane & 2;
            const float send = up_ ? mv[0] : mv[1], keep = up_ ? mv[1] : mv[0]; mv[0] = keep + dppf<0x4E>(send);
            mv[0] += dppf<0xB1>(mv[0]);
            if (!(lane & 1)) red[wid * 32 + (lane >> 1)] = mv[0];
        }
        if constexpr (G == 77) {
            if (tid < 16) {
                const int c = tid & 7, t = tid >> 3;
                float g = 0.f, u = 0.f;
#pragma unroll
                for (int w = 0; w < 8; ++w) { g += red[w * 32 + 16 * t + c]; u += red[w * 32 + 16 * t + 8 + c]; }
                g = bfround(g); u = bfround(u);
                hs[t * 8 + c] = bfround(bfround(g * sigmoidf_(g)) * u);
            }
        }
        if constexpr (G == 93) {
#pragma unroll
            for (int j = 0; j < 8; ++j) wdv[j] = *(const LAS u16*)((LAS char*)lds_ + MOE_WD + j * 1024 + tid * 2);
#pragma unroll
            for (int j = 0; j < 4; ++j) { const f32x4 t = *(const LAS f32x4*)(hs + 4 * j); hsv[4 * j] = t[0]; hsv[4 * j + 1] = t[1]; hsv[4 * j + 2] = t[2]; hsv[4 * j + 3] = t[3]; }
        }
        if constexpr (G == 95) {
            float y0 = 0.f, y1 = 0.f;
#pragma unroll
            for (int j = 0; j < 8; ++j) { const float w = lo16(wdv[j]); y0 += hsv[j] * w; y1 += hsv[8 + j] * w; }
            float* yp = (float*)(ws + WS_YP) + (size_t)cg * 1024;
            store_f32_wt(yp + tid, y0); store_f32_wt(yp + 512 + tid, y1);
        }
        (void)s; (void)i;
    };

    const char* Kh = ws + WS_KF + (size_t)h * 64 * 8192;
    const unsigned vlin = lane * 16;
    bf16x8 qf[8];
    {
        lptr xA[4];
#pragma unroll
        for (int ks = 0; ks < 4; ++ks) xA[ks] = lds + (32 * wid + l31) * 128 + (((2 * ks + hi) ^ ((l31 >> 1) & 7)) << 4);
        const int tq = (lane & 15) >> 2, tp = lane & 3, tblk = (lane >> 4) & 1;
        lptr wA[4];
#pragma unroll
        for (int c = 0; c < 4; ++c) wA[c] = lds + 32768 + (8 * hi + tq) * 256 + ((c ^ tq) << 6) + tblk * 32 + tp * 8;
        auto dmaS = [&](int s, int j) { q_dma(Q, P, C, s, j); };
        auto rdW = [&](int s, int i) { const int o = ((s + 2) % 3) * QST + (i >> 2) * 4096; return cat(ldstr(wA[i & 3] + o), ldstr(wA[i & 3] + o + 1024)); };
        auto rdX = [&](int s, int ks) { return ldsr128(xA[ks] + ((s + 2) % 3) * QST); };
        f32x16 acc[4] = {};
        bf16x8 wfr[8], xfr[2];
        unsigned kvf = 0;
        WAIT_BAR(14);
        STAMP(1, 1);
        xfr[0] = rdX(0, 0);
#pragma unroll
        for (int i = 0; i < 4; ++i) wfr[i] = rdW(0, i);
        static_for<128>([&](auto SI) {
            {
                constexpr int s = SI / 16, i = SI % 16;
                if (i == 12 && s < 7) {
                    if (s == 0) { WAIT_BAR(8); } else if (s == 1) { WAIT_BAR(6); } else if (s == 2) { WAIT_BAR(8); } else if (s < 6) { WAIT_BAR(6); } else { WAIT_BAR(0); }
                }
                const int nx = i + 4;
                if (nx < 16) { if ((nx & 3) == 0) xfr[(nx >> 2) & 1] = rdX(s, nx >> 2); wfr[nx & 7] = rdW(s, nx); }
                else if (s < 7) { if (nx == 16) xfr[0] = rdX(s + 1, 0); wfr[nx & 7] = rdW(s + 1, nx - 16); }
                acc[i & 3] = MFMA(wfr[i & 7], xfr[(i >> 2) & 1], acc[i & 3]);
                if (i == 12 || i == 14) { if (s + 3 < 8) dmaS(s + 3, (i - 12) / 2); }
                else if (s == 0) { if (i < 6) dmaS(2, i); }
                else if (i < 12 && i % 3 == 0) { if (s + 2 < 8) dmaS(s + 2, 2 + i / 3); }
                if (s == 6 && grp == 0) {
                    if (i == 12) glds16x4_dev(Kh + rg * 4096, vlin, lds0 + RING0 * 16384 + rg * 4096);
                    if (i == 14) glds16x4_dev(Kh + 16384 + rg * 4096, vlin, lds0 + (RING0 + 2) * 16384 + rg * 4096);
                }
                moe_gap(SI);
                if (s == 1 && i == 13 && tid == 0) flag_store(C.sy + SY_KV + h * 8 + bi, C.tag);
                if (s == 4 && i == 3 && wid == 0)
                    asm volatile("global_load_dword %0, %1, %2 sc1" : "=v"(kvf) : "v"((lane & 7) * 4), "s"(C.sy + SY_KV + h * 8));
                if (s == 5 && i == 13 && wid == 0) {
                    asm volatile("" : "+v"(kvf));
                    if (__builtin_expect(!__all(kvf == C.tag), 0)) wait_flags<1>(C.sy + SY_KV + h * 8, 8, C.tag, lane);

                }
                if (s == 6 && i == 13 && tid == 0) flag_store(C.sy + SY_Y + bid, C.tag);
                SBAR();
            }
        });
#pragma unroll
        for (int c = 0; c < 4; ++c) { qf[2 * c] = pack8(acc[c], 0, QSCALE); qf[2 * c + 1] = pack8(acc[c], 1, QSCALE); }
    }

    STAMP(1, 2);
    const int n1 = 2 * bi + 2;
    auto tile_of = [&](int n, int g) { return n < n1 ? n : n1 + 2 * (n - n1) + g; };
    auto jstar = [&](int tn) { return tn <= 2 * n1 ? tn : tn <= 2 * n1 + 2 ? tn + 1 : 2 * tn - 2 * n1 - 2; };
    auto jg1 = [&](int tn) { return tn == 2 * n1 + 1 ? tn : 2 * tn - 2 * n1 - 1; };
    int tb_d0, tb_d1, tb_kv, tb_rel;
    auto make_job = [&](int tn, int& d0, int& d1) {
        const int n = tn >> 1;
        int a_off, b_off; bool a_ok, b_ok;
        const int kb = (int)WS_KF + h * 64 * 8192 + rg * 4096, vb = (int)WS_VF + h * 64 * 8192 + rg * 4096;
        if (tn & 1) { a_ok = n >= 1 && n <= 17; a_off = vb + tile_of(n - 1, 0) * 16384; b_ok = n >= n1 && n <= 16; b_off = kb + tile_of(n, 1) * 16384; }
        else        { a_ok = n <= 16;           a_off = kb + tile_of(n, 0) * 16384;     b_ok = n - 2 >= n1 && n <= 18; b_off = vb + tile_of(n - 2, 1) * 16384; }
        const int da = a_off | ((jstar(tn) + RING0) & 7) | 8, db = b_off | ((jg1(tn) + RING0) & 7) | 8;
        d0 = a_ok ? da : b_ok ? db : 0; d1 = (a_ok && b_ok) ? db : 0;
    };
    {
        const int n = lane < 19 ? lane : 18;
        make_job(2 * n + grp + 3, tb_d0, tb_d1);
        const int ks = ((n < n1 || grp == 0 ? jstar(2 * n) : jg1(2 * n + 1)) + RING0) & 7;
        const int vs = ((n < n1 || grp == 0 ? jstar(2 * n + 3) : jg1(2 * n + 4)) + RING0) & 7;
        tb_kv = ks | vs << 8;
        tb_rel = 128 * ((grp || n >= n1) ? blkB : blkA) + 32 * rg - 64 * tile_of(n, grp);
    }
    auto job_from = [&](int d0, int d1) -> DmaJob {
        DmaJob d;
        d.has0 = d0 & 8; d.has1 = d1 & 8;
        d.src0 = ws + (unsigned)(d0 & ~4095); d.src1 = ws + (unsigned)(d1 & ~4095);
        d.dst0 = lds0 + (d0 & 7) * 16384 + rg * 4096; d.dst1 = lds0 + (d1 & 7) * 16384 + rg * 4096;
        return d;
    };
    auto job = [&](int n) { return job_from(__builtin_amdgcn_readlane(tb_d0, n), __builtin_amdgcn_readlane(tb_d1, n)); };
    if (grp == 1) {
#pragma unroll
        for (int c = 0; c < 8; ++c) *(LAS bf16x8*)((LAS char*)lds_ + K2_QB + rg * 8192 + c * 1024 + lane * 16) = qf[c];
    }
    f32x16 o[4] = {};
    SMState st; st.mhat = 0.f; st.l = 0.f; st.negm = f32x16{};
    const lptr kp0 = lds + lane * 16;
    char* ctx = ws + WS_CTX;
    const rsrc_t ctxr = make_rsrc(ctx, (unsigned)((size_t)SEQ * NHD * 2));
    auto store_ctx = [&](int blk, int d, const f32x16& v, float inv) {
        const unsigned off = (unsigned)(((4 * blk + rg) * 32 + h) * 8 + 2 * d) * 1024 + lane * 16;
        store_wt(ctxr, off, pack8(v, 0, inv));
        store_wt(ctxr, off + 1024, pack8(v, 1, inv));
    };
    auto half_sum = [&](float v) { auto rr = __builtin_amdgcn_permlane32_swap(__float_as_uint(v), __float_as_uint(v), false, false);
                                   return __uint_as_float(rr[0]) + __uint_as_float(rr[1]); };
    auto kslot = [&](int n) { return kp0 + (__builtin_amdgcn_readlane(tb_kv, n) & 7) * 16384; };
    auto vslot = [&](int n) { return kp0 + ((__builtin_amdgcn_readlane(tb_kv, n) >> 8) & 7) * 16384; };
    WAIT_BAR(0);
    bool first = true, pend = false;
    float invA = 0.f, fpend = 1.f;
    f32x16 sA0, sA1, sB0, sB1; bf16x8 pf[4], vpre[LA], kpre[LA];
#pragma unroll
    for (int i = 0; i < LA; ++i) kpre[i] = ldsr128a(kslot(0) + KOFF(i));
    if (grp == 1) { WAIT_BAR(0); }
    SEG_DECL; SEG_START();
    const int nsw = grp == 0 ? n1 - 1 : 100;
    auto headB = [&](int n) {
        if (__builtin_expect(n == nsw + 1, 0)) { invA = 1.0f / half_sum(st.l); st.mhat = 0.f; st.l = 0.f; first = true; }
    };
    auto tailB = [&](int n) {
        first = false;
        if (__builtin_expect(pend || (unsigned)(n - nsw) < 3u, 0)) {
            if (n == nsw + 2 && tid == 0) flag_store(C.sy + SY_CTX + blkA * 32 + h, C.tag);
            if (pend) {
#pragma unroll
                for (int d = 0; d < 4; ++d)
#pragma unroll
                    for (int r = 0; r < 16; ++r) o[d][r] *= fpend;
                pend = false;
            }
            if (n == nsw + 1) {
#pragma unroll
                for (int d = 0; d < 4; ++d) store_ctx(blkA, d, o[d], invA);
#pragma unroll
                for (int d = 0; d < 4; ++d) o[d] = f32x16{};
            }
            if (n == nsw) {
#pragma unroll
                for (int c = 0; c < 8; ++c) qf[c] = ldsr128(lds + K2_QB + rg * 8192 + c * 1024 + lane * 16);
                st.negm = f32x16{};
            }
        }
    };
#define BARRIER_L() asm volatile("s_waitcnt lgkmcnt(0)\n\ts_barrier" ::: "memory")
    lptr kcur = kslot(0), vcur = kp0;
    DmaJob dj = job(0);
    int rel = 0;
    auto pinjob = [&]() { asm volatile("" : "+s"(dj.src0), "+s"(dj.src1), "+s"(dj.dst0), "+s"(dj.dst1)); };
    auto midA = [&](int n) { vcur = vslot(n > 0 ? n - 1 : 0); rel = __builtin_amdgcn_readlane(tb_rel, n); asm volatile("" : "+v"(vcur), "+s"(rel)); };
    auto midB = [&](int n) { kcur = kslot(n + 1); dj = job(n + 1); asm volatile("" : "+v"(kcur)); pinjob(); };
#define RELARGS first, rel < 63, rel + l31 - 4 * hi, pend, fpend
    attn_A<true, false>(kcur, vcur, qf, st.negm, kpre, vpre, sA0, sA1, sA0, sA1, pf, st.l, dj, vlin, [&]() { midA(0); });
    BARRIER_L();
    attn_B<false, true>(vcur, kcur, o, pf, vpre, kpre, sA0, sA1, st, RELARGS, [&]() { midB(0); });
    tailB(0);
    WAIT_BAR(0);
#define STEP(n, C0, C1, P0, P1) do { \
        attn_A<true, true>(kcur, vcur, qf, st.negm, kpre, vpre, C0, C1, P0, P1, pf, st.l, dj, vlin, [&]() { midA(n); }); \
        SEG(0); \
        BARRIER_L(); \
        SEG(2); \
        headB(n); \
        attn_B<true, true>(vcur, kcur, o, pf, vpre, kpre, C0, C1, st, RELARGS, [&]() { midB(n); }); \
        tailB(n); \
        SEG(3); \
        if ((n) < 16) { WAIT_BAR(0); }                  \
        else {                                          \
            if (wid < 2) asm volatile("global_load_dword %0, %1, %2" : "=v"(pfd) : "v"(vlin * 8), "s"((const char*)wo + (size_t)(bid * 2 + wid) * 8192)); \
            BARRIER_L(); \
        } \
        SEG(4); \
    } while (0)
    unsigned pfd = 0;
    for (int n = 1; n < 17; n += 2) {
        STEP(n, sB0, sB1, sA0, sA1);
        STEP(n + 1, sA0, sA1, sB0, sB1);
    }
#undef STEP
    attn_A<false, true>(kcur, vcur, qf, st.negm, kpre, vpre, sB0, sB1, sA0, sA1, pf, st.l, dj, vlin, [&]() { midA(17); });
    BARRIER_L();
    attn_B<true, false>(vcur, kcur, o, pf, vpre, kpre, sB0, sB1, st, false, false, 0, pend, fpend, [&]() {});
    BARRIER_L();
    SEG_OUT(1);
    if (grp == 0) { BARRIER_L(); }
    STAMP(1, 3);
    unsigned cfl = 0;
    const int ort = (bid & 7) * 4 + ((bid >> 3) & 3);
    if (wid == 0) asm volatile("global_load_dword %0, %1, %2 sc1" : "=v"(cfl) : "v"((lane & 31) * 4), "s"(C.sy + SY_CTX + (ort >> 1) * 32));
    BARRIER_L();
    {
        const float lsum = half_sum(st.l);
        LAS char* xch = (LAS char*)lds_;
        LAS char* mine = xch + wid * 8192;
        auto put = [&](int dd, const f32x16& v) {
#pragma unroll
            for (int q4 = 0; q4 < 4; ++q4)
                *(LAS f32x4*)(mine + (dd * 4 + q4) * 1024 + lane * 16) = (f32x4){v[4 * q4], v[4 * q4 + 1], v[4 * q4 + 2], v[4 * q4 + 3]};
        };
        if (grp) { put(0, o[0]); put(1, o[1]); } else { put(0, o[2]); put(1, o[3]); }
        LAS float* ml = (LAS float*)(xch + 65536);
        ml[wid * 128 + lane] = st.mhat; ml[wid * 128 + 64 + lane] = lsum;
        BARRIER_L();
        const int ow = wid ^ 4;
        const float m2 = ml[ow * 128 + lane], l2 = ml[ow * 128 + 64 + lane];
        const float mm = fmaxf(st.mhat, m2);
        const float f1 = __builtin_amdgcn_exp2f(st.mhat - mm), f2 = __builtin_amdgcn_exp2f(m2 - mm);
        const float inv = 1.0f / (lsum * f1 + l2 * f2);
        const float c1 = f1 * inv, c2 = f2 * inv;
        const LAS char* theirs = xch + ow * 8192;
        auto fin = [&](int dd, f32x16 v) {
#pragma unroll
            for (int q4 = 0; q4 < 4; ++q4) {
                const f32x4 t = *(const LAS f32x4*)(theirs + (dd * 4 + q4) * 1024 + lane * 16);
#pragma unroll
                for (int e = 0; e < 4; ++e) v[4 * q4 + e] = v[4 * q4 + e] * c1 + t[e] * c2;
            }
            store_ctx(blkB, 2 * grp + dd, v, 1.0f);
        };
        if (grp) { fin(0, o[2]); fin(1, o[3]); } else { fin(0, o[0]); fin(1, o[1]); }
    }
    asm volatile("s_waitcnt vmcnt(0)" : "+v"(pfd), "+v"(cfl));
    if (wid == 0) {
        const unsigned bal = (unsigned)__ballot(cfl == C.tag);
        unsigned m0 = 0;
#pragma unroll
        for (int g = 0; g < 8; ++g) m0 |= (((bal >> (4 * g)) & 15u) == 15u ? 1u : 0u) << g;
        if (__builtin_popcount(m0) < 2) m0 = 0;
        if (lane == 0) *(LAS unsigned*)((LAS char*)lds_ + 163840 - 16) = m0;
    }
    BARRIER_L();
    if (tid == 0) { flag_store(C.sy + SY_CTX + blkB * 32 + h, C.tag); flag_store(C.sy + SY_CTX + blkA * 32 + h, C.tag); }
    STAMP(1, 4);
}

constexpr int K3_SLOT = 32768, K3_NS = 5, K3_LDS = K3_NS * K3_SLOT;
DI void phase_oproj(const Params& P, const Ctx& C, const Routed& R) {
    char* ws = P.ws; char* lds_ = C.lds_;
    const lptr lds = C.lds;
    const unsigned lds0 = C.lds0;
    const int lane = C.lane, l31 = lane & 31, hi = lane >> 5, wid = C.wid;
    const int bid = C.bid, rt = (bid & 7) * 4 + ((bid >> 3) & 3), ct = bid >> 5;
    STAMP(2, 0);
    const unsigned m0r = __builtin_amdgcn_readfirstlane(*(const LAS unsigned*)((LAS char*)lds_ + 163840 - 16));
    const unsigned m0 = m0r ? m0r : 0xFFu;
    const int nAg = __builtin_popcount(m0);
    unsigned ord = 0;
    { int u = 0;
#pragma unroll
      for (int g = 0; g < 8; ++g) if ((m0 >> g) & 1) { ord |= (unsigned)g << (4 * u); ++u; }
#pragma unroll
      for (int g = 0; g < 8; ++g) if (!((m0 >> g) & 1)) { ord |= (unsigned)g << (4 * u); ++u; } }
    if (wid >= 4) {
        const int lw = wid - 4;
        const char* csrc = ws + WS_CTX + (size_t)(2 * rt + (lw >> 1)) * 32 * 8192 + (lw & 1) * 4096;
        const char* wsrc = (const char*)P.wo + ct * 128;
        unsigned voffW[4];
#pragma unroll
        for (int j = 0; j < 4; ++j) { const int row = (lw * 4 + j) * 8 + (lane >> 3);
            voffW[j] = row * 1024 + (((lane & 7) ^ (((row >> 1) & 1) << 2)) << 4); }
        const unsigned vlin = lane * 16;
        auto head = [&](int t) { return (int)(4 * ((ord >> (4 * (t >> 2))) & 7) + (t & 3)); };
        auto stageW = [&](int t, int slot) {
            const unsigned b = lds0 + slot * K3_SLOT + lw * 4096;
#pragma unroll
            for (int j = 0; j < 4; ++j) glds16(wsrc + (size_t)head(t) * 131072, voffW[j], b + 16384 + j * 1024);
        };
        auto stageC = [&](int t, int slot) { glds16x4_dev(csrc + (size_t)head(t) * 8192, vlin, lds0 + slot * K3_SLOT + lw * 4096); };
        if (!m0r) asm volatile("s_barrier" ::: "memory");
#pragma unroll
        for (int t = 0; t < 4; ++t) { stageC(t, t); stageW(t, t); }
        int slot = 4;
        for (int hh = 0; hh < 28; ++hh) {
            WAIT_BAR(24);
            stageC(hh + 4, slot); stageW(hh + 4, slot);
            slot = slot == K3_NS - 1 ? 0 : slot + 1;
        }
        WAIT_BAR(24); WAIT_BAR(16); WAIT_BAR(8); WAIT_BAR(0);
        WAIT_BAR(0);
        WAIT_BAR(0);
        return;
    }
    const int qg2 = wid >> 1, cb = wid & 1;
    const int i0 = R.sel0, i1 = R.sel1;
    const bool has0 = (i0 >> 6) == rt, has1 = (i1 >> 6) == rt;
    const int q = rt * 64 + qg2 * 32 + l31;
    const unsigned* cflags = C.sy + SY_CTX + (rt >> 1) * 32;
    if (!m0r) { if (wid == 0) wait_flags<1>(cflags, 32, C.tag, lane); asm volatile("s_barrier" ::: "memory"); }
    const float g = __uint_as_float(flag_load((const unsigned*)(ws + WS_G) + q));
    s16x4 xb[4];
#pragma unroll
    for (int q4 = 0; q4 < 4; ++q4) xb[q4] = *(const s16x4*)(P.ax + (size_t)q * DM + ct * 64 + cb * 32 + 8 * q4 + 4 * hi);
    const int tq = (lane & 15) >> 2, tp = lane & 3, tblk = (lane >> 4) & 1;
    const lptr wA = lds + 16384 + (4 * hi + tq) * 128 + ((cb ^ ((tq >> 1) & 1)) << 6) + tblk * 32 + tp * 8;
    const lptr cA = lds + qg2 * 8192 + lane * 16;
    f32x16 accs[8];
    int bo = 0;
    unsigned yf[4]; f32x4 yv4[4];
    static_for<8>([&](auto U) {
        constexpr int u = U;
        if (wid == 1) {
            if constexpr (u == 0) {
#pragma unroll
                for (int j = 0; j < 4; ++j) yf[j] = flag_load(C.sy + SY_Y + lane + 64 * j);
            } else if constexpr (u == 1) {
                if (!__all(yf[0] == C.tag && yf[1] == C.tag && yf[2] == C.tag && yf[3] == C.tag)) wait_flags<4>(C.sy + SY_Y, 256, C.tag, lane);
                const rsrc_t ypr = make_rsrc(ws + WS_YP, MOE_WGS * 4096);
#pragma unroll
                for (int j = 0; j < 4; ++j)
                    yv4[j] = __builtin_bit_cast(f32x4, __builtin_amdgcn_raw_buffer_load_b128(ypr, (lane + 64 * j) * 4096 + bid * 16, 0, 16));
            } else if constexpr (u == 3) {
                f32x4 y = (yv4[0] + yv4[1]) + (yv4[2] + yv4[3]);
#pragma unroll
                for (int e = 0; e < 4; ++e) y[e] = wave_sum(y[e], lane);
                if (lane < 4) store_f32_wt((float*)(ws + WS_Y) + bid * 4 + lane, lane == 0 ? y[0] : lane == 1 ? y[1] : lane == 2 ? y[2] : y[3]);
            } else if constexpr (u == 6) {
                asm volatile("s_waitcnt vmcnt(0)" ::: "memory");
                if (lane == 0) flag_store(C.sy + SY_Y2 + bid, C.tag);
            }
        }
        accs[u] = f32x16{};
        for (int hh = 0; hh < 4; ++hh) {
            asm volatile("s_waitcnt lgkmcnt(0)\n\ts_barrier" ::: "memory");
#pragma unroll
            for (int f = 0; f < 8; ++f) {
                const bf16x8 cf = ldsr128(cA + bo + f * 1024);
                const bf16x8 wf = cat(ldstr(wA + bo + f * 2048), ldstr(wA + bo + f * 2048 + 1024));
                accs[u] = MFMA(wf, cf, accs[u]);
            }
            bo = bo == (K3_NS - 1) * K3_SLOT ? 0 : bo + K3_SLOT;
        }
        if constexpr (u == 0) STAMP(2, 2);
        if (wid == 0 && u == nAg - 2 && nAg < 8) wait_flags<1>(cflags, 32, C.tag, lane);
    });
    WAIT_BAR(0);
    STAMP(2, 3);
    f32x16 acc;
    {
        LAS char* mine = (LAS char*)lds_ + wid * 32768 + lane * 16;
#pragma unroll
        for (int u = 0; u < 8; ++u) {
            LAS char* d = mine + ((ord >> (4 * u)) & 7) * 4096;
#pragma unroll
            for (int q4 = 0; q4 < 4; ++q4) *(LAS f32x4*)(d + q4 * 1024) = (f32x4){accs[u][4 * q4], accs[u][4 * q4 + 1], accs[u][4 * q4 + 2], accs[u][4 * q4 + 3]};
        }
#pragma unroll
        for (int q4 = 0; q4 < 4; ++q4) {
            f32x4 t = *(const LAS f32x4*)(mine + q4 * 1024);
#pragma unroll
            for (int gg = 1; gg < 8; ++gg) t += *(const LAS f32x4*)(mine + gg * 4096 + q4 * 1024);
            acc[4 * q4] = t[0]; acc[4 * q4 + 1] = t[1]; acc[4 * q4 + 2] = t[2]; acc[4 * q4 + 3] = t[3];
        }
    }
    if (has0 || has1) {
        wait_flags<1>(C.sy + SY_Y2 + ct * 16, 16, C.tag, lane);
        wait_flags<1>(C.sy + SY_Y2 + 128 + ct * 16, 16, C.tag, lane);
    }
    WAIT_BAR(0);
    {
        const bool r0 = (q == i0), r1 = (q == i1);
        const float* yv = (const float*)(ws + WS_Y) + (r1 ? 512 : 0) + ct * 64;
#pragma unroll
        for (int q4 = 0; q4 < 4; ++q4) {
            const int ml = cb * 32 + 8 * q4 + 4 * hi, m = ct * 64 + ml;
            float res[4];
#pragma unroll
            for (int e = 0; e < 4; ++e) {
                float b = bf2f((u16)xb[q4][e]);
                if (r0 || r1) b += __uint_as_float(flag_load((const unsigned*)(yv + ml + e))) * (r1 ? R.sig1 : R.sig0);
                res[e] = g * acc[4 * q4 + e] + (1.0f - g) * b;
            }
            u32x2 w2 = {cvtpk(res[0], res[1]), cvtpk(res[2], res[3])};
            *(u32x2*)(P.out + (size_t)q * DM + m) = w2;
        }
    }
    STAMP(2, 4);
}

__global__ __launch_bounds__(512, 2)
void mega_kernel(Params Parg) {
    extern __shared__ __attribute__((aligned(16))) char lds_[];
    const int wid = __builtin_amdgcn_readfirstlane(threadIdx.x >> 6);
    unsigned tag;
    Routed R;
    QPre Q;
    {
        const Params P = load_params();
        unsigned tagv;
        asm volatile("global_load_dword %0, %1, %2 sc1" : "=v"(tagv) : "v"(0), "s"((const unsigned*)(P.ws + WS_SYNC) + SY_EPOCH));
        Ctx C = make_ctx(lds_, P.ws, wid, 0u);
        phase_kv(P, C, Q, tagv);
        tag = __builtin_amdgcn_readfirstlane(C.tag);
    }
    { const Params P = load_params(); phase_attn(P, make_ctx(lds_, P.ws, wid, tag), Q, R); }
    {
        const Params P = load_params();
        const Ctx C = make_ctx(lds_, P.ws, wid, tag);
        phase_oproj(P, C, R);
        if (C.tid == 0) __hip_atomic_fetch_add(C.sy + SY_EPOCH, 1u, __ATOMIC_RELAXED, __HIP_MEMORY_SCOPE_AGENT);
    }
}

extern "C" void kernel_launch(void* const* d_in, const int* in_sizes, int n_in, void* d_out, int out_size, void* d_ws, size_t ws_size,
                              hipStream_t stream) {
    const u16* ax = (const u16*)d_in[0]; const u16* ckv = (const u16*)d_in[1]; const u16* wq = (const u16*)d_in[2];
    const u16* wuk = (const u16*)d_in[3]; const u16* wuv = (const u16*)d_in[4]; const u16* wo = (const u16*)d_in[5];
      const u16* wr = (const u16*)d_in[7]; const u16* wg = (const u16*)d_in[8];
    const u16* wu = (const u16*)d_in[9]; const u16* wd = (const u16*)d_in[10]; const u16* gw = (const u16*)d_in[11];
    char* ws = (char*)d_ws;
    (void)hipFuncSetAttribute((const void*)mega_kernel, hipFuncAttributeMaxDynamicSharedMemorySize, 163840);
    Params P{ax, ckv, wq, wuk, wuv, wo, wr, wg, wu, wd, gw, ws, (u16*)d_out};
    mega_kernel<<<256, 512, 163840, stream>>>(P);
}
```

```cpp
#include <hip/hip_runtime.h>
#include <hip/hip_bf16.h>
#include <stdint.h>
#include <utility>

typedef unsigned short u16;
typedef short bf16x8 __attribute__((ext_vector_type(8)));
typedef short s16x4 __attribute__((ext_vector_type(4)));
typedef short v4i16_t __attribute__((ext_vector_type(4)));
typedef float f32x16 __attribute__((ext_vector_type(16)));
typedef float f32x4 __attribute__((ext_vector_type(4)));
typedef unsigned u32x4 __attribute__((ext_vector_type(4)));
typedef unsigned u32x2 __attribute__((ext_vector_type(2)));
#define LAS __attribute__((address_space(3)))
#define DI __device__ __forceinline__
#define MFMA(a, b, c) __builtin_amdgcn_mfma_f32_32x32x16_bf16((a), (b), (c), 0, 0, 0)
#define SBAR() __builtin_amdgcn_sched_barrier(0)

constexpr int SEQ = 2048, DM = 512, NH = 32, HD = 128, NHD = NH * HD;
constexpr int FF = 1792;
constexpr int MOE_WGS = 224, MOE_COLS = 8;

constexpr size_t WS_KF = 0;
constexpr size_t WS_VF = WS_KF + (size_t)SEQ * NHD * 2;
constexpr size_t WS_CTX = WS_VF + (size_t)SEQ * NHD * 2;
constexpr size_t WS_R = WS_CTX + (size_t)SEQ * NHD * 2;
constexpr size_t WS_G = WS_R + SEQ * 4;
constexpr size_t WS_Y = WS_G + SEQ * 4;
constexpr size_t WS_YP = WS_Y + 4096;
constexpr size_t WS_SYNC = WS_YP + 224 * 4096;
constexpr int SY_EPOCH = 0, SY_KV = 256, SY_R = 512, SY_Y = 768, SY_Y2 = 1024, SY_CTX = 1280;

constexpr size_t WS_DBG = 128u << 20;
#define STAMP(K, i) do {} while (0)
#define SEG_DECL
#define SEG_PARAMS
#define SEG_ARGS
#define SEG_START() do {} while (0)
#define SEG(i) do {} while (0)
#define SEG_OUT(K) do {} while (0)
template <int... Is, class F> DI void static_for_(std::integer_sequence<int, Is...>, F&& f) { (f(std::integral_constant<int, Is>{}), ...); }
template <int N, class F> DI void static_for(F&& f) { static_for_(std::make_integer_sequence<int, N>{}, f); }
DI unsigned cvtpk(float lo, float hi) {
    typedef float f32x2 __attribute__((ext_vector_type(2)));
    typedef __bf16 bf16x2 __attribute__((ext_vector_type(2)));
    f32x2 v = {lo, hi}; bf16x2 b = __builtin_convertvector(v, bf16x2); return __builtin_bit_cast(unsigned, b);
}
DI float bf2f(u16 v) { return __uint_as_float((unsigned)v << 16); }
DI float bfround(float x) { return __uint_as_float(cvtpk(x, x) << 16); }
DI bf16x8 pack8(const f32x16& x, int s, float sc) {
    u32x4 w = {cvtpk(x[8 * s] * sc, x[8 * s + 1] * sc), cvtpk(x[8 * s + 2] * sc, x[8 * s + 3] * sc),
               cvtpk(x[8 * s + 4] * sc, x[8 * s + 5] * sc), cvtpk(x[8 * s + 6] * sc, x[8 * s + 7] * sc)};
    return __builtin_bit_cast(bf16x8, w);
}
DI bf16x8 pack8(const f32x16& x, int s) {
    u32x4 w = {cvtpk(x[8 * s], x[8 * s + 1]), cvtpk(x[8 * s + 2], x[8 * s + 3]),
               cvtpk(x[8 * s + 4], x[8 * s + 5]), cvtpk(x[8 * s + 6], x[8 * s + 7])};
    return __builtin_bit_cast(bf16x8, w);
}
DI void glds16(const void* sbase, unsigned voff, unsigned lds_addr) {
    lds_addr = __builtin_amdgcn_readfirstlane(lds_addr);
    asm volatile("s_mov_b32 m0, %2\n\ts_nop 0\n\tglobal_load_lds_dwordx4 %0, %1" :: "v"(voff), "s"(sbase), "s"(lds_addr) : "memory");
}
DI void glds16x4(const void* sbase, unsigned voff, unsigned lds_addr) {
    lds_addr = __builtin_amdgcn_readfirstlane(lds_addr);
    asm volatile("s_mov_b32 m0, %2\n\ts_nop 0\n\tglobal_load_lds_dwordx4 %0, %1\n\tglobal_load_lds_dwordx4 %0, %1 offset:1024\n\t"
                 "global_load_lds_dwordx4 %0, %1 offset:2048\n\tglobal_load_lds_dwordx4 %0, %1 offset:3072"
                 :: "v"(voff), "s"(sbase), "s"(lds_addr) : "memory");
}
DI unsigned flag_load(const unsigned* p) { return __hip_atomic_load(p, __ATOMIC_RELAXED, __HIP_MEMORY_SCOPE_AGENT); }
DI void flag_store(unsigned* p, unsigned v) { __hip_atomic_store(p, v, __ATOMIC_RELAXED, __HIP_MEMORY_SCOPE_AGENT); }
DI void store_f32_wt(float* p, float v) { __hip_atomic_store((unsigned*)p, __float_as_uint(v), __ATOMIC_RELAXED, __HIP_MEMORY_SCOPE_AGENT); }
template <int NW> DI void wait_flags(const unsigned* f, int n, unsigned tag, int lane) {
    const unsigned long long t0 = __builtin_amdgcn_s_memrealtime();
    for (;;) {
        bool ok = true;
#pragma unroll
        for (int j = 0; j < NW; ++j) { const int i = lane + 64 * j; ok &= (i < n ? flag_load(f + i) : tag) == tag; }
        if (__all(ok)) break;
        if (__builtin_amdgcn_s_memrealtime() - t0 > 5000000ull) break;
        __builtin_amdgcn_s_sleep(2);
    }
}
DI void acquire_agent() { __builtin_amdgcn_fence(__ATOMIC_ACQUIRE, "agent"); }
DI void glds16x4_dev(const void* sbase, unsigned voff, unsigned lds_addr) {
    lds_addr = __builtin_amdgcn_readfirstlane(lds_addr);
    asm volatile("s_mov_b32 m0, %2\n\ts_nop 0\n\tglobal_load_lds_dwordx4 %0, %1 sc1\n\tglobal_load_lds_dwordx4 %0, %1 offset:1024 sc1\n\t"
                 "global_load_lds_dwordx4 %0, %1 offset:2048 sc1\n\tglobal_load_lds_dwordx4 %0, %1 offset:3072 sc1"
                 :: "v"(voff), "s"(sbase), "s"(lds_addr) : "memory");
}
#define WAIT_BAR(N) asm volatile("s_waitcnt vmcnt(" #N ") lgkmcnt(0)\n\ts_barrier" ::: "memory")
#define WAIT_CNT(c) do { if ((c) == 8) { WAIT_BAR(8); } else if ((c) == 4) { WAIT_BAR(4); } else { WAIT_BAR(0); } } while (0)
DI int lane_now() { int l; asm volatile("v_mbcnt_lo_u32_b32 %0, -1, 0\n\tv_mbcnt_hi_u32_b32 %0, -1, %0" : "=v"(l)); return l; }
typedef LAS const char* lptr;
DI bf16x8 ldsr128(lptr p) { return *(const LAS bf16x8*)p; }
DI bf16x8 ldsr128a(lptr p) { return *(const LAS bf16x8*)p; }
DI s16x4 ldstr(lptr p) { return __builtin_bit_cast(s16x4, __builtin_amdgcn_ds_read_tr16_b64_v4i16((LAS v4i16_t*)p)); }
DI bf16x8 cat(s16x4 a, s16x4 b) { return __builtin_shufflevector(a, b, 0, 1, 2, 3, 4, 5, 6, 7); }
DI unsigned shx(unsigned v, int m, int lane) { return (unsigned)__builtin_amdgcn_ds_bpermute((lane ^ m) << 2, (int)v); }
DI float shx(float v, int m, int lane) { return __uint_as_float(shx(__float_as_uint(v), m, lane)); }
template <int CTRL> DI unsigned dppu(unsigned v) { return (unsigned)__builtin_amdgcn_update_dpp(0, (int)v, CTRL, 0xF, 0xF, true); }
template <int CTRL> DI float dppf(float v) { return __uint_as_float(dppu<CTRL>(__float_as_uint(v))); }
DI float wave_sum(float v, int lane) {
#pragma unroll
    for (int o = 32; o >= 1; o >>= 1) v += shx(v, o, lane);
    return v;
}
DI float sigmoidf_(float x) { return 1.0f / (1.0f + __expf(-x)); }

constexpr int K1_STAGE = 49152, K1_LDS = 163840;
typedef __amdgpu_buffer_rsrc_t rsrc_t;
DI rsrc_t make_rsrc(const void* p, unsigned bytes) { return __builtin_amdgcn_make_buffer_rsrc((void*)p, (short)0, (int)bytes, 0x00020000); }
DI void store_wt(rsrc_t r, unsigned off, bf16x8 v) {
    __builtin_amdgcn_raw_buffer_store_b128(__builtin_bit_cast(u32x4, v), r, (int)off, 0, 16);
}
#define WAIT_VM(N) asm volatile("s_waitcnt vmcnt(" #N ")" ::: "memory")

template <bool ISV, class Pre, class Mid, class Tail, class St>
DI void kv_body(const char* cbase, const char* kbase, const char* vbase, rsrc_t outr, unsigned outoff, lptr lds, unsigned lds0, int wid, int lane,
                Pre&& pre, Mid&& mid, Tail&& tail, St&& stamp2) {
    const int l31 = lane & 31, hi = lane >> 5;
    unsigned voffW[2], voffC[2], voffG[2];
#pragma unroll
    for (int j = 0; j < 2; ++j) {
        const int n = 2 * wid + j;
        { const int row = 4 * n + (lane >> 4); voffW[j] = row * 8192 + (((lane & 15) ^ ((row & 3) << 2)) << 4); }
        { const int row = 8 * n + (lane >> 3); voffC[j] = row * 1024 + (((lane & 7) ^ ((row >> 1) & 7)) << 4); }
        voffG[j] = (lane ^ ((wid + 8 * j) & 15)) << 4;
    }
    auto stage = [&](int s) {
        const unsigned b = lds0 + (s % 3) * K1_STAGE + 2 * wid * 1024;
#pragma unroll
        for (int j = 0; j < 2; ++j) glds16(kbase + (size_t)s * 64 * 8192, voffW[j], b + j * 1024);
#pragma unroll
        for (int j = 0; j < 2; ++j) glds16(vbase + (size_t)s * 64 * 8192, voffW[j], b + 16384 + j * 1024);
#pragma unroll
        for (int j = 0; j < 2; ++j) glds16(cbase + s * 128, voffC[j], b + 32768 + j * 1024);
    };
    auto gbuf = [&](int k) { return k == 4 ? 98304 : k == 5 ? 131072 : k == 6 ? 0 : 49152; };
    auto stageG = [&](int k) {
#pragma unroll
        for (int j = 0; j < 4; ++j) glds16(cbase + (size_t)(32 * k + wid + 8 * j) * 1024, voffG[j & 1], lds0 + gbuf(k) + (wid + 8 * j) * 1024);
    };
    const int tq = (lane & 15) >> 2, tp = lane & 3, tblk = (lane >> 4) & 1;
    const lptr wA = lds + (ISV ? 16384 : 0) + (8 * hi + tq) * 256 + (((wid & 3) ^ tq) << 6) + tblk * 32 + tp * 8;
    lptr cA[4];
#pragma unroll
    for (int ks = 0; ks < 4; ++ks) cA[ks] = lds + 32768 + l31 * 128 + (((2 * ks + hi) ^ ((l31 >> 1) & 7)) << 4);
    auto mm = [&](const bf16x8& w, const bf16x8& c, const f32x16& acc) { return ISV ? MFMA(c, w, acc) : MFMA(w, c, acc); };
    auto out_tile = [&](int kg, const f32x16& acc) {
        store_wt(outr, outoff + kg * 8192, pack8(acc, 0));
        store_wt(outr, outoff + kg * 8192 + 1024, pack8(acc, 1));
    };

    bf16x8 wf[32];
    f32x16 acc[4] = {};
    lptr gA[8];
    stage(0); stage(1); stage(2);
    pre();
    auto rdW = [&](int s, int ks) { const int bo = (s % 3) * K1_STAGE; wf[4 * s + ks] = cat(ldstr(wA + bo + ks * 4096), ldstr(wA + bo + ks * 4096 + 1024)); };
    auto rdC = [&](int s, int i) { return ldsr128(cA[i >> 2] + (s % 3) * K1_STAGE + (i & 3) * 4096); };
    auto rdG = [&](int k, int ks) { return ldsr128(gA[ks & 7] + gbuf(k) + (ks >> 3) * 256); };
    auto dmaS = [&](int s, int j) {
        const unsigned b = lds0 + (s % 3) * K1_STAGE + 2 * wid * 1024;
        if (j < 2) glds16(kbase + (size_t)s * 64 * 8192, voffW[j], b + j * 1024);
        else if (j < 4) glds16(vbase + (size_t)s * 64 * 8192, voffW[j - 2], b + 16384 + (j - 2) * 1024);
        else glds16(cbase + s * 128, voffC[j - 4], b + 32768 + (j - 4) * 1024);
    };
    auto dmaG = [&](int k, int j) { glds16(cbase + (size_t)(32 * k + wid + 8 * j) * 1024, voffG[j & 1], lds0 + gbuf(k) + (wid + 8 * j) * 1024); };
    bf16x8 cf[8];
    WAIT_BAR(12);
    rdW(0, 0);
#pragma unroll
    for (int i = 0; i < 4; ++i) cf[i] = rdC(0, i);
    static_for<128>([&](auto SI) {
        {
            constexpr int s = SI / 16, i = SI % 16;
            if (i == 12) {
                if (s < 6) { WAIT_BAR(6); } else if (s == 6) { WAIT_BAR(8); } else { WAIT_BAR(8); }
            }
            if (s == 7 && i == 11) {
                const int ln = lane_now();
#pragma unroll
                for (int j = 0; j < 8; ++j) gA[j] = lds + (ln & 31) * 1024 + (((2 * j + (ln >> 5)) ^ (ln & 15)) << 4);
            }
            if (s == 1 && i == 13) mid();
            const int nx = i + 4;
            if (nx < 16) { if ((nx & 3) == 0) rdW(s, nx >> 2); cf[nx & 7] = rdC(s, nx); }
            else if (s < 7) { if (nx == 16) rdW(s + 1, 0); cf[nx & 7] = rdC(s + 1, nx - 16); }
            else cf[nx & 7] = rdG(4, nx - 16);
            acc[i & 3] = mm(wf[4 * s + (i >> 2)], cf[i & 7], acc[i & 3]);
            if (s + 3 < 8) { if (i == 12 || i == 14) dmaS(s + 3, (i - 12) / 2); }
            else if (i >= 12) {
                const int j = i - 12;
                if (s == 5) dmaG(4, j);
                else if (s == 6) dmaG(6, j);
                else dmaG(7, j);
            }
            if (i < 12 && i % 3 == 0) { if (s >= 1 && s + 2 < 8) dmaS(s + 2, 2 + i / 3); }
            if (s == 6 && i >= 2 && i < 10 && !(i & 1)) dmaG(5, (i - 2) / 2);
            if (s == 7 && i == 15) out_tile(0, acc[0]);
            SBAR();
        }
    });
    stamp2();
    f32x16 a2[2];
    static_for<128>([&](auto KI) {
        {
            constexpr int k = 4 + KI / 32, ks = KI % 32;
            if (ks == 28 && k < 7) {
                if (k == 4) { WAIT_BAR(16); } else if (k == 5) { WAIT_BAR(16); } else { WAIT_BAR(21); }
            }
            const int nx = ks + 4;
            if (nx < 32) cf[nx & 7] = rdG(k, nx); else if (k < 7) cf[nx & 7] = rdG(k + 1, nx - 32);
            if (ks == 0) a2[k & 1] = mm(wf[0], cf[0], f32x16{}); else a2[k & 1] = mm(wf[ks], cf[ks & 7], a2[k & 1]);
            if constexpr (k == 4) { if constexpr (ks < 3) out_tile(ks + 1, acc[(ks + 1) & 3]); }
            else if constexpr (ks == 3) out_tile(k - 1, a2[(k - 1) & 1]);
            tail(KI);
            SBAR();
        }
    });
    out_tile(7, a2[1]);
}

DI void ld_u16(unsigned& d, const void* sbase, unsigned voff) { asm volatile("global_load_ushort %0, %1, %2" : "=v"(d) : "v"(voff), "s"(sbase)); }
DI void ld_x4(u32x4& d, const void* sbase, unsigned voff) { asm volatile("global_load_dwordx4 %0, %1, %2" : "=v"(d) : "v"(voff), "s"(sbase)); }
DI void ld_u16_nop(unsigned& d, const void* sbase, unsigned voff) { asm volatile("s_nop 4\n\tglobal_load_ushort %0, %1, %2" : "=v"(d) : "v"(voff), "s"(sbase)); }
DI void ld_x4_nop(u32x4& d, const void* sbase, unsigned voff) { asm volatile("s_nop 4\n\tglobal_load_dwordx4 %0, %1, %2" : "=v"(d) : "v"(voff), "s"(sbase)); }
struct Params {
    const u16 *ax, *ckv, *wq, *wuk, *wuv, *wo, *wr, *wg, *wu, *wd, *gw;
    char* ws; u16* out;
};
struct Ctx { lptr lds; char* lds_; unsigned lds0; int tid, lane, wid, bid; unsigned tag; unsigned* sy; };
typedef __attribute__((address_space(4))) const Params* kparams_t;
DI Params load_params() {
    kparams_t pp = (kparams_t)__builtin_amdgcn_kernarg_segment_ptr();
    asm volatile("" : "+s"(pp));
    Params P;
    P.ax = pp->ax; P.ckv = pp->ckv; P.wq = pp->wq; P.wuk = pp->wuk; P.wuv = pp->wuv; P.wo = pp->wo; P.wr = pp->wr; P.wg = pp->wg; P.wu = pp->wu;
    P.wd = pp->wd; P.gw = pp->gw; P.ws = pp->ws; P.out = pp->out;
    return P;
}
DI Ctx make_ctx(char* lds_, char* ws, int wid, unsigned tag) {
    Ctx C;
    C.lds = (lptr)lds_; C.lds_ = lds_; C.lds0 = (unsigned)(uintptr_t)C.lds;
    int lane; asm volatile("v_mbcnt_lo_u32_b32 %0, -1, 0\n\tv_mbcnt_hi_u32_b32 %0, -1, %0" : "=v"(lane));
    asm volatile("" : "+s"(wid), "+s"(tag));
    C.lane = lane; C.wid = wid; C.tid = wid * 64 + lane; C.bid = blockIdx.x; C.tag = tag; C.sy = (unsigned*)(ws + WS_SYNC);
    return C;
}

#define GU_K 5
#define GU_KS 8
constexpr int QST = 49152;
struct QPre { unsigned voffX[4], voffW[2]; u32x4 rv, wgv, wuv; };
constexpr int MOE_WD = 147456 + 2048;
DI int moe_f0(int bid) { return ((bid & 7) * 28 + ((bid >> 3) < 28 ? (bid >> 3) : 27)) * MOE_COLS; }
DI void q_dma(const QPre& Q, const Params& P, const Ctx& C, int s, int j) {
    const int wid = C.wid, h = (C.bid & 7) * 4 + ((C.bid >> 3) & 3), bi = C.bid >> 5;
    const char* xbase = (const char*)P.ax + (size_t)(((wid >> 2) ? 15 - bi : bi) * 128 + (wid & 3) * 32) * 1024;
    const char* wbase = (const char*)P.wq + h * 256;
    const unsigned b = C.lds0 + ((s + 2) % 3) * QST;
    if (j < 2) glds16(wbase + (size_t)s * 64 * 8192, Q.voffW[j], b + 32768 + (2 * wid + j) * 1024);
    else glds16(xbase + s * 128, Q.voffX[j - 2], b + (4 * wid + j - 2) * 1024);
}
DI void phase_kv(const Params& P, Ctx& C, QPre& Q, unsigned tagv) {
    char* ws = P.ws;
    const int lane = C.lane, wid = C.wid, bid = C.bid, h = (bid & 7) * 4 + ((bid >> 3) & 3), p = bid >> 5;
    STAMP(0, 0);
    const int tok = bid * 8 + wid;
    const bf16x8 bxv = *(const bf16x8*)(P.ax + tok * DM + lane * 8), wrv = *(const bf16x8*)(P.wr + lane * 8);
    const bf16x8 gwv = *(const bf16x8*)(P.gw + lane * 8);
    auto pre = [&]() {
        float sr = 0.f, sg = 0.f;
#pragma unroll
        for (int j = 0; j < 8; ++j) { sr += bf2f((u16)bxv[j]) * bf2f((u16)wrv[j]); sg += bf2f((u16)bxv[j]) * bf2f((u16)gwv[j]); }
        sr = wave_sum(sr, lane); sg = wave_sum(sg, lane);
        { unsigned t;
          asm volatile("s_waitcnt vmcnt(18)\n\tv_readfirstlane_b32 %0, %1" : "=s"(t) : "v"(tagv));
          C.tag = t + 1u; }
        if (lane == 0) { store_f32_wt((float*)(ws + WS_R) + tok, bfround(sr)); store_f32_wt((float*)(ws + WS_G) + tok, sigmoidf_(sg)); }
        STAMP(0, 1);
    };
    auto mid = [&]() { if (C.tid == 0) flag_store(C.sy + SY_R + bid, C.tag); };
    const char* cbase = (const char*)P.ckv + (size_t)p * 256 * 1024;
    const char* kbase = (const char*)P.wuk + h * 256;
    const char* vbase = (const char*)P.wuv + h * 256;
    const unsigned outoff = (unsigned)((h * 64 + 8 * p) * 8 + 2 * (wid & 3)) * 1024 + lane * 16;
    const unsigned kvbytes = (unsigned)((size_t)SEQ * NHD * 2);
    unsigned rp[4];
    auto tail = [&](auto KI) {
        constexpr int k = 4 + KI / 32, ks = KI % 32;
        if constexpr (k == 4 && ks == 20) {
            const int ln = lane_now();
#pragma unroll
            for (int j = 0; j < 4; ++j) { const int r = 8 * j + (ln >> 3); Q.voffX[j] = r * 1024 + (((ln & 7) ^ ((r >> 1) & 7)) << 4); }
#pragma unroll
            for (int j = 0; j < 2; ++j) { const int row = 4 * (2 * wid + j) + (ln >> 4); Q.voffW[j] = row * 8192 + (((ln & 15) ^ ((row & 3) << 2)) << 4); }
            if (wid == 0) {
#pragma unroll
                for (int j = 0; j < 4; ++j) asm volatile("global_load_dword %0, %1, %2 sc1" : "=v"(rp[j]) : "v"(ln * 4), "s"(C.sy + SY_R + 64 * j));
            }
        }
        if constexpr (k == GU_K && ks == GU_KS) ld_x4(Q.wgv, (const char*)P.wg + moe_f0(bid) * 2, (wid * 64 + lane_now()) * (FF * 2));
        if constexpr (k == GU_K && ks == GU_KS + 1) ld_x4(Q.wuv, (const char*)P.wu + moe_f0(bid) * 2, (wid * 64 + lane_now()) * (FF * 2));
        if constexpr (k == 6 && ks == 8) glds16((const char*)P.wd + (size_t)(moe_f0(bid) + wid) * 1024, lane_now() * 16, C.lds0 + MOE_WD + wid * 1024);
        if constexpr (k == 5 && ks >= 28) q_dma(Q, P, C, 0, ks - 28);
        if constexpr (k == 6 && ks < 2) q_dma(Q, P, C, 0, 4 + ks);
        if constexpr (k == 6 && ks == 20) {
            if (wid == 0) {
                asm volatile("s_waitcnt vmcnt(13)" : "+v"(rp[0]), "+v"(rp[1]), "+v"(rp[2]), "+v"(rp[3]));
                if (__builtin_expect(!__all(rp[0] == C.tag && rp[1] == C.tag && rp[2] == C.tag && rp[3] == C.tag), 0)) wait_flags<4>(C.sy + SY_R, 256, C.tag, lane_now());
            }
        }
        if constexpr (k == 6 && ks >= 28) q_dma(Q, P, C, 1, ks - 28);
        if constexpr (k == 7 && ks < 2) q_dma(Q, P, C, 1, 4 + ks);
        if constexpr (k == 7 && ks == 5) asm volatile("global_load_dwordx4 %0, %1, %2 sc1" : "=v"(Q.rv) : "v"((wid * 64 + lane_now()) * 16), "s"(P.ws + WS_R));
    };
    auto st2 = [&]() { STAMP(0, 2); };
    if (wid < 4) kv_body<false>(cbase, kbase, vbase, make_rsrc(ws + WS_KF, kvbytes), outoff, C.lds, C.lds0, wid, lane, pre, mid, tail, st2);
    else         kv_body<true>(cbase, kbase, vbase, make_rsrc(ws + WS_VF, kvbytes), outoff, C.lds, C.lds0, wid, lane, pre, mid, tail, st2);
    STAMP(0, 3);
}

DI unsigned umax_(unsigned a, unsigned b) { return a > b ? a : b; }
DI unsigned umin_(unsigned a, unsigned b) { return a < b ? a : b; }
DI float lo16(unsigned v) { return __uint_as_float(v << 16); }
DI float hi16(unsigned v) { return __uint_as_float(v & 0xFFFF0000u); }

constexpr int K2_QST = 49152, K2_SLOT = 32768, K2_QB = 131072, K2_LDS = 163840;
constexpr float QSCALE = 0.08838834764831845f * 1.4426950408889634f;
constexpr float THR = 8.0f;
constexpr int RING0 = 3;

#define SETPRIO(x) __builtin_amdgcn_s_setprio(x)
struct SMState { float mhat, l; f32x16 negm; };
#define PIN(x) asm volatile("" : "+v"(x))
DI float max3(float a, float b, float c) { return fmaxf(fmaxf(a, b), c); }

#define EXP2(x) __builtin_amdgcn_exp2f(x)
#define VOFF(i) (((i) >> 3) * 8192 + (2 * ((i) & 3) + (((i) >> 2) & 1)) * 1024)
#define KOFF(i) (((i) & 1) * 8192 + ((i) >> 1) * 1024)
DI float fadd_s(float a, float b) { float r; asm("v_add_f32_e32 %0, %1, %2" : "=v"(r) : "v"(a), "v"(b)); return r; }
DI float max3_s(float a, float b, float c) { float r; asm("v_max3_f32 %0, %1, %2, %3" : "=v"(r) : "v"(a), "v"(b), "v"(c)); return r; }
DI float max2_s(float a, float b) { float r; asm("v_max_f32_e32 %0, %1, %2" : "=v"(r) : "v"(a), "v"(b)); return r; }
template <int OFF> DI void glds_step(const void* sbase, unsigned voff, unsigned lds_addr) {
    if (OFF == 0) asm volatile("s_mov_b32 m0, %2\n\ts_nop 0\n\tglobal_load_lds_dwordx4 %0, %1 sc1" :: "v"(voff), "s"(sbase), "s"(lds_addr));
    else asm volatile("global_load_lds_dwordx4 %0, %1 offset:%c2 sc1" :: "v"(voff), "s"(sbase), "i"(OFF));
}
DI f32x16 mfma_c(bf16x8 a, bf16x8 b, const f32x16& c) {
    f32x16 d; asm volatile("v_mfma_f32_32x32x16_bf16 %0, %1, %2, %3" : "=&v"(d) : "v"(a), "v"(b), "v"(c)); return d;
}
struct DmaJob { const char* src0; const char* src1; unsigned dst0, dst1; bool has0, has1; };

#define LA 4
template <bool QK, bool FIN, class Mid>
DI void attn_A(lptr kp, const lptr& vpn, const bf16x8 (&qf)[8], const f32x16& negm, const bf16x8 (&kpre)[LA], bf16x8 (&vpre)[LA], f32x16& s0, f32x16& s1,
               const f32x16& p0, const f32x16& p1, bf16x8 (&pf)[4], float& l, const DmaJob& dj, unsigned vlin, Mid&& mid) {
    bf16x8 fr[16];
    unsigned pw[16];
    float sa = 0.f, sb = 0.f;
#pragma unroll
    for (int i = 0; i < LA; ++i) fr[i] = kpre[i];
#pragma unroll
    for (int i = 0; i < 16; ++i) {
        if (QK && i + LA < 16) fr[i + LA] = ldsr128a(kp + KOFF(i + LA));
        if (i + LA >= 16) vpre[i + LA - 16] = ldsr128a(vpn + VOFF(i + LA - 16));
        if (QK) {
            if (i == 0) s0 = mfma_c(fr[0], qf[0], negm);
            else if (i == 1) s1 = mfma_c(fr[1], qf[0], negm);
            else if (i & 1) s1 = MFMA(fr[i], qf[i >> 1], s1);
            else s0 = MFMA(fr[i], qf[i >> 1], s0);
        }
        if (FIN) {
            const float e0 = i < 8 ? p0[2 * i] : p1[2 * i - 16], e1 = i < 8 ? p0[2 * i + 1] : p1[2 * i - 15];
            if (i == 0) { sa = e0; sb = e1; } else { sa = fadd_s(sa, e0); sb = fadd_s(sb, e1); PIN(sa); PIN(sb); }
            pw[i] = cvtpk(e0, e1);
            PIN(pw[i]);
        }
        if (i == 3) mid();
        if (QK) {
            if (i == 1 && dj.has0) { glds_step<0>(dj.src0, vlin, dj.dst0); glds_step<1024>(dj.src0, vlin, dj.dst0); }
            if (i == 5 && dj.has0) { glds_step<2048>(dj.src0, vlin, dj.dst0); glds_step<3072>(dj.src0, vlin, dj.dst0); }
            if (i == 9 && dj.has1) { glds_step<0>(dj.src1, vlin, dj.dst1); glds_step<1024>(dj.src1, vlin, dj.dst1); }
            if (i == 13 && dj.has1) { glds_step<2048>(dj.src1, vlin, dj.dst1); glds_step<3072>(dj.src1, vlin, dj.dst1); }
        }
        SBAR();
    }
    if (FIN) {
        l += sa + sb;
#pragma unroll
        for (int i = 0; i < 4; ++i) { u32x4 w = {pw[4 * i], pw[4 * i + 1], pw[4 * i + 2], pw[4 * i + 3]}; pf[i] = __builtin_bit_cast(bf16x8, w); }
    }
}
DI void dma_plain(const DmaJob& dj, unsigned vlin) {
    if (dj.has0) glds16x4_dev(dj.src0, vlin, dj.dst0);
    if (dj.has1) glds16x4_dev(dj.src1, vlin, dj.dst1);
}
template <bool PV, bool SM, class Mid>
DI void attn_B(lptr vp, const lptr& kpn, f32x16 (&o)[4], const bf16x8 (&pf)[4], const bf16x8 (&vpre)[LA], bf16x8 (&kpre)[LA],
               f32x16& s0, f32x16& s1, SMState& st, bool first, bool mask, int qrel  , bool& pend, float& fpend, Mid&& mid) {
    bf16x8 fr[16];
    float ma = 0.f, mb = 0.f, mc = 0.f;
#pragma unroll
    for (int i = 0; i < LA; ++i) fr[i] = vpre[i];
    if (SM && __builtin_expect(mask, 0)) {
        const float NEG = -__builtin_inff();
#pragma unroll
        for (int r = 0; r < 16; ++r) { const int c = (r & 3) + 8 * (r >> 2);
            if (c > qrel) s0[r] = NEG;
            if (c + 32 > qrel) s1[r] = NEG; }
    }
    SETPRIO(1);
    static_for<16>([&](auto I) {
        constexpr int i = I;
        if (PV && i + LA < 16) fr[i + LA] = ldsr128a(vp + VOFF(i + LA));
        if (i + LA >= 16) kpre[i + LA - 16] = ldsr128a(kpn + KOFF(i + LA - 16));
        if (PV) o[i & 3] = MFMA(fr[i], pf[i >> 2], o[i & 3]);
        if (SM) {
            if (i == 0) { ma = max3(s0[0], s0[1], s0[2]); mb = max3(s1[0], s1[1], s1[2]); mc = max3(s0[3], s0[4], s1[3]); PIN(ma); PIN(mb); PIN(mc); }
            if (i == 1) { ma = max3_s(ma, s0[5], s0[6]); mb = max3_s(mb, s1[4], s1[5]); mc = max3_s(mc, s0[7], s1[6]); PIN(ma); PIN(mb); PIN(mc); }
            if (i == 2) { ma = max3_s(ma, s0[8], s0[9]); mb = max3_s(mb, s1[7], s1[8]); mc = max3_s(mc, s0[10], s1[9]); PIN(ma); PIN(mb); PIN(mc); }
            if (i == 3) { ma = max3_s(ma, s0[11], s0[12]); mb = max3_s(mb, s1[10], s1[11]); mc = max3_s(mc, s0[13], s1[12]); PIN(ma); PIN(mb); PIN(mc); }
            if (i == 4) { ma = max3_s(ma, s0[14], s0[15]); mb = max3_s(mb, s1[13], s1[14]); mc = max2_s(mc, s1[15]); PIN(ma); PIN(mb); PIN(mc); }
            if (i == 5) {
                float rm = max3_s(ma, mb, mc);
                auto rr = __builtin_amdgcn_permlane32_swap(__float_as_uint(rm), __float_as_uint(rm), false, false);
                rm = max2_s(__uint_as_float(rr[0]), __uint_as_float(rr[1]));
                if (__builtin_expect(first || __any(rm > THR), 0)) {
                    const float dl = first ? rm : fmaxf(rm, 0.f), f = first ? 1.0f : __builtin_amdgcn_exp2f(-dl);
                    st.mhat += dl; st.l *= f;
#pragma unroll
                    for (int r = 0; r < 16; ++r) { s0[r] -= dl; s1[r] -= dl; st.negm[r] = -st.mhat; }
                    pend = !first; fpend = f;
                }
            }
            if (i >= 6) {
                constexpr int e0 = i < 14 ? 3 * (i - 6) : 24 + 4 * (i - 14), e1 = i < 14 ? e0 + 3 : e0 + 4;
#pragma unroll
                for (int e = e0; e < e1; ++e) { if (e < 16) { s0[e] = EXP2(s0[e]); } else { s1[e - 16] = EXP2(s1[e - 16]); } }
                PIN(s0); PIN(s1);
            }
        }
        if (i == 7) mid();
        SBAR();
    });
    SETPRIO(0);
}

struct Routed { int sel0, sel1; float sig0, sig1; };
DI void phase_attn(const Params& P, const Ctx& C, QPre& Q, Routed& R) {
    char* ws = P.ws; char* lds_ = C.lds_;
    const u16 *x = P.ax, *wq = P.wq, *wo = P.wo, *bx = P.ax, *wg = P.wg, *wu = P.wu, *wd = P.wd;
    const lptr lds = C.lds;
    const unsigned lds0 = C.lds0;
    const int tid = C.tid, lane = C.lane, l31 = lane & 31, hi = lane >> 5, wid = C.wid;
    const int bid = C.bid, h = (bid & 7) * 4 + ((bid >> 3) & 3), bi = bid >> 5;
    const int grp = wid >> 2, rg = wid & 3;
    const int blkA = bi, blkB = 15 - bi;

    STAMP(1, 0);
    LAS unsigned* sk = (LAS unsigned*)((LAS char*)lds_ + 147456);
    LAS float* hs = (LAS float*)((LAS char*)lds_ + 147456 + 128);
    LAS float* red = (LAS float*)((LAS char*)lds_ + 147456 + 256);
    const int cg = moe_f0(bid) / MOE_COLS;
    unsigned k1 = 0, k2 = 0, xs0, xs1;
    u32x4 skv[4];
    float mv[32], ma0, ma1, hsv[16];
    unsigned wdv[8];
    auto top2 = [&](unsigned b1, unsigned b2) { k2 = umax_(umin_(k1, b1), umax_(k2, b2)); k1 = umax_(k1, b1); };
    auto ins = [&](unsigned key) { k2 = umax_(k2, umin_(k1, key)); k1 = umax_(k1, key); };
    auto moe_gap = [&](auto GI) {
        constexpr int G = GI, s = G / 16, i = G % 16;
        if constexpr (G == 13 || G == 14) {
            if constexpr (G == 13) asm volatile("" : "+v"(Q.rv));
#pragma unroll
            for (int j = 2 * (G - 13); j < 2 * (G - 13) + 2; ++j) {
                const unsigned u = Q.rv[j] >> 16;
                const unsigned o = (u & 0x8000u) ? (~u & 0xFFFFu) : (u | 0x8000u);
                ins((o << 16) | (unsigned)(0xFFFF - (tid * 4 + j)));
            }
            PIN(k1); PIN(k2);
        }
        if constexpr (G == 15) { top2(dppu<0x128>(k1), dppu<0x128>(k2)); PIN(k1); PIN(k2); }
        if constexpr (G == 16) { top2(dppu<0x124>(k1), dppu<0x124>(k2)); PIN(k1); PIN(k2); }
        if constexpr (G == 17) { top2(dppu<0x122>(k1), dppu<0x122>(k2)); PIN(k1); PIN(k2); }
        if constexpr (G == 18) { top2(dppu<0x121>(k1), dppu<0x121>(k2)); PIN(k1); PIN(k2); }
        if constexpr (G == 19) { auto a = __builtin_amdgcn_permlane16_swap(k1, k1, false, false); auto b = __builtin_amdgcn_permlane16_swap(k2, k2, false, false);
            k1 = a[0]; k2 = b[0]; top2(a[1], b[1]); PIN(k1); PIN(k2); }
        if constexpr (G == 20) { auto a = __builtin_amdgcn_permlane32_swap(k1, k1, false, false); auto b = __builtin_amdgcn_permlane32_swap(k2, k2, false, false);
            k1 = a[0]; k2 = b[0]; top2(a[1], b[1]);
            if (lane == 0) { sk[wid * 2] = k1; sk[wid * 2 + 1] = k2; } }
        if constexpr (G == 29) {
#pragma unroll
            for (int j = 0; j < 4; ++j) skv[j] = *(const LAS u32x4*)(sk + 4 * j);
            k1 = 0; k2 = 0;
        }
        if constexpr (G == 30 || G == 31) {
#pragma unroll
            for (int j = 8 * (G - 30); j < 8 * (G - 30) + 8; j += 2) top2(skv[j >> 2][j & 3], skv[j >> 2][(j & 3) + 1]);
            PIN(k1); PIN(k2);
        }
        if constexpr (G == 32) {
            k1 = __builtin_amdgcn_readfirstlane(k1); k2 = __builtin_amdgcn_readfirstlane(k2);
            R.sel0 = 0xFFFF - (int)(k1 & 0xFFFFu); R.sel1 = 0xFFFF - (int)(k2 & 0xFFFFu);
            ld_u16_nop(xs0, (const char*)bx + R.sel0 * 1024, tid * 2);
            ld_u16_nop(xs1, (const char*)bx + R.sel1 * 1024, tid * 2);
        }
        if constexpr (G == 33) {
            const unsigned o0 = k1 >> 16, o1 = k2 >> 16;
            const float r0 = __uint_as_float(((o0 & 0x8000u) ? (o0 & 0x7FFFu) : (~o0 & 0xFFFFu)) << 16);
            const float r1 = __uint_as_float(((o1 & 0x8000u) ? (o1 & 0x7FFFu) : (~o1 & 0xFFFFu)) << 16);
            R.sig0 = __uint_as_float(__builtin_amdgcn_readfirstlane(__float_as_uint(sigmoidf_(r0))));
            R.sig1 = __uint_as_float(__builtin_amdgcn_readfirstlane(__float_as_uint(sigmoidf_(r1))));
        }
        if constexpr (G >= 61 && G <= 64) {
            if constexpr (G == 61) { asm volatile("" : "+v"(xs0), "+v"(xs1), "+v"(Q.wgv), "+v"(Q.wuv)); ma0 = lo16(xs0); ma1 = lo16(xs1); }
            constexpr int c = 2 * (G - 61);
            const float g0 = lo16(Q.wgv[c >> 1]), g1 = hi16(Q.wgv[c >> 1]), u0 = lo16(Q.wuv[c >> 1]), u1 = hi16(Q.wuv[c >> 1]);
            mv[c] = ma0 * g0; mv[c + 1] = ma0 * g1; mv[8 + c] = ma0 * u0; mv[9 + c] = ma0 * u1;
            mv[16 + c] = ma1 * g0; mv[17 + c] = ma1 * g1; mv[24 + c] = ma1 * u0; mv[25 + c] = ma1 * u1;
            PIN(mv[c]); PIN(mv[c + 1]); PIN(mv[8 + c]); PIN(mv[9 + c]); PIN(mv[16 + c]); PIN(mv[17 + c]); PIN(mv[24 + c]); PIN(mv[25 + c]);
        }
        if constexpr (G >= 65 && G <= 68) {
#pragma unroll
            for (int j = 4 * (G - 65); j < 4 * (G - 65) + 4; ++j) { auto r = __builtin_amdgcn_permlane32_swap(__float_as_uint(mv[j]), __float_as_uint(mv[j + 16]), false, false);
                mv[j] = __uint_as_float(r[0]) + __uint_as_float(r[1]); PIN(mv[j]); }
        }
        if constexpr (G == 69 || G == 70) {
#pragma unroll
            for (int j = 4 * (G - 69); j < 4 * (G - 69) + 4; ++j) { auto r = __builtin_amdgcn_permlane16_swap(__float_as_uint(mv[j]), __float_as_uint(mv[j + 8]), false, false);
                mv[j] = __uint_as_float(r[0]) + __uint_as_float(r[1]); PIN(mv[j]); }
        }
        if constexpr (G == 71) { const bool up_ = lane & 8;
#pragma unroll
            for (int j = 0; j < 4; ++j) { const float send = up_ ? mv[j] : mv[j + 4], keep = up_ ? mv[j + 4] : mv[j]; mv[j] = keep + dppf<0x128>(send); PIN(mv[j]); } }
        if constexpr (G == 72) { const bool up_ = lane & 4;
#pragma unroll
            for (int j = 0; j < 2; ++j) { const float send = up_ ? mv[j] : mv[j + 2], keep = up_ ? mv[j + 2] : mv[j]; mv[j] = keep + dppf<0x1B>(dppf<0x141>(send)); PIN(mv[j]); } }
        if constexpr (G == 73) { const bool up_ = lane & 2;
            const float send = up_ ? mv[0] : mv[1], keep = up_ ? mv[1] : mv[0]; mv[0] = keep + dppf<0x4E>(send);
            mv[0] += dppf<0xB1>(mv[0]);
            if (!(lane & 1)) red[wid * 32 + (lane >> 1)] = mv[0];
        }
        if constexpr (G == 77) {
            if (tid < 16) {
                const int c = tid & 7, t = tid >> 3;
                float g = 0.f, u = 0.f;
#pragma unroll
                for (int w = 0; w < 8; ++w) { g += red[w * 32 + 16 * t + c]; u += red[w * 32 + 16 * t + 8 + c]; }
                g = bfround(g); u = bfround(u);
                hs[t * 8 + c] = bfround(bfround(g * sigmoidf_(g)) * u);
            }
        }
        if constexpr (G == 93) {
#pragma unroll
            for (int j = 0; j < 8; ++j) wdv[j] = *(const LAS u16*)((LAS char*)lds_ + MOE_WD + j * 1024 + tid * 2);
#pragma unroll
            for (int j = 0; j < 4; ++j) { const f32x4 t = *(const LAS f32x4*)(hs + 4 * j); hsv[4 * j] = t[0]; hsv[4 * j + 1] = t[1]; hsv[4 * j + 2] = t[2]; hsv[4 * j + 3] = t[3]; }
        }
        if constexpr (G == 95) {
            float y0 = 0.f, y1 = 0.f;
#pragma unroll
            for (int j = 0; j < 8; ++j) { const float w = lo16(wdv[j]); y0 += hsv[j] * w; y1 += hsv[8 + j] * w; }
            float* yp = (float*)(ws + WS_YP) + (size_t)cg * 1024;
            store_f32_wt(yp + tid, y0); store_f32_wt(yp + 512 + tid, y1);
        }
        (void)s; (void)i;
    };

    const char* Kh = ws + WS_KF + (size_t)h * 64 * 8192;
    const unsigned vlin = lane * 16;
    bf16x8 qf[8];
    {
        lptr xA[4];
#pragma unroll
        for (int ks = 0; ks < 4; ++ks) xA[ks] = lds + (32 * wid + l31) * 128 + (((2 * ks + hi) ^ ((l31 >> 1) & 7)) << 4);
        const int tq = (lane & 15) >> 2, tp = lane & 3, tblk = (lane >> 4) & 1;
        lptr wA[4];
#pragma unroll
        for (int c = 0; c < 4; ++c) wA[c] = lds + 32768 + (8 * hi + tq) * 256 + ((c ^ tq) << 6) + tblk * 32 + tp * 8;
        auto dmaS = [&](int s, int j) { q_dma(Q, P, C, s, j); };
        auto rdW = [&](int s, int i) { const int o = ((s + 2) % 3) * QST + (i >> 2) * 4096; return cat(ldstr(wA[i & 3] + o), ldstr(wA[i & 3] + o + 1024)); };
        auto rdX = [&](int s, int ks) { return ldsr128(xA[ks] + ((s + 2) % 3) * QST); };
        f32x16 acc[4] = {};
        bf16x8 wfr[8], xfr[2];
        unsigned kvf = 0;
        WAIT_BAR(14);
        STAMP(1, 1);
        xfr[0] = rdX(0, 0);
#pragma unroll
        for (int i = 0; i < 4; ++i) wfr[i] = rdW(0, i);
        static_for<128>([&](auto SI) {
            {
                constexpr int s = SI / 16, i = SI % 16;
                if (i == 12 && s < 7) {
                    if (s == 0) { WAIT_BAR(8); } else if (s == 1) { WAIT_BAR(6); } else if (s == 2) { WAIT_BAR(8); } else if (s < 6) { WAIT_BAR(6); } else { WAIT_BAR(0); }
                }
                const int nx = i + 4;
                if (nx < 16) { if ((nx & 3) == 0) xfr[(nx >> 2) & 1] = rdX(s, nx >> 2); wfr[nx & 7] = rdW(s, nx); }
                else if (s < 7) { if (nx == 16) xfr[0] = rdX(s + 1, 0); wfr[nx & 7] = rdW(s + 1, nx - 16); }
                acc[i & 3] = MFMA(wfr[i & 7], xfr[(i >> 2) & 1], acc[i & 3]);
                if (i == 12 || i == 14) { if (s + 3 < 8) dmaS(s + 3, (i - 12) / 2); }
                else if (s == 0) { if (i < 6) dmaS(2, i); }
                else if (i < 12 && i % 3 == 0) { if (s + 2 < 8) dmaS(s + 2, 2 + i / 3); }
                if (s == 6 && grp == 0) {
                    if (i == 12) glds16x4_dev(Kh + rg * 4096, vlin, lds0 + RING0 * 16384 + rg * 4096);
                    if (i == 14) glds16x4_dev(Kh + 16384 + rg * 4096, vlin, lds0 + (RING0 + 2) * 16384 + rg * 4096);
                }
                moe_gap(SI);
                if (s == 1 && i == 13 && tid == 0) flag_store(C.sy + SY_KV + h * 8 + bi, C.tag);
                if (s == 4 && i == 3 && wid == 0)
                    asm volatile("global_load_dword %0, %1, %2 sc1" : "=v"(kvf) : "v"((lane & 7) * 4), "s"(C.sy + SY_KV + h * 8));
                if (s == 5 && i == 13 && wid == 0) {
                    asm volatile("" : "+v"(kvf));
                    if (__builtin_expect(!__all(kvf == C.tag), 0)) wait_flags<1>(C.sy + SY_KV + h * 8, 8, C.tag, lane);

                }
                if (s == 6 && i == 13 && tid == 0) flag_store(C.sy + SY_Y + bid, C.tag);
                SBAR();
            }
        });
#pragma unroll
        for (int c = 0; c < 4; ++c) { qf[2 * c] = pack8(acc[c], 0, QSCALE); qf[2 * c + 1] = pack8(acc[c], 1, QSCALE); }
    }

    STAMP(1, 2);
    const int n1 = 2 * bi + 2;
    auto tile_of = [&](int n, int g) { return n < n1 ? n : n1 + 2 * (n - n1) + g; };
    auto jstar = [&](int tn) { return tn <= 2 * n1 ? tn : tn <= 2 * n1 + 2 ? tn + 1 : 2 * tn - 2 * n1 - 2; };
    auto jg1 = [&](int tn) { return tn == 2 * n1 + 1 ? tn : 2 * tn - 2 * n1 - 1; };
    int tb_d0, tb_d1, tb_kv, tb_rel;
    auto make_job = [&](int tn, int& d0, int& d1) {
        const int n = tn >> 1;
        int a_off, b_off; bool a_ok, b_ok;
        const int kb = (int)WS_KF + h * 64 * 8192 + rg * 4096, vb = (int)WS_VF + h * 64 * 8192 + rg * 4096;
        if (tn & 1) { a_ok = n >= 1 && n <= 17; a_off = vb + tile_of(n - 1, 0) * 16384; b_ok = n >= n1 && n <= 16; b_off = kb + tile_of(n, 1) * 16384; }
        else        { a_ok = n <= 16;           a_off = kb + tile_of(n, 0) * 16384;     b_ok = n - 2 >= n1 && n <= 18; b_off = vb + tile_of(n - 2, 1) * 16384; }
        const int da = a_off | ((jstar(tn) + RING0) & 7) | 8, db = b_off | ((jg1(tn) + RING0) & 7) | 8;
        d0 = a_ok ? da : b_ok ? db : 0; d1 = (a_ok && b_ok) ? db : 0;
    };
    {
        const int n = lane < 19 ? lane : 18;
        make_job(2 * n + grp + 3, tb_d0, tb_d1);
        const int ks = ((n < n1 || grp == 0 ? jstar(2 * n) : jg1(2 * n + 1)) + RING0) & 7;
        const int vs = ((n < n1 || grp == 0 ? jstar(2 * n + 3) : jg1(2 * n + 4)) + RING0) & 7;
        tb_kv = ks | vs << 8;
        tb_rel = 128 * ((grp || n >= n1) ? blkB : blkA) + 32 * rg - 64 * tile_of(n, grp);
    }
    auto job_from = [&](int d0, int d1) -> DmaJob {
        DmaJob d;
        d.has0 = d0 & 8; d.has1 = d1 & 8;
        d.src0 = ws + (unsigned)(d0 & ~4095); d.src1 = ws + (unsigned)(d1 & ~4095);
        d.dst0 = lds0 + (d0 & 7) * 16384 + rg * 4096; d.dst1 = lds0 + (d1 & 7) * 16384 + rg * 4096;
        return d;
    };
    auto job = [&](int n) { return job_from(__builtin_amdgcn_readlane(tb_d0, n), __builtin_amdgcn_readlane(tb_d1, n)); };
    if (grp == 1) {
#pragma unroll
        for (int c = 0; c < 8; ++c) *(LAS bf16x8*)((LAS char*)lds_ + K2_QB + rg * 8192 + c * 1024 + lane * 16) = qf[c];
    }
    f32x16 o[4] = {};
    SMState st; st.mhat = 0.f; st.l = 0.f; st.negm = f32x16{};
    const lptr kp0 = lds + lane * 16;
    char* ctx = ws + WS_CTX;
    const rsrc_t ctxr = make_rsrc(ctx, (unsigned)((size_t)SEQ * NHD * 2));
    auto store_ctx = [&](int blk, int d, const f32x16& v, float inv) {
        const unsigned off = (unsigned)(((4 * blk + rg) * 32 + h) * 8 + 2 * d) * 1024 + lane * 16;
        store_wt(ctxr, off, pack8(v, 0, inv));
        store_wt(ctxr, off + 1024, pack8(v, 1, inv));
    };
    auto half_sum = [&](float v) { auto rr = __builtin_amdgcn_permlane32_swap(__float_as_uint(v), __float_as_uint(v), false, false);
                                   return __uint_as_float(rr[0]) + __uint_as_float(rr[1]); };
    auto kslot = [&](int n) { return kp0 + (__builtin_amdgcn_readlane(tb_kv, n) & 7) * 16384; };
    auto vslot = [&](int n) { return kp0 + ((__builtin_amdgcn_readlane(tb_kv, n) >> 8) & 7) * 16384; };
    WAIT_BAR(0);
    bool first = true, pend = false;
    float invA = 0.f, fpend = 1.f;
    f32x16 sA0, sA1, sB0, sB1; bf16x8 pf[4], vpre[LA], kpre[LA];
#pragma unroll
    for (int i = 0; i < LA; ++i) kpre[i] = ldsr128a(kslot(0) + KOFF(i));
    if (grp == 1) { WAIT_BAR(0); }
    SEG_DECL; SEG_START();
    const int nsw = grp == 0 ? n1 - 1 : 100;
    auto headB = [&](int n) {
        if (__builtin_expect(n == nsw + 1, 0)) { invA = 1.0f / half_sum(st.l); st.mhat = 0.f; st.l = 0.f; first = true; }
    };
    auto tailB = [&](int n) {
        first = false;
        if (__builtin_expect(pend || (unsigned)(n - nsw) < 3u, 0)) {
            if (n == nsw + 2 && tid == 0) flag_store(C.sy + SY_CTX + blkA * 32 + h, C.tag);
            if (pend) {
#pragma unroll
                for (int d = 0; d < 4; ++d)
#pragma unroll
                    for (int r = 0; r < 16; ++r) o[d][r] *= fpend;
                pend = false;
            }
            if (n == nsw + 1) {
#pragma unroll
                for (int d = 0; d < 4; ++d) store_ctx(blkA, d, o[d], invA);
#pragma unroll
                for (int d = 0; d < 4; ++d) o[d] = f32x16{};
            }
            if (n == nsw) {
#pragma unroll
                for (int c = 0; c < 8; ++c) qf[c] = ldsr128(lds + K2_QB + rg * 8192 + c * 1024 + lane * 16);
                st.negm = f32x16{};
            }
        }
    };
#define BARRIER_L() asm volatile("s_waitcnt lgkmcnt(0)\n\ts_barrier" ::: "memory")
    lptr kcur = kslot(0), vcur = kp0;
    DmaJob dj = job(0);
    int rel = 0;
    auto pinjob = [&]() { asm volatile("" : "+s"(dj.src0), "+s"(dj.src1), "+s"(dj.dst0), "+s"(dj.dst1)); };
    auto midA = [&](int n) { vcur = vslot(n > 0 ? n - 1 : 0); rel = __builtin_amdgcn_readlane(tb_rel, n); asm volatile("" : "+v"(vcur), "+s"(rel)); };
    auto midB = [&](int n) { kcur = kslot(n + 1); dj = job(n + 1); asm volatile("" : "+v"(kcur)); pinjob(); };
#define RELARGS first, rel < 63, rel + l31 - 4 * hi, pend, fpend
    attn_A<true, false>(kcur, vcur, qf, st.negm, kpre, vpre, sA0, sA1, sA0, sA1, pf, st.l, dj, vlin, [&]() { midA(0); });
    BARRIER_L();
    attn_B<false, true>(vcur, kcur, o, pf, vpre, kpre, sA0, sA1, st, RELARGS, [&]() { midB(0); });
    tailB(0);
    WAIT_BAR(0);
#define STEP(n, C0, C1, P0, P1) do { \
        attn_A<true, true>(kcur, vcur, qf, st.negm, kpre, vpre, C0, C1, P0, P1, pf, st.l, dj, vlin, [&]() { midA(n); }); \
        SEG(0); \
        BARRIER_L(); \
        SEG(2); \
        headB(n); \
        attn_B<true, true>(vcur, kcur, o, pf, vpre, kpre, C0, C1, st, RELARGS, [&]() { midB(n); }); \
        tailB(n); \
        SEG(3); \
        if ((n) < 16) { WAIT_BAR(0); } else { BARRIER_L(); }     \
        SEG(4); \
    } while (0)
    unsigned pfd;
    for (int n = 1; n < 17; n += 2) {
        STEP(n, sB0, sB1, sA0, sA1);
        STEP(n + 1, sA0, sA1, sB0, sB1);
    }
#undef STEP
    asm volatile("global_load_dword %0, %1, %2" : "=v"(pfd) : "v"(vlin * 8),
                 "s"((const char*)wo + (size_t)(wid < 2 ? bid * 2 + wid : (bid >> 3) * 4 + ((wid - 2) & 3)) * 8192));
    attn_A<false, true>(kcur, vcur, qf, st.negm, kpre, vpre, sB0, sB1, sA0, sA1, pf, st.l, dj, vlin, [&]() { midA(17); });
    BARRIER_L();
    attn_B<true, false>(vcur, kcur, o, pf, vpre, kpre, sB0, sB1, st, false, false, 0, pend, fpend, [&]() {});
    BARRIER_L();
    SEG_OUT(1);
    if (grp == 0) { BARRIER_L(); }
    STAMP(1, 3);
    unsigned cfl = 0;
    const int ort = (bid & 7) * 4 + ((bid >> 3) & 3);
    if (wid == 0) asm volatile("global_load_dword %0, %1, %2 sc1" : "=v"(cfl) : "v"((lane & 31) * 4), "s"(C.sy + SY_CTX + (ort >> 1) * 32));
    BARRIER_L();
    {
        const float lsum = half_sum(st.l);
        LAS char* xch = (LAS char*)lds_;
        LAS char* mine = xch + wid * 8192;
        auto put = [&](int dd, const f32x16& v) {
#pragma unroll
            for (int q4 = 0; q4 < 4; ++q4)
                *(LAS f32x4*)(mine + (dd * 4 + q4) * 1024 + lane * 16) = (f32x4){v[4 * q4], v[4 * q4 + 1], v[4 * q4 + 2], v[4 * q4 + 3]};
        };
        if (grp) { put(0, o[0]); put(1, o[1]); } else { put(0, o[2]); put(1, o[3]); }
        LAS float* ml = (LAS float*)(xch + 65536);
        ml[wid * 128 + lane] = st.mhat; ml[wid * 128 + 64 + lane] = lsum;
        BARRIER_L();
        const int ow = wid ^ 4;
        const float m2 = ml[ow * 128 + lane], l2 = ml[ow * 128 + 64 + lane];
        const float mm = fmaxf(st.mhat, m2);
        const float f1 = __builtin_amdgcn_exp2f(st.mhat - mm), f2 = __builtin_amdgcn_exp2f(m2 - mm);
        const float inv = 1.0f / (lsum * f1 + l2 * f2);
        const float c1 = f1 * inv, c2 = f2 * inv;
        const LAS char* theirs = xch + ow * 8192;
        auto fin = [&](int dd, f32x16 v) {
#pragma unroll
            for (int q4 = 0; q4 < 4; ++q4) {
                const f32x4 t = *(const LAS f32x4*)(theirs + (dd * 4 + q4) * 1024 + lane * 16);
#pragma unroll
                for (int e = 0; e < 4; ++e) v[4 * q4 + e] = v[4 * q4 + e] * c1 + t[e] * c2;
            }
            store_ctx(blkB, 2 * grp + dd, v, 1.0f);
        };
        if (grp) { fin(0, o[2]); fin(1, o[3]); } else { fin(0, o[0]); fin(1, o[1]); }
    }
    asm volatile("s_waitcnt vmcnt(0)" : "+v"(pfd), "+v"(cfl));
    if (wid == 0) {
        const unsigned bal = (unsigned)__ballot(cfl == C.tag);
        unsigned m0 = 0;
#pragma unroll
        for (int g = 0; g < 8; ++g) m0 |= (((bal >> (4 * g)) & 15u) == 15u ? 1u : 0u) << g;
        if (__builtin_popcount(m0) < 2) m0 = 0;
        if (lane == 0) *(LAS unsigned*)((LAS char*)lds_ + 163840 - 16) = m0;
    }
    BARRIER_L();
    if (tid == 0) { flag_store(C.sy + SY_CTX + blkB * 32 + h, C.tag); flag_store(C.sy + SY_CTX + blkA * 32 + h, C.tag); }
    STAMP(1, 4);
}

constexpr int K3_SLOT = 32768, K3_NS = 5;
DI void phase_oproj(const Params& P, const Ctx& C, const Routed& R) {
    char* ws = P.ws; char* lds_ = C.lds_;
    const lptr lds = C.lds;
    const unsigned lds0 = C.lds0;
    const int lane = C.lane, l31 = lane & 31, hi = lane >> 5, wid = C.wid;
    const int bid = C.bid, rt = (bid & 7) * 4 + ((bid >> 3) & 3), ct = bid >> 5;
    STAMP(2, 0);
    const unsigned m0r = __builtin_amdgcn_readfirstlane(*(const LAS unsigned*)((LAS char*)lds_ + 163840 - 16));
    const unsigned m0 = m0r ? m0r : 0xFFu;
    const int nAg = __builtin_popcount(m0);
    const int rot = 0;
    unsigned ord = 0;
    { int u = 0;
#pragma unroll
      for (int k = 0; k < 8; ++k) { const int g = (k + rot) & 7; if ((m0 >> g) & 1) { ord |= (unsigned)g << (4 * u); ++u; } }
#pragma unroll
      for (int k = 0; k < 8; ++k) { const int g = (k + rot) & 7; if (!((m0 >> g) & 1)) { ord |= (unsigned)g << (4 * u); ++u; } } }
    if (wid >= 4) {
        const int lw = wid - 4;
        const char* csrc = ws + WS_CTX + (size_t)(2 * rt + (lw >> 1)) * 32 * 8192 + (lw & 1) * 4096;
        const char* wsrc = (const char*)P.wo + ct * 128;
        unsigned voffW[4];
#pragma unroll
        for (int j = 0; j < 4; ++j) { const int row = (lw * 4 + j) * 8 + (lane >> 3);
            voffW[j] = row * 1024 + (((lane & 7) ^ (((row >> 1) & 1) << 2)) << 4); }
        const unsigned vlin = lane * 16;
        auto head = [&](int t) { return (int)(4 * ((ord >> (4 * (t >> 2))) & 7) + (t & 3)); };
        auto stageW = [&](int t, int slot) {
            const unsigned b = lds0 + slot * K3_SLOT + lw * 4096;
#pragma unroll
            for (int j = 0; j < 4; ++j) glds16(wsrc + (size_t)head(t) * 131072, voffW[j], b + 16384 + j * 1024);
        };
        auto stageC = [&](int t, int slot) { glds16x4_dev(csrc + (size_t)head(t) * 8192, vlin, lds0 + slot * K3_SLOT + lw * 4096); };
        if (!m0r) asm volatile("s_barrier" ::: "memory");
#pragma unroll
        for (int t = 0; t < 4; ++t) { stageC(t, t); stageW(t, t); }
        int slot = 4;
        for (int hh = 0; hh < 28; ++hh) {
            WAIT_BAR(24);
            stageC(hh + 4, slot); stageW(hh + 4, slot);
            slot = slot == K3_NS - 1 ? 0 : slot + 1;
        }
        WAIT_BAR(24); WAIT_BAR(16); WAIT_BAR(8); WAIT_BAR(0);
        return;
    }
    const int qg2 = wid >> 1, cb = wid & 1;
    const int i0 = R.sel0, i1 = R.sel1;
    const bool has0 = (i0 >> 6) == rt, has1 = (i1 >> 6) == rt;
    const int q = rt * 64 + qg2 * 32 + l31;
    const unsigned* cflags = C.sy + SY_CTX + (rt >> 1) * 32;
    if (!m0r) { if (wid == 0) wait_flags<1>(cflags, 32, C.tag, lane); asm volatile("s_barrier" ::: "memory"); }
    const float g = __uint_as_float(flag_load((const unsigned*)(ws + WS_G) + q));
    s16x4 xb[4];
#pragma unroll
    for (int q4 = 0; q4 < 4; ++q4) xb[q4] = *(const s16x4*)(P.ax + (size_t)q * DM + ct * 64 + cb * 32 + 8 * q4 + 4 * hi);
    const int tq = (lane & 15) >> 2, tp = lane & 3, tblk = (lane >> 4) & 1;
    const lptr wA = lds + 16384 + (4 * hi + tq) * 128 + ((cb ^ ((tq >> 1) & 1)) << 6) + tblk * 32 + tp * 8;
    const lptr cA = lds + qg2 * 8192 + lane * 16;
    f32x16 accs[8] = {};
    int bo = 0;
    auto run4 = [&](f32x16& acc) {
#pragma unroll 1
        for (int hh = 0; hh < 4; ++hh) {
            asm volatile("s_waitcnt lgkmcnt(0)\n\ts_barrier" ::: "memory");
            bf16x8 cf[8], wf[8];
#pragma unroll
            for (int f = 0; f < 4; ++f) { wf[f] = cat(ldstr(wA + bo + f * 2048), ldstr(wA + bo + f * 2048 + 1024)); cf[f] = ldsr128(cA + bo + f * 1024); }
            SBAR();
#pragma unroll
            for (int f = 0; f < 8; ++f) {
                acc = MFMA(wf[f], cf[f], acc);
                if (f + 4 < 8) { wf[f + 4] = cat(ldstr(wA + bo + (f + 4) * 2048), ldstr(wA + bo + (f + 4) * 2048 + 1024)); cf[f + 4] = ldsr128(cA + bo + (f + 4) * 1024); }
                SBAR();
            }
            bo = bo == (K3_NS - 1) * K3_SLOT ? 0 : bo + K3_SLOT;
        }
    };
    auto slots = [&](int u0, int u1) {
        for (int u = u0; u < u1; ++u) {
            switch ((ord >> (4 * u)) & 7) {
                case 0: run4(accs[0]); break; case 1: run4(accs[1]); break; case 2: run4(accs[2]); break; case 3: run4(accs[3]); break;
                case 4: run4(accs[4]); break; case 5: run4(accs[5]); break; case 6: run4(accs[6]); break; default: run4(accs[7]); break;
            }
            if (wid == 0 && u == nAg - 2 && nAg < 8) wait_flags<1>(cflags, 32, C.tag, lane);
        }
    };
    unsigned yf[4]; f32x4 yv4[4];
    if (wid == 1) {
#pragma unroll
        for (int j = 0; j < 4; ++j) yf[j] = flag_load(C.sy + SY_Y + lane + 64 * j);
    }
    slots(0, 1);
    STAMP(2, 2);
    if (wid == 1) {
        if (!__all(yf[0] == C.tag && yf[1] == C.tag && yf[2] == C.tag && yf[3] == C.tag)) wait_flags<4>(C.sy + SY_Y, 256, C.tag, lane);
        const rsrc_t ypr = make_rsrc(ws + WS_YP, MOE_WGS * 4096);
#pragma unroll
        for (int j = 0; j < 4; ++j)
            yv4[j] = __builtin_bit_cast(f32x4, __builtin_amdgcn_raw_buffer_load_b128(ypr, (lane + 64 * j) * 4096 + bid * 16, 0, 16));
    }
    slots(1, 3);
    if (wid == 1) {
        f32x4 y = (yv4[0] + yv4[1]) + (yv4[2] + yv4[3]);
#pragma unroll
        for (int e = 0; e < 4; ++e) y[e] = wave_sum(y[e], lane);
        if (lane < 4) store_f32_wt((float*)(ws + WS_Y) + bid * 4 + lane, lane == 0 ? y[0] : lane == 1 ? y[1] : lane == 2 ? y[2] : y[3]);
    }
    slots(3, 6);
    if (wid == 1) {
        asm volatile("s_waitcnt vmcnt(0)" ::: "memory");
        if (lane == 0) flag_store(C.sy + SY_Y2 + bid, C.tag);
    }
    slots(6, 8);
    STAMP(2, 3);
    f32x16 acc = accs[0];
#pragma unroll
    for (int gg = 1; gg < 8; ++gg) acc += accs[gg];
    if (has0 || has1) {
        wait_flags<1>(C.sy + SY_Y2 + ct * 16, 16, C.tag, lane);
        wait_flags<1>(C.sy + SY_Y2 + 128 + ct * 16, 16, C.tag, lane);
    }
    {
        const bool r0 = (q == i0), r1 = (q == i1);
        const float* yv = (const float*)(ws + WS_Y) + (r1 ? 512 : 0) + ct * 64;
        u32x2 w2[4];
#pragma unroll
        for (int q4 = 0; q4 < 4; ++q4) {
            const int ml = cb * 32 + 8 * q4 + 4 * hi;
            float res[4];
#pragma unroll
            for (int e = 0; e < 4; ++e) {
                float b = bf2f((u16)xb[q4][e]);
                if (r0 || r1) b += __uint_as_float(flag_load((const unsigned*)(yv + ml + e))) * (r1 ? R.sig1 : R.sig0);
                res[e] = g * acc[4 * q4 + e] + (1.0f - g) * b;
            }
            w2[q4] = (u32x2){cvtpk(res[0], res[1]), cvtpk(res[2], res[3])};
        }
        const rsrc_t outr = make_rsrc(P.out, SEQ * DM * 2);
#pragma unroll
        for (int k = 0; k < 4; k += 2) {
            auto rx = __builtin_amdgcn_permlane32_swap(w2[k][0], w2[k + 1][0], false, false);
            auto ry = __builtin_amdgcn_permlane32_swap(w2[k][1], w2[k + 1][1], false, false);
            const u32x4 v = {rx[0], ry[0], rx[1], ry[1]};
            __builtin_amdgcn_raw_buffer_store_b128(v, outr, (q * DM + ct * 64 + cb * 32 + 8 * k + 8 * hi) * 2, 0, 16);
        }
    }
    STAMP(2, 4);
}

__global__ __launch_bounds__(512, 2)
void mega_kernel(Params Parg) {
    extern __shared__ __attribute__((aligned(16))) char lds_[];
    const int wid = __builtin_amdgcn_readfirstlane(threadIdx.x >> 6);
    unsigned tag;
    Routed R;
    QPre Q;
    {
        const Params P = load_params();
        unsigned tagv;
        asm volatile("global_load_dword %0, %1, %2 sc1" : "=v"(tagv) : "v"(0), "s"((const unsigned*)(P.ws + WS_SYNC) + SY_EPOCH));
        Ctx C = make_ctx(lds_, P.ws, wid, 0u);
        phase_kv(P, C, Q, tagv);
        tag = __builtin_amdgcn_readfirstlane(C.tag);
    }
    { const Params P = load_params(); phase_attn(P, make_ctx(lds_, P.ws, wid, tag), Q, R); }
    {
        const Params P = load_params();
        const Ctx C = make_ctx(lds_, P.ws, wid, tag);
        phase_oproj(P, C, R);
        if (C.tid == 0) __hip_atomic_fetch_add(C.sy + SY_EPOCH, 1u, __ATOMIC_RELAXED, __HIP_MEMORY_SCOPE_AGENT);
    }
}

extern "C" void kernel_launch(void* const* d_in, const int* in_sizes, int n_in, void* d_out, int out_size, void* d_ws, size_t ws_size,
                              hipStream_t stream) {
    const u16* ax = (const u16*)d_in[0]; const u16* ckv = (const u16*)d_in[1]; const u16* wq = (const u16*)d_in[2];
    const u16* wuk = (const u16*)d_in[3]; const u16* wuv = (const u16*)d_in[4]; const u16* wo = (const u16*)d_in[5];
      const u16* wr = (const u16*)d_in[7]; const u16* wg = (const u16*)d_in[8];
    const u16* wu = (const u16*)d_in[9]; const u16* wd = (const u16*)d_in[10]; const u16* gw = (const u16*)d_in[11];
    char* ws = (char*)d_ws;
    (void)hipFuncSetAttribute((const void*)mega_kernel, hipFuncAttributeMaxDynamicSharedMemorySize, 163840);
    Params P{ax, ckv, wq, wuk, wuv, wo, wr, wg, wu, wd, gw, ws, (u16*)d_out};
    mega_kernel<<<256, 512, 163840, stream>>>(P);
}
```

```cpp
#include <hip/hip_runtime.h>
#include <hip/hip_bf16.h>
#include <stdint.h>
#include <utility>

typedef unsigned short u16;
typedef short bf16x8 __attribute__((ext_vector_type(8)));
typedef short s16x4 __attribute__((ext_vector_type(4)));
typedef short v4i16_t __attribute__((ext_vector_type(4)));
typedef float f32x16 __attribute__((ext_vector_type(16)));
typedef float f32x4 __attribute__((ext_vector_type(4)));
typedef unsigned u32x4 __attribute__((ext_vector_type(4)));
typedef unsigned u32x2 __attribute__((ext_vector_type(2)));
#define LAS __attribute__((address_space(3)))
#define DI __device__ __forceinline__
#define MFMA(a, b, c) __builtin_amdgcn_mfma_f32_32x32x16_bf16((a), (b), (c), 0, 0, 0)
#define MFMA16(a, b, c) __builtin_amdgcn_mfma_f32_16x16x32_bf16((a), (b), (c), 0, 0, 0)
#define SBAR() __builtin_amdgcn_sched_barrier(0)

constexpr int SEQ = 2048, DM = 512, NH = 32, HD = 128, NHD = NH * HD;
constexpr int FF = 1792;
constexpr int MOE_WGS = 224, MOE_COLS = 8;

constexpr size_t WS_KF = 0;
constexpr size_t WS_VF = WS_KF + (size_t)SEQ * NHD * 2;
constexpr size_t WS_CTX = WS_VF + (size_t)SEQ * NHD * 2;
constexpr size_t WS_R = WS_CTX + (size_t)SEQ * NHD * 2;
constexpr size_t WS_G = WS_R + SEQ * 4;
constexpr size_t WS_Y = WS_G + SEQ * 4;
constexpr size_t WS_YP = WS_Y + 4096;
constexpr size_t WS_SYNC = WS_YP + 224 * 4096;
constexpr int SY_EPOCH = 0, SY_KV = 256, SY_R = 512, SY_Y = 768, SY_Y2 = 1024, SY_CTX = 1280;

constexpr size_t WS_DBG = 128u << 20;
#define STAMP(K, i) do {} while (0)
#define SEG_DECL
#define SEG_PARAMS
#define SEG_ARGS
#define SEG_START() do {} while (0)
#define SEG(i) do {} while (0)
#define SEG_OUT(K) do {} while (0)
template <int... Is, class F> DI void static_for_(std::integer_sequence<int, Is...>, F&& f) { (f(std::integral_constant<int, Is>{}), ...); }
template <int N, class F> DI void static_for(F&& f) { static_for_(std::make_integer_sequence<int, N>{}, f); }
DI unsigned cvtpk(float lo, float hi) {
    typedef float f32x2 __attribute__((ext_vector_type(2)));
    typedef __bf16 bf16x2 __attribute__((ext_vector_type(2)));
    f32x2 v = {lo, hi}; bf16x2 b = __builtin_convertvector(v, bf16x2); return __builtin_bit_cast(unsigned, b);
}
DI float bf2f(u16 v) { return __uint_as_float((unsigned)v << 16); }
DI float bfround(float x) { return __uint_as_float(cvtpk(x, x) << 16); }
DI bf16x8 pack8(const f32x16& x, int s, float sc) {
    u32x4 w = {cvtpk(x[8 * s] * sc, x[8 * s + 1] * sc), cvtpk(x[8 * s + 2] * sc, x[8 * s + 3] * sc),
               cvtpk(x[8 * s + 4] * sc, x[8 * s + 5] * sc), cvtpk(x[8 * s + 6] * sc, x[8 * s + 7] * sc)};
    return __builtin_bit_cast(bf16x8, w);
}
DI bf16x8 pack8(const f32x16& x, int s) {
    u32x4 w = {cvtpk(x[8 * s], x[8 * s + 1]), cvtpk(x[8 * s + 2], x[8 * s + 3]),
               cvtpk(x[8 * s + 4], x[8 * s + 5]), cvtpk(x[8 * s + 6], x[8 * s + 7])};
    return __builtin_bit_cast(bf16x8, w);
}
DI void glds16(const void* sbase, unsigned voff, unsigned lds_addr) {
    lds_addr = __builtin_amdgcn_readfirstlane(lds_addr);
    asm volatile("s_mov_b32 m0, %2\n\ts_nop 0\n\tglobal_load_lds_dwordx4 %0, %1" :: "v"(voff), "s"(sbase), "s"(lds_addr) : "memory");
}
DI void glds16x4(const void* sbase, unsigned voff, unsigned lds_addr) {
    lds_addr = __builtin_amdgcn_readfirstlane(lds_addr);
    asm volatile("s_mov_b32 m0, %2\n\ts_nop 0\n\tglobal_load_lds_dwordx4 %0, %1\n\tglobal_load_lds_dwordx4 %0, %1 offset:1024\n\t"
                 "global_load_lds_dwordx4 %0, %1 offset:2048\n\tglobal_load_lds_dwordx4 %0, %1 offset:3072"
                 :: "v"(voff), "s"(sbase), "s"(lds_addr) : "memory");
}
DI unsigned flag_load(const unsigned* p) { return __hip_atomic_load(p, __ATOMIC_RELAXED, __HIP_MEMORY_SCOPE_AGENT); }
DI void flag_store(unsigned* p, unsigned v) { __hip_atomic_store(p, v, __ATOMIC_RELAXED, __HIP_MEMORY_SCOPE_AGENT); }
DI void store_f32_wt(float* p, float v) { __hip_atomic_store((unsigned*)p, __float_as_uint(v), __ATOMIC_RELAXED, __HIP_MEMORY_SCOPE_AGENT); }
template <int NW> DI void wait_flags(const unsigned* f, int n, unsigned tag, int lane) {
    const unsigned long long t0 = __builtin_amdgcn_s_memrealtime();
    for (;;) {
        bool ok = true;
#pragma unroll
        for (int j = 0; j < NW; ++j) { const int i = lane + 64 * j; ok &= (i < n ? flag_load(f + i) : tag) == tag; }
        if (__all(ok)) break;
        if (__builtin_amdgcn_s_memrealtime() - t0 > 5000000ull) break;
        __builtin_amdgcn_s_sleep(2);
    }
}
DI void acquire_agent() { __builtin_amdgcn_fence(__ATOMIC_ACQUIRE, "agent"); }
DI void glds16x4_dev(const void* sbase, unsigned voff, unsigned lds_addr) {
    lds_addr = __builtin_amdgcn_readfirstlane(lds_addr);
    asm volatile("s_mov_b32 m0, %2\n\ts_nop 0\n\tglobal_load_lds_dwordx4 %0, %1 sc1\n\tglobal_load_lds_dwordx4 %0, %1 offset:1024 sc1\n\t"
                 "global_load_lds_dwordx4 %0, %1 offset:2048 sc1\n\tglobal_load_lds_dwordx4 %0, %1 offset:3072 sc1"
                 :: "v"(voff), "s"(sbase), "s"(lds_addr) : "memory");
}
#define WAIT_BAR(N) asm volatile("s_waitcnt vmcnt(" #N ") lgkmcnt(0)\n\ts_barrier" ::: "memory")
#define WAIT_CNT(c) do { if ((c) == 8) { WAIT_BAR(8); } else if ((c) == 4) { WAIT_BAR(4); } else { WAIT_BAR(0); } } while (0)
DI int lane_now() { int l; asm volatile("v_mbcnt_lo_u32_b32 %0, -1, 0\n\tv_mbcnt_hi_u32_b32 %0, -1, %0" : "=v"(l)); return l; }
typedef LAS const char* lptr;
DI bf16x8 ldsr128(lptr p) { return *(const LAS bf16x8*)p; }
DI bf16x8 ldsr128a(lptr p) { return *(const LAS bf16x8*)p; }
DI s16x4 ldstr(lptr p) { return __builtin_bit_cast(s16x4, __builtin_amdgcn_ds_read_tr16_b64_v4i16((LAS v4i16_t*)p)); }
DI bf16x8 cat(s16x4 a, s16x4 b) { return __builtin_shufflevector(a, b, 0, 1, 2, 3, 4, 5, 6, 7); }
DI unsigned shx(unsigned v, int m, int lane) { return (unsigned)__builtin_amdgcn_ds_bpermute((lane ^ m) << 2, (int)v); }
DI float shx(float v, int m, int lane) { return __uint_as_float(shx(__float_as_uint(v), m, lane)); }
template <int CTRL> DI unsigned dppu(unsigned v) { return (unsigned)__builtin_amdgcn_update_dpp(0, (int)v, CTRL, 0xF, 0xF, true); }
template <int CTRL> DI float dppf(float v) { return __uint_as_float(dppu<CTRL>(__float_as_uint(v))); }
DI float wave_sum(float v, int lane) {
#pragma unroll
    for (int o = 32; o >= 1; o >>= 1) v += shx(v, o, lane);
    return v;
}
DI float sigmoidf_(float x) { return 1.0f / (1.0f + __expf(-x)); }

constexpr int K1_STAGE = 49152, K1_LDS = 163840;
typedef __amdgpu_buffer_rsrc_t rsrc_t;
DI rsrc_t make_rsrc(const void* p, unsigned bytes) { return __builtin_amdgcn_make_buffer_rsrc((void*)p, (short)0, (int)bytes, 0x00020000); }
DI void store_wt(rsrc_t r, unsigned off, bf16x8 v) {
    __builtin_amdgcn_raw_buffer_store_b128(__builtin_bit_cast(u32x4, v), r, (int)off, 0, 16);
}
#define WAIT_VM(N) asm volatile("s_waitcnt vmcnt(" #N ")" ::: "memory")

template <bool ISV, class Pre, class Mid, class Tail, class St>
DI void kv_body(const char* cbase, const char* kbase, const char* vbase, rsrc_t outr, unsigned outoff, lptr lds, unsigned lds0, int wid, int lane,
                Pre&& pre, Mid&& mid, Tail&& tail, St&& stamp2) {
    const int r16 = lane & 15, q4 = lane >> 4;
    unsigned voffW[2], voffC[2], voffG[2];
#pragma unroll
    for (int j = 0; j < 2; ++j) {
        const int n = 2 * wid + j;
        { const int row = 4 * n + (lane >> 4); voffW[j] = row * 8192 + (((lane & 15) ^ ((row & 3) << 2) ^ (((row >> 3) & 1) << 1)) << 4); }
        { const int row = 8 * n + (lane >> 3); voffC[j] = row * 1024 + (((lane & 7) ^ ((row >> 1) & 7)) << 4); }
        voffG[j] = (lane ^ ((wid + 8 * j) & 15)) << 4;
    }
    auto stage = [&](int s) {
        const unsigned b = lds0 + (s % 3) * K1_STAGE + 2 * wid * 1024;
#pragma unroll
        for (int j = 0; j < 2; ++j) glds16(kbase + (size_t)s * 64 * 8192, voffW[j], b + j * 1024);
#pragma unroll
        for (int j = 0; j < 2; ++j) glds16(vbase + (size_t)s * 64 * 8192, voffW[j], b + 16384 + j * 1024);
#pragma unroll
        for (int j = 0; j < 2; ++j) glds16(cbase + s * 128, voffC[j], b + 32768 + j * 1024);
    };
    auto gbuf = [&](int k) { return k == 4 ? 98304 : k == 5 ? 131072 : k == 6 ? 0 : 49152; };
    const int tq = (lane & 15) >> 2, tp = lane & 3;
    lptr wA[2], cA[2];
#pragma unroll
    for (int b = 0; b < 2; ++b) {
        wA[b] = lds + (ISV ? 16384 : 0) + (8 * q4 + tq) * 256 + (((wid & 3) ^ tq) << 6) + ((b ^ (q4 & 1)) << 5) + tp * 8;
        cA[b] = lds + 32768 + r16 * 128 + (((4 * b + q4) ^ (r16 >> 1)) << 4);
    }
    auto mm = [&](const bf16x8& w, const bf16x8& c, const f32x4& acc) { return ISV ? MFMA16(c, w, acc) : MFMA16(w, c, acc); };
    const unsigned olane = outoff + (q4 >> 1) * 1024 + (ISV ? (r16 + 32 * (q4 & 1)) * 16 : (16 * (r16 >> 3) + (r16 & 7) + 32 * (q4 & 1)) * 16);
    auto out_tile = [&](int kg, const f32x4& t00, const f32x4& t01, const f32x4& t10, const f32x4& t11) {
        auto pk = [](const f32x4& x, const f32x4& y) { u32x4 w = {cvtpk(x[0], x[1]), cvtpk(x[2], x[3]), cvtpk(y[0], y[1]), cvtpk(y[2], y[3])}; return __builtin_bit_cast(bf16x8, w); };
        if (ISV) { store_wt(outr, olane + kg * 8192, pk(t00, t10)); store_wt(outr, olane + kg * 8192 + 256, pk(t01, t11)); }
        else     { store_wt(outr, olane + kg * 8192, pk(t00, t01)); store_wt(outr, olane + kg * 8192 + 128, pk(t10, t11)); }
    };

    bf16x8 wf[16][2];
    f32x4 acc[8][2] = {};
    lptr gA[4];
    stage(0); stage(1); stage(2);
    pre();
    auto rdW = [&](int s, int sl) { const int o = (s % 3) * K1_STAGE + sl * 8192;
#pragma unroll
        for (int b = 0; b < 2; ++b) wf[2 * s + sl][b] = cat(ldstr(wA[b] + o), ldstr(wA[b] + o + 1024)); };
    auto rdC = [&](int s, int i) { return ldsr128(cA[i >> 3] + (s % 3) * K1_STAGE + (i & 7) * 2048); };
    auto rdG = [&](int k, int ks) { return ldsr128(gA[(ks >> 1) & 3] + gbuf(k) + (ks & 1) * 16384 + (ks >> 3) * 256); };
    auto dmaS = [&](int s, int j) {
        const unsigned b = lds0 + (s % 3) * K1_STAGE + 2 * wid * 1024;
        if (j < 2) glds16(kbase + (size_t)s * 64 * 8192, voffW[j], b + j * 1024);
        else if (j < 4) glds16(vbase + (size_t)s * 64 * 8192, voffW[j - 2], b + 16384 + (j - 2) * 1024);
        else glds16(cbase + s * 128, voffC[j - 4], b + 32768 + (j - 4) * 1024);
    };
    auto dmaG = [&](int k, int j) { glds16(cbase + (size_t)(32 * k + wid + 8 * j) * 1024, voffG[j & 1], lds0 + gbuf(k) + (wid + 8 * j) * 1024); };
    bf16x8 cf[8];
    WAIT_BAR(12);
    rdW(0, 0);
#pragma unroll
    for (int i = 0; i < 4; ++i) cf[i] = rdC(0, i);
    static_for<128>([&](auto SI) {
        {
            constexpr int s = SI / 16, i = SI % 16;
            if (i == 12) {
                if (s < 6) { WAIT_BAR(6); } else if (s == 6) { WAIT_BAR(8); } else { WAIT_BAR(8); }
            }
            if (s == 7 && i == 11) {
                const int ln = lane_now();
#pragma unroll
                for (int j = 0; j < 4; ++j) gA[j] = lds + (ln & 15) * 1024 + (((4 * j + (ln >> 4)) ^ (ln & 15)) << 4);
            }
            if (s == 1 && i == 13) mid();
            constexpr int nx = i + 4;
            if constexpr (nx < 16) { if (nx == 8) rdW(s, 1); cf[nx & 7] = rdC(s, nx); }
            else if constexpr (s < 7) { if (nx == 16) rdW(s + 1, 0); cf[nx & 7] = rdC(s + 1, nx - 16); }
            else cf[nx & 7] = rdG(4, nx - 16);
            acc[i & 7][0] = mm(wf[2 * s + (i >> 3)][0], cf[i & 7], acc[i & 7][0]);
            acc[i & 7][1] = mm(wf[2 * s + (i >> 3)][1], cf[i & 7], acc[i & 7][1]);
            if (s + 3 < 8) { if (i == 12 || i == 14) dmaS(s + 3, (i - 12) / 2); }
            else if (i >= 12) {
                const int j = i - 12;
                if (s == 5) dmaG(4, j);
                else if (s == 6) dmaG(6, j);
                else dmaG(7, j);
            }
            if (i < 12 && i % 3 == 0) { if (s >= 1 && s + 2 < 8) dmaS(s + 2, 2 + i / 3); }
            if (s == 6 && i >= 2 && i < 10 && !(i & 1)) dmaG(5, (i - 2) / 2);
            if (s == 7 && i == 15) out_tile(0, acc[0][0], acc[0][1], acc[1][0], acc[1][1]);
            SBAR();
        }
    });
    stamp2();
    f32x4 a2[2][2][2];
    static_for<128>([&](auto KI) {
        {
            constexpr int k = 4 + KI / 32, ks = KI % 32;
            if (ks == 28 && k < 7) {
                if (k == 4) { WAIT_BAR(16); } else if (k == 5) { WAIT_BAR(16); } else { WAIT_BAR(21); }
            }
            constexpr int nx = ks + 4;
            if constexpr (nx < 32) cf[nx & 7] = rdG(k, nx); else if constexpr (k < 7) cf[nx & 7] = rdG(k + 1, nx - 32);
#pragma unroll
            for (int b = 0; b < 2; ++b) a2[k & 1][ks & 1][b] = mm(wf[ks >> 1][b], cf[ks & 7], ks < 2 ? f32x4{} : a2[k & 1][ks & 1][b]);
            if constexpr (k == 4) { if constexpr (ks < 3) out_tile(ks + 1, acc[2 * ks + 2][0], acc[2 * ks + 2][1], acc[2 * ks + 3][0], acc[2 * ks + 3][1]); }
            else if constexpr (ks == 3) out_tile(k - 1, a2[(k - 1) & 1][0][0], a2[(k - 1) & 1][0][1], a2[(k - 1) & 1][1][0], a2[(k - 1) & 1][1][1]);
            tail(KI);
            SBAR();
        }
    });
    out_tile(7, a2[1][0][0], a2[1][0][1], a2[1][1][0], a2[1][1][1]);
}

DI void ld_u16(unsigned& d, const void* sbase, unsigned voff) { asm volatile("global_load_ushort %0, %1, %2" : "=v"(d) : "v"(voff), "s"(sbase)); }
DI void ld_x4(u32x4& d, const void* sbase, unsigned voff) { asm volatile("global_load_dwordx4 %0, %1, %2" : "=v"(d) : "v"(voff), "s"(sbase)); }
DI void ld_u16_nop(unsigned& d, const void* sbase, unsigned voff) { asm volatile("s_nop 4\n\tglobal_load_ushort %0, %1, %2" : "=v"(d) : "v"(voff), "s"(sbase)); }
DI void ld_x4_nop(u32x4& d, const void* sbase, unsigned voff) { asm volatile("s_nop 4\n\tglobal_load_dwordx4 %0, %1, %2" : "=v"(d) : "v"(voff), "s"(sbase)); }
struct Params {
    const u16 *ax, *ckv, *wq, *wuk, *wuv, *wo, *wr, *wg, *wu, *wd, *gw;
    char* ws; u16* out;
};
struct Ctx { lptr lds; char* lds_; unsigned lds0; int tid, lane, wid, bid; unsigned tag; unsigned* sy; };
typedef __attribute__((address_space(4))) const Params* kparams_t;
DI Params load_params() {
    kparams_t pp = (kparams_t)__builtin_amdgcn_kernarg_segment_ptr();
    asm volatile("" : "+s"(pp));
    Params P;
    P.ax = pp->ax; P.ckv = pp->ckv; P.wq = pp->wq; P.wuk = pp->wuk; P.wuv = pp->wuv; P.wo = pp->wo; P.wr = pp->wr; P.wg = pp->wg; P.wu = pp->wu;
    P.wd = pp->wd; P.gw = pp->gw; P.ws = pp->ws; P.out = pp->out;
    return P;
}
DI Ctx make_ctx(char* lds_, char* ws, int wid, unsigned tag) {
    Ctx C;
    C.lds = (lptr)lds_; C.lds_ = lds_; C.lds0 = (unsigned)(uintptr_t)C.lds;
    int lane; asm volatile("v_mbcnt_lo_u32_b32 %0, -1, 0\n\tv_mbcnt_hi_u32_b32 %0, -1, %0" : "=v"(lane));
    asm volatile("" : "+s"(wid), "+s"(tag));
    C.lane = lane; C.wid = wid; C.tid = wid * 64 + lane; C.bid = blockIdx.x; C.tag = tag; C.sy = (unsigned*)(ws + WS_SYNC);
    return C;
}

#define GU_K 5
#define GU_KS 8
constexpr int QST = 49152;
struct QPre { unsigned voffX[4], voffW[2]; u32x4 rv, wgv, wuv; };
constexpr int MOE_WD = 147456 + 2048;
DI int moe_f0(int bid) { return ((bid & 7) * 28 + ((bid >> 3) < 28 ? (bid >> 3) : 27)) * MOE_COLS; }
DI void q_dma(const QPre& Q, const Params& P, const Ctx& C, int s, int j) {
    const int wid = C.wid, h = (C.bid & 7) * 4 + ((C.bid >> 3) & 3), bi = C.bid >> 5;
    const char* xbase = (const char*)P.ax + (size_t)(((wid >> 2) ? 15 - bi : bi) * 128 + (wid & 3) * 32) * 1024;
    const char* wbase = (const char*)P.wq + h * 256;
    const unsigned b = C.lds0 + ((s + 2) % 3) * QST;
    if (j < 2) glds16(wbase + (size_t)s * 64 * 8192, Q.voffW[j], b + 32768 + (2 * wid + j) * 1024);
    else glds16(xbase + s * 128, Q.voffX[j - 2], b + (4 * wid + j - 2) * 1024);
}
DI void phase_kv(const Params& P, Ctx& C, QPre& Q, unsigned tagv) {
    char* ws = P.ws;
    const int lane = C.lane, wid = C.wid, bid = C.bid, h = (bid & 7) * 4 + ((bid >> 3) & 3), p = bid >> 5;
    STAMP(0, 0);
    const int tok = bid * 8 + wid;
    const bf16x8 bxv = *(const bf16x8*)(P.ax + tok * DM + lane * 8), wrv = *(const bf16x8*)(P.wr + lane * 8);
    const bf16x8 gwv = *(const bf16x8*)(P.gw + lane * 8);
    auto pre = [&]() {
        float sr = 0.f, sg = 0.f;
#pragma unroll
        for (int j = 0; j < 8; ++j) { sr += bf2f((u16)bxv[j]) * bf2f((u16)wrv[j]); sg += bf2f((u16)bxv[j]) * bf2f((u16)gwv[j]); }
        sr = wave_sum(sr, lane); sg = wave_sum(sg, lane);
        { unsigned t;
          asm volatile("s_waitcnt vmcnt(18)\n\tv_readfirstlane_b32 %0, %1" : "=s"(t) : "v"(tagv));
          C.tag = t + 1u; }
        if (lane == 0) { store_f32_wt((float*)(ws + WS_R) + tok, bfround(sr)); store_f32_wt((float*)(ws + WS_G) + tok, sigmoidf_(sg)); }
    };
    auto mid = [&]() { if (C.tid == 0) flag_store(C.sy + SY_R + bid, C.tag); };
    const char* cbase = (const char*)P.ckv + (size_t)p * 256 * 1024;
    const char* kbase = (const char*)P.wuk + h * 256;
    const char* vbase = (const char*)P.wuv + h * 256;
    const unsigned outoff = (unsigned)((h * 64 + 8 * p) * 8 + 2 * (wid & 3)) * 1024;
    const unsigned kvbytes = (unsigned)((size_t)SEQ * NHD * 2);
    unsigned rp[4];
    auto tail = [&](auto KI) {
        constexpr int k = 4 + KI / 32, ks = KI % 32;
        if constexpr (k == 4 && ks == 20) {
            const int ln = lane_now();
#pragma unroll
            for (int j = 0; j < 4; ++j) { const int r = 8 * j + (ln >> 3); Q.voffX[j] = r * 1024 + (((ln & 7) ^ ((r >> 1) & 7)) << 4); }
#pragma unroll
            for (int j = 0; j < 2; ++j) { const int row = 4 * (2 * wid + j) + (ln >> 4); Q.voffW[j] = row * 8192 + (((ln & 15) ^ ((row & 3) << 2)) << 4); }
            if (wid == 0) {
#pragma unroll
                for (int j = 0; j < 4; ++j) asm volatile("global_load_dword %0, %1, %2 sc1" : "=v"(rp[j]) : "v"(ln * 4), "s"(C.sy + SY_R + 64 * j));
            }
        }
        if constexpr (k == GU_K && ks == GU_KS) ld_x4(Q.wgv, (const char*)P.wg + moe_f0(bid) * 2, (wid * 64 + lane_now()) * (FF * 2));
        if constexpr (k == GU_K && ks == GU_KS + 1) ld_x4(Q.wuv, (const char*)P.wu + moe_f0(bid) * 2, (wid * 64 + lane_now()) * (FF * 2));
        if constexpr (k == 6 && ks == 8) glds16((const char*)P.wd + (size_t)(moe_f0(bid) + wid) * 1024, lane_now() * 16, C.lds0 + MOE_WD + wid * 1024);
        if constexpr (k == 5 && ks >= 28) q_dma(Q, P, C, 0, ks - 28);
        if constexpr (k == 6 && ks < 2) q_dma(Q, P, C, 0, 4 + ks);
        if constexpr (k == 6 && ks == 20) {
            if (wid == 0) {
                asm volatile("s_waitcnt vmcnt(13)" : "+v"(rp[0]), "+v"(rp[1]), "+v"(rp[2]), "+v"(rp[3]));
                if (__builtin_expect(!__all(rp[0] == C.tag && rp[1] == C.tag && rp[2] == C.tag && rp[3] == C.tag), 0)) wait_flags<4>(C.sy + SY_R, 256, C.tag, lane_now());
            }
        }
        if constexpr (k == 6 && ks >= 28) q_dma(Q, P, C, 1, ks - 28);
        if constexpr (k == 7 && ks < 2) q_dma(Q, P, C, 1, 4 + ks);
        if constexpr (k == 7 && ks == 5) asm volatile("global_load_dwordx4 %0, %1, %2 sc1" : "=v"(Q.rv) : "v"((wid * 64 + lane_now()) * 16), "s"(P.ws + WS_R));
    };
    auto st2 = [&]() { };
    if (wid < 4) kv_body<false>(cbase, kbase, vbase, make_rsrc(ws + WS_KF, kvbytes), outoff, C.lds, C.lds0, wid, lane, pre, mid, tail, st2);
    else         kv_body<true>(cbase, kbase, vbase, make_rsrc(ws + WS_VF, kvbytes), outoff, C.lds, C.lds0, wid, lane, pre, mid, tail, st2);
    STAMP(0, 3);
}

DI unsigned umax_(unsigned a, unsigned b) { return a > b ? a : b; }
DI unsigned umin_(unsigned a, unsigned b) { return a < b ? a : b; }
DI float lo16(unsigned v) { return __uint_as_float(v << 16); }
DI float hi16(unsigned v) { return __uint_as_float(v & 0xFFFF0000u); }

constexpr int K2_QST = 49152, K2_SLOT = 32768, K2_QB = 131072, K2_LDS = 163840;
constexpr float QSCALE = 0.08838834764831845f * 1.4426950408889634f;
constexpr float THR = 8.0f;
constexpr int RING0 = 3;

#define SETPRIO(x) __builtin_amdgcn_s_setprio(x)
struct SMState { float mhat, l; f32x16 negm; };
#define PIN(x) asm volatile("" : "+v"(x))
DI float max3(float a, float b, float c) { return fmaxf(fmaxf(a, b), c); }

#define EXP2(x) __builtin_amdgcn_exp2f(x)
#define VOFF(i) (((i) >> 3) * 8192 + (2 * ((i) & 3) + (((i) >> 2) & 1)) * 1024)
#define KOFF(i) (((i) & 1) * 8192 + ((i) >> 1) * 1024)
DI float fadd_s(float a, float b) { float r; asm("v_add_f32_e32 %0, %1, %2" : "=v"(r) : "v"(a), "v"(b)); return r; }
DI float max3_s(float a, float b, float c) { float r; asm("v_max3_f32 %0, %1, %2, %3" : "=v"(r) : "v"(a), "v"(b), "v"(c)); return r; }
DI float max2_s(float a, float b) { float r; asm("v_max_f32_e32 %0, %1, %2" : "=v"(r) : "v"(a), "v"(b)); return r; }
template <int OFF> DI void glds_step(const void* sbase, unsigned voff, unsigned lds_addr) {
    if (OFF == 0) asm volatile("s_mov_b32 m0, %2\n\ts_nop 0\n\tglobal_load_lds_dwordx4 %0, %1 sc1" :: "v"(voff), "s"(sbase), "s"(lds_addr));
    else asm volatile("global_load_lds_dwordx4 %0, %1 offset:%c2 sc1" :: "v"(voff), "s"(sbase), "i"(OFF));
}
DI f32x16 mfma_c(bf16x8 a, bf16x8 b, const f32x16& c) {
    f32x16 d; asm volatile("v_mfma_f32_32x32x16_bf16 %0, %1, %2, %3" : "=&v"(d) : "v"(a), "v"(b), "v"(c)); return d;
}
struct DmaJob { const char* src0; const char* src1; unsigned dst0, dst1; bool has0, has1; };

#define LA 4
template <bool QK, bool FIN, class Mid>
DI void attn_A(lptr kp, const lptr& vpn, const bf16x8 (&qf)[8], const f32x16& negm, const bf16x8 (&kpre)[LA], bf16x8 (&vpre)[LA], f32x16& s0, f32x16& s1,
               const f32x16& p0, const f32x16& p1, bf16x8 (&pf)[4], float& l, const DmaJob& dj, unsigned vlin, Mid&& mid) {
    bf16x8 fr[16];
    unsigned pw[16];
    float sa = 0.f, sb = 0.f;
#pragma unroll
    for (int i = 0; i < LA; ++i) fr[i] = kpre[i];
#pragma unroll
    for (int i = 0; i < 16; ++i) {
        if (QK && i + LA < 16) fr[i + LA] = ldsr128a(kp + KOFF(i + LA));
        if (i + LA >= 16) vpre[i + LA - 16] = ldsr128a(vpn + VOFF(i + LA - 16));
        if (QK) {
            if (i == 0) s0 = mfma_c(fr[0], qf[0], negm);
            else if (i == 1) s1 = mfma_c(fr[1], qf[0], negm);
            else if (i & 1) s1 = MFMA(fr[i], qf[i >> 1], s1);
            else s0 = MFMA(fr[i], qf[i >> 1], s0);
        }
        if (FIN) {
            const float e0 = i < 8 ? p0[2 * i] : p1[2 * i - 16], e1 = i < 8 ? p0[2 * i + 1] : p1[2 * i - 15];
            if (i == 0) { sa = e0; sb = e1; } else { sa = fadd_s(sa, e0); sb = fadd_s(sb, e1); PIN(sa); PIN(sb); }
            pw[i] = cvtpk(e0, e1);
            PIN(pw[i]);
        }
        if (i == 3) mid();
        if (QK) {
            if (i == 1 && dj.has0) { glds_step<0>(dj.src0, vlin, dj.dst0); glds_step<1024>(dj.src0, vlin, dj.dst0); }
            if (i == 5 && dj.has0) { glds_step<2048>(dj.src0, vlin, dj.dst0); glds_step<3072>(dj.src0, vlin, dj.dst0); }
            if (i == 9 && dj.has1) { glds_step<0>(dj.src1, vlin, dj.dst1); glds_step<1024>(dj.src1, vlin, dj.dst1); }
            if (i == 13 && dj.has1) { glds_step<2048>(dj.src1, vlin, dj.dst1); glds_step<3072>(dj.src1, vlin, dj.dst1); }
        }
        SBAR();
    }
    if (FIN) {
        l += sa + sb;
#pragma unroll
        for (int i = 0; i < 4; ++i) { u32x4 w = {pw[4 * i], pw[4 * i + 1], pw[4 * i + 2], pw[4 * i + 3]}; pf[i] = __builtin_bit_cast(bf16x8, w); }
    }
}
DI void dma_plain(const DmaJob& dj, unsigned vlin) {
    if (dj.has0) glds16x4_dev(dj.src0, vlin, dj.dst0);
    if (dj.has1) glds16x4_dev(dj.src1, vlin, dj.dst1);
}
template <bool PV, bool SM, class Mid>
DI void attn_B(lptr vp, const lptr& kpn, f32x16 (&o)[4], const bf16x8 (&pf)[4], const bf16x8 (&vpre)[LA], bf16x8 (&kpre)[LA],
               f32x16& s0, f32x16& s1, SMState& st, bool first, bool mask, int qrel  , bool& pend, float& fpend, Mid&& mid) {
    bf16x8 fr[16];
    float ma = 0.f, mb = 0.f, mc = 0.f;
#pragma unroll
    for (int i = 0; i < LA; ++i) fr[i] = vpre[i];
    if (SM && __builtin_expect(mask, 0)) {
        const float NEG = -__builtin_inff();
#pragma unroll
        for (int r = 0; r < 16; ++r) { const int c = (r & 3) + 8 * (r >> 3) + 16 * ((r >> 2) & 1);
            if (c > qrel) s0[r] = NEG;
            if (c + 32 > qrel) s1[r] = NEG; }
    }
    SETPRIO(1);
    static_for<16>([&](auto I) {
        constexpr int i = I;
        if (PV && i + LA < 16) fr[i + LA] = ldsr128a(vp + VOFF(i + LA));
        if (i + LA >= 16) kpre[i + LA - 16] = ldsr128a(kpn + KOFF(i + LA - 16));
        if (PV) o[i & 3] = MFMA(fr[i], pf[i >> 2], o[i & 3]);
        if (SM) {
            if (i == 0) { ma = max3(s0[0], s0[1], s0[2]); mb = max3(s1[0], s1[1], s1[2]); mc = max3(s0[3], s0[4], s1[3]); PIN(ma); PIN(mb); PIN(mc); }
            if (i == 1) { ma = max3_s(ma, s0[5], s0[6]); mb = max3_s(mb, s1[4], s1[5]); mc = max3_s(mc, s0[7], s1[6]); PIN(ma); PIN(mb); PIN(mc); }
            if (i == 2) { ma = max3_s(ma, s0[8], s0[9]); mb = max3_s(mb, s1[7], s1[8]); mc = max3_s(mc, s0[10], s1[9]); PIN(ma); PIN(mb); PIN(mc); }
            if (i == 3) { ma = max3_s(ma, s0[11], s0[12]); mb = max3_s(mb, s1[10], s1[11]); mc = max3_s(mc, s0[13], s1[12]); PIN(ma); PIN(mb); PIN(mc); }
            if (i == 4) { ma = max3_s(ma, s0[14], s0[15]); mb = max3_s(mb, s1[13], s1[14]); mc = max2_s(mc, s1[15]); PIN(ma); PIN(mb); PIN(mc); }
            if (i == 5) {
                float rm = max3_s(ma, mb, mc);
                auto rr = __builtin_amdgcn_permlane32_swap(__float_as_uint(rm), __float_as_uint(rm), false, false);
                rm = max2_s(__uint_as_float(rr[0]), __uint_as_float(rr[1]));
                if (__builtin_expect(first || __any(rm > THR), 0)) {
                    const float dl = first ? rm : fmaxf(rm, 0.f), f = first ? 1.0f : __builtin_amdgcn_exp2f(-dl);
                    st.mhat += dl; st.l *= f;
#pragma unroll
                    for (int r = 0; r < 16; ++r) { s0[r] -= dl; s1[r] -= dl; st.negm[r] = -st.mhat; }
                    pend = !first; fpend = f;
                }
            }
            if (i >= 6) {
                constexpr int e0 = i < 14 ? 3 * (i - 6) : 24 + 4 * (i - 14), e1 = i < 14 ? e0 + 3 : e0 + 4;
#pragma unroll
                for (int e = e0; e < e1; ++e) { if (e < 16) { s0[e] = EXP2(s0[e]); } else { s1[e - 16] = EXP2(s1[e - 16]); } }
                PIN(s0); PIN(s1);
            }
        }
        if (i == 7) mid();
        SBAR();
    });
    SETPRIO(0);
}

struct Routed { int sel0, sel1; float sig0, sig1; };
DI void phase_attn(const Params& P, const Ctx& C, QPre& Q, Routed& R) {
    char* ws = P.ws; char* lds_ = C.lds_;
    const u16 *x = P.ax, *wq = P.wq, *wo = P.wo, *bx = P.ax, *wg = P.wg, *wu = P.wu, *wd = P.wd;
    const lptr lds = C.lds;
    const unsigned lds0 = C.lds0;
    const int tid = C.tid, lane = C.lane, l31 = lane & 31, hi = lane >> 5, wid = C.wid;
    const int bid = C.bid, h = (bid & 7) * 4 + ((bid >> 3) & 3), bi = bid >> 5;
    const int grp = wid >> 2, rg = wid & 3;
    const int blkA = bi, blkB = 15 - bi;

    STAMP(1, 0);
    LAS unsigned* sk = (LAS unsigned*)((LAS char*)lds_ + 147456);
    LAS float* hs = (LAS float*)((LAS char*)lds_ + 147456 + 128);
    LAS float* red = (LAS float*)((LAS char*)lds_ + 147456 + 256);
    const int cg = moe_f0(bid) / MOE_COLS;
    unsigned k1 = 0, k2 = 0, xs0, xs1;
    u32x4 skv[4];
    float mv[32], ma0, ma1, hsv[16];
    unsigned wdv[8];
    auto top2 = [&](unsigned b1, unsigned b2) { k2 = umax_(umin_(k1, b1), umax_(k2, b2)); k1 = umax_(k1, b1); };
    auto ins = [&](unsigned key) { k2 = umax_(k2, umin_(k1, key)); k1 = umax_(k1, key); };
    auto moe_gap = [&](auto GI) {
        constexpr int G = GI, s = G / 16, i = G % 16;
        if constexpr (G == 13 || G == 14) {
            if constexpr (G == 13) asm volatile("" : "+v"(Q.rv));
#pragma unroll
            for (int j = 2 * (G - 13); j < 2 * (G - 13) + 2; ++j) {
                const unsigned u = Q.rv[j] >> 16;
                const unsigned o = (u & 0x8000u) ? (~u & 0xFFFFu) : (u | 0x8000u);
                ins((o << 16) | (unsigned)(0xFFFF - (tid * 4 + j)));
            }
            PIN(k1); PIN(k2);
        }
        if constexpr (G == 15) { top2(dppu<0x128>(k1), dppu<0x128>(k2)); PIN(k1); PIN(k2); }
        if constexpr (G == 16) { top2(dppu<0x124>(k1), dppu<0x124>(k2)); PIN(k1); PIN(k2); }
        if constexpr (G == 17) { top2(dppu<0x122>(k1), dppu<0x122>(k2)); PIN(k1); PIN(k2); }
        if constexpr (G == 18) { top2(dppu<0x121>(k1), dppu<0x121>(k2)); PIN(k1); PIN(k2); }
        if constexpr (G == 19) { auto a = __builtin_amdgcn_permlane16_swap(k1, k1, false, false); auto b = __builtin_amdgcn_permlane16_swap(k2, k2, false, false);
            k1 = a[0]; k2 = b[0]; top2(a[1], b[1]); PIN(k1); PIN(k2); }
        if constexpr (G == 20) { auto a = __builtin_amdgcn_permlane32_swap(k1, k1, false, false); auto b = __builtin_amdgcn_permlane32_swap(k2, k2, false, false);
            k1 = a[0]; k2 = b[0]; top2(a[1], b[1]);
            if (lane == 0) { sk[wid * 2] = k1; sk[wid * 2 + 1] = k2; } }
        if constexpr (G == 29) {
#pragma unroll
            for (int j = 0; j < 4; ++j) skv[j] = *(const LAS u32x4*)(sk + 4 * j);
            k1 = 0; k2 = 0;
        }
        if constexpr (G == 30 || G == 31) {
#pragma unroll
            for (int j = 8 * (G - 30); j < 8 * (G - 30) + 8; j += 2) top2(skv[j >> 2][j & 3], skv[j >> 2][(j & 3) + 1]);
            PIN(k1); PIN(k2);
        }
        if constexpr (G == 32) {
            k1 = __builtin_amdgcn_readfirstlane(k1); k2 = __builtin_amdgcn_readfirstlane(k2);
            R.sel0 = 0xFFFF - (int)(k1 & 0xFFFFu); R.sel1 = 0xFFFF - (int)(k2 & 0xFFFFu);
            ld_u16_nop(xs0, (const char*)bx + R.sel0 * 1024, tid * 2);
            ld_u16_nop(xs1, (const char*)bx + R.sel1 * 1024, tid * 2);
        }
        if constexpr (G == 33) {
            const unsigned o0 = k1 >> 16, o1 = k2 >> 16;
            const float r0 = __uint_as_float(((o0 & 0x8000u) ? (o0 & 0x7FFFu) : (~o0 & 0xFFFFu)) << 16);
            const float r1 = __uint_as_float(((o1 & 0x8000u) ? (o1 & 0x7FFFu) : (~o1 & 0xFFFFu)) << 16);
            R.sig0 = __uint_as_float(__builtin_amdgcn_readfirstlane(__float_as_uint(sigmoidf_(r0))));
            R.sig1 = __uint_as_float(__builtin_amdgcn_readfirstlane(__float_as_uint(sigmoidf_(r1))));
        }
        if constexpr (G >= 61 && G <= 64) {
            if constexpr (G == 61) { asm volatile("" : "+v"(xs0), "+v"(xs1), "+v"(Q.wgv), "+v"(Q.wuv)); ma0 = lo16(xs0); ma1 = lo16(xs1); }
            constexpr int c = 2 * (G - 61);
            const float g0 = lo16(Q.wgv[c >> 1]), g1 = hi16(Q.wgv[c >> 1]), u0 = lo16(Q.wuv[c >> 1]), u1 = hi16(Q.wuv[c >> 1]);
            mv[c] = ma0 * g0; mv[c + 1] = ma0 * g1; mv[8 + c] = ma0 * u0; mv[9 + c] = ma0 * u1;
            mv[16 + c] = ma1 * g0; mv[17 + c] = ma1 * g1; mv[24 + c] = ma1 * u0; mv[25 + c] = ma1 * u1;
            PIN(mv[c]); PIN(mv[c + 1]); PIN(mv[8 + c]); PIN(mv[9 + c]); PIN(mv[16 + c]); PIN(mv[17 + c]); PIN(mv[24 + c]); PIN(mv[25 + c]);
        }
        if constexpr (G >= 65 && G <= 68) {
#pragma unroll
            for (int j = 4 * (G - 65); j < 4 * (G - 65) + 4; ++j) { auto r = __builtin_amdgcn_permlane32_swap(__float_as_uint(mv[j]), __float_as_uint(mv[j + 16]), false, false);
                mv[j] = __uint_as_float(r[0]) + __uint_as_float(r[1]); PIN(mv[j]); }
        }
        if constexpr (G == 69 || G == 70) {
#pragma unroll
            for (int j = 4 * (G - 69); j < 4 * (G - 69) + 4; ++j) { auto r = __builtin_amdgcn_permlane16_swap(__float_as_uint(mv[j]), __float_as_uint(mv[j + 8]), false, false);
                mv[j] = __uint_as_float(r[0]) + __uint_as_float(r[1]); PIN(mv[j]); }
        }
        if constexpr (G == 71) { const bool up_ = lane & 8;
#pragma unroll
            for (int j = 0; j < 4; ++j) { const float send = up_ ? mv[j] : mv[j + 4], keep = up_ ? mv[j + 4] : mv[j]; mv[j] = keep + dppf<0x128>(send); PIN(mv[j]); } }
        if constexpr (G == 72) { const bool up_ = lane & 4;
#pragma unroll
            for (int j = 0; j < 2; ++j) { const float send = up_ ? mv[j] : mv[j + 2], keep = up_ ? mv[j + 2] : mv[j]; mv[j] = keep + dppf<0x1B>(dppf<0x141>(send)); PIN(mv[j]); } }
        if constexpr (G == 73) { const bool up_ = lane & 2;
            const float send = up_ ? mv[0] : mv[1], keep = up_ ? mv[1] : mv[0]; mv[0] = keep + dppf<0x4E>(send);
            mv[0] += dppf<0xB1>(mv[0]);
            if (!(lane & 1)) red[wid * 32 + (lane >> 1)] = mv[0];
        }
        if constexpr (G == 77) {
            if (tid < 16) {
                const int c = tid & 7, t = tid >> 3;
                float g = 0.f, u = 0.f;
#pragma unroll
                for (int w = 0; w < 8; ++w) { g += red[w * 32 + 16 * t + c]; u += red[w * 32 + 16 * t + 8 + c]; }
                g = bfround(g); u = bfround(u);
                hs[t * 8 + c] = bfround(bfround(g * sigmoidf_(g)) * u);
            }
        }
        if constexpr (G == 93) {
#pragma unroll
            for (int j = 0; j < 8; ++j) wdv[j] = *(const LAS u16*)((LAS char*)lds_ + MOE_WD + j * 1024 + tid * 2);
#pragma unroll
            for (int j = 0; j < 4; ++j) { const f32x4 t = *(const LAS f32x4*)(hs + 4 * j); hsv[4 * j] = t[0]; hsv[4 * j + 1] = t[1]; hsv[4 * j + 2] = t[2]; hsv[4 * j + 3] = t[3]; }
        }
        if constexpr (G == 95) {
            float y0 = 0.f, y1 = 0.f;
#pragma unroll
            for (int j = 0; j < 8; ++j) { const float w = lo16(wdv[j]); y0 += hsv[j] * w; y1 += hsv[8 + j] * w; }
            float* yp = (float*)(ws + WS_YP) + (size_t)cg * 1024;
            store_f32_wt(yp + tid, y0); store_f32_wt(yp + 512 + tid, y1);
        }
        (void)s; (void)i;
    };

    const char* Kh = ws + WS_KF + (size_t)h * 64 * 8192;
    const unsigned vlin = lane * 16;
    bf16x8 qf[8];
    {
        lptr xA[4];
#pragma unroll
        for (int ks = 0; ks < 4; ++ks) xA[ks] = lds + (32 * wid + l31) * 128 + (((2 * ks + hi) ^ ((l31 >> 1) & 7)) << 4);
        const int tq = (lane & 15) >> 2, tp = lane & 3, tblk = (lane >> 4) & 1;
        lptr wA[4];
#pragma unroll
        for (int c = 0; c < 4; ++c) wA[c] = lds + 32768 + (8 * hi + tq) * 256 + ((c ^ tq) << 6) + (tp >> 1) * 32 + tblk * 16 + (tp & 1) * 8;
        auto dmaS = [&](int s, int j) { q_dma(Q, P, C, s, j); };
        auto rdW = [&](int s, int i) { const int o = ((s + 2) % 3) * QST + (i >> 2) * 4096; return cat(ldstr(wA[i & 3] + o), ldstr(wA[i & 3] + o + 1024)); };
        auto rdX = [&](int s, int ks) { return ldsr128(xA[ks] + ((s + 2) % 3) * QST); };
        f32x16 acc[4] = {};
        bf16x8 wfr[8], xfr[2];
        unsigned kvf = 0;
        WAIT_BAR(14);
        STAMP(1, 1);
        xfr[0] = rdX(0, 0);
#pragma unroll
        for (int i = 0; i < 4; ++i) wfr[i] = rdW(0, i);
        static_for<128>([&](auto SI) {
            {
                constexpr int s = SI / 16, i = SI % 16;
                if (i == 12 && s < 7) {
                    if (s == 0) { WAIT_BAR(8); } else if (s == 1) { WAIT_BAR(6); } else if (s == 2) { WAIT_BAR(8); } else if (s < 6) { WAIT_BAR(6); } else { WAIT_BAR(0); }
                }
                const int nx = i + 4;
                if (nx < 16) { if ((nx & 3) == 0) xfr[(nx >> 2) & 1] = rdX(s, nx >> 2); wfr[nx & 7] = rdW(s, nx); }
                else if (s < 7) { if (nx == 16) xfr[0] = rdX(s + 1, 0); wfr[nx & 7] = rdW(s + 1, nx - 16); }
                acc[i & 3] = MFMA(wfr[i & 7], xfr[(i >> 2) & 1], acc[i & 3]);
                if (i == 12 || i == 14) { if (s + 3 < 8) dmaS(s + 3, (i - 12) / 2); }
                else if (s == 0) { if (i < 6) dmaS(2, i); }
                else if (i < 12 && i % 3 == 0) { if (s + 2 < 8) dmaS(s + 2, 2 + i / 3); }
                if (s == 6 && grp == 0) {
                    if (i == 12) glds16x4_dev(Kh + rg * 4096, vlin, lds0 + RING0 * 16384 + rg * 4096);
                    if (i == 14) glds16x4_dev(Kh + 16384 + rg * 4096, vlin, lds0 + (RING0 + 2) * 16384 + rg * 4096);
                }
                moe_gap(SI);
                if (s == 1 && i == 13 && tid == 0) flag_store(C.sy + SY_KV + h * 8 + bi, C.tag);
                if (s == 4 && i == 3 && wid == 0)
                    asm volatile("global_load_dword %0, %1, %2 sc1" : "=v"(kvf) : "v"((lane & 7) * 4), "s"(C.sy + SY_KV + h * 8));
                if (s == 5 && i == 13 && wid == 0) {
                    asm volatile("" : "+v"(kvf));
                    if (__builtin_expect(!__all(kvf == C.tag), 0)) wait_flags<1>(C.sy + SY_KV + h * 8, 8, C.tag, lane);

                }
                if (s == 6 && i == 13 && tid == 0) flag_store(C.sy + SY_Y + bid, C.tag);
                SBAR();
            }
        });
#pragma unroll
        for (int c = 0; c < 4; ++c) { qf[2 * c] = pack8(acc[c], 0, QSCALE); qf[2 * c + 1] = pack8(acc[c], 1, QSCALE); }
    }

    STAMP(1, 2);
    const int n1 = 2 * bi + 2;
    auto tile_of = [&](int n, int g) { return n < n1 ? n : n1 + 2 * (n - n1) + g; };
    auto jstar = [&](int tn) { return tn <= 2 * n1 ? tn : tn <= 2 * n1 + 2 ? tn + 1 : 2 * tn - 2 * n1 - 2; };
    auto jg1 = [&](int tn) { return tn == 2 * n1 + 1 ? tn : 2 * tn - 2 * n1 - 1; };
    int tb_d0, tb_d1, tb_kv, tb_rel;
    auto make_job = [&](int tn, int& d0, int& d1) {
        const int n = tn >> 1;
        int a_off, b_off; bool a_ok, b_ok;
        const int kb = (int)WS_KF + h * 64 * 8192 + rg * 4096, vb = (int)WS_VF + h * 64 * 8192 + rg * 4096;
        if (tn & 1) { a_ok = n >= 1 && n <= 17; a_off = vb + tile_of(n - 1, 0) * 16384; b_ok = n >= n1 && n <= 16; b_off = kb + tile_of(n, 1) * 16384; }
        else        { a_ok = n <= 16;           a_off = kb + tile_of(n, 0) * 16384;     b_ok = n - 2 >= n1 && n <= 18; b_off = vb + tile_of(n - 2, 1) * 16384; }
        const int da = a_off | ((jstar(tn) + RING0) & 7) | 8, db = b_off | ((jg1(tn) + RING0) & 7) | 8;
        d0 = a_ok ? da : b_ok ? db : 0; d1 = (a_ok && b_ok) ? db : 0;
    };
    {
        const int n = lane < 19 ? lane : 18;
        make_job(2 * n + grp + 3, tb_d0, tb_d1);
        const int ks = ((n < n1 || grp == 0 ? jstar(2 * n) : jg1(2 * n + 1)) + RING0) & 7;
        const int vs = ((n < n1 || grp == 0 ? jstar(2 * n + 3) : jg1(2 * n + 4)) + RING0) & 7;
        tb_kv = ks | vs << 8;
        tb_rel = 128 * ((grp || n >= n1) ? blkB : blkA) + 32 * rg - 64 * tile_of(n, grp);
    }
    auto job_from = [&](int d0, int d1) -> DmaJob {
        DmaJob d;
        d.has0 = d0 & 8; d.has1 = d1 & 8;
        d.src0 = ws + (unsigned)(d0 & ~4095); d.src1 = ws + (unsigned)(d1 & ~4095);
        d.dst0 = lds0 + (d0 & 7) * 16384 + rg * 4096; d.dst1 = lds0 + (d1 & 7) * 16384 + rg * 4096;
        return d;
    };
    auto job = [&](int n) { return job_from(__builtin_amdgcn_readlane(tb_d0, n), __builtin_amdgcn_readlane(tb_d1, n)); };
    if (grp == 1) {
#pragma unroll
        for (int c = 0; c < 8; ++c) *(LAS bf16x8*)((LAS char*)lds_ + K2_QB + rg * 8192 + c * 1024 + lane * 16) = qf[c];
    }
    f32x16 o[4] = {};
    SMState st; st.mhat = 0.f; st.l = 0.f; st.negm = f32x16{};
    const lptr kp0 = lds + lane * 16;
    char* ctx = ws + WS_CTX;
    const rsrc_t ctxr = make_rsrc(ctx, (unsigned)((size_t)SEQ * NHD * 2));
    auto store_ctx = [&](int blk, int d, const f32x16& v, float inv) {
        const unsigned off = (unsigned)(((4 * blk + rg) * 32 + h) * 8 + 2 * d) * 1024 + lane * 16;
        store_wt(ctxr, off, pack8(v, 0, inv));
        store_wt(ctxr, off + 1024, pack8(v, 1, inv));
    };
    auto half_sum = [&](float v) { auto rr = __builtin_amdgcn_permlane32_swap(__float_as_uint(v), __float_as_uint(v), false, false);
                                   return __uint_as_float(rr[0]) + __uint_as_float(rr[1]); };
    auto kslot = [&](int n) { return kp0 + (__builtin_amdgcn_readlane(tb_kv, n) & 7) * 16384; };
    auto vslot = [&](int n) { return kp0 + ((__builtin_amdgcn_readlane(tb_kv, n) >> 8) & 7) * 16384; };
    WAIT_BAR(0);
    bool first = true, pend = false;
    float invA = 0.f, fpend = 1.f;
    f32x16 sA0, sA1, sB0, sB1; bf16x8 pf[4], vpre[LA], kpre[LA];
#pragma unroll
    for (int i = 0; i < LA; ++i) kpre[i] = ldsr128a(kslot(0) + KOFF(i));
    if (grp == 1) { WAIT_BAR(0); }
    SEG_DECL; SEG_START();
    const int nsw = grp == 0 ? n1 - 1 : 100;
    auto headB = [&](int n) {
        if (__builtin_expect(n == nsw + 1, 0)) { invA = 1.0f / half_sum(st.l); st.mhat = 0.f; st.l = 0.f; first = true; }
    };
    auto tailB = [&](int n) {
        first = false;
        if (__builtin_expect(pend || (unsigned)(n - nsw) < 3u, 0)) {
            if (n == nsw + 2 && tid == 0) flag_store(C.sy + SY_CTX + blkA * 32 + h, C.tag);
            if (pend) {
#pragma unroll
                for (int d = 0; d < 4; ++d)
#pragma unroll
                    for (int r = 0; r < 16; ++r) o[d][r] *= fpend;
                pend = false;
            }
            if (n == nsw + 1) {
#pragma unroll
                for (int d = 0; d < 4; ++d) store_ctx(blkA, d, o[d], invA);
#pragma unroll
                for (int d = 0; d < 4; ++d) o[d] = f32x16{};
            }
            if (n == nsw) {
#pragma unroll
                for (int c = 0; c < 8; ++c) qf[c] = ldsr128(lds + K2_QB + rg * 8192 + c * 1024 + lane * 16);
                st.negm = f32x16{};
            }
        }
    };
#define BARRIER_L() asm volatile("s_waitcnt lgkmcnt(0)\n\ts_barrier" ::: "memory")
    lptr kcur = kslot(0), vcur = kp0;
    DmaJob dj = job(0);
    int rel = 0;
    auto pinjob = [&]() { asm volatile("" : "+s"(dj.src0), "+s"(dj.src1), "+s"(dj.dst0), "+s"(dj.dst1)); };
    auto midA = [&](int n) { vcur = vslot(n > 0 ? n - 1 : 0); rel = __builtin_amdgcn_readlane(tb_rel, n); asm volatile("" : "+v"(vcur), "+s"(rel)); };
    auto midB = [&](int n) { kcur = kslot(n + 1); dj = job(n + 1); asm volatile("" : "+v"(kcur)); pinjob(); };
#define RELARGS first, rel < 63, rel + l31 - 4 * hi, pend, fpend
    attn_A<true, false>(kcur, vcur, qf, st.negm, kpre, vpre, sA0, sA1, sA0, sA1, pf, st.l, dj, vlin, [&]() { midA(0); });
    BARRIER_L();
    attn_B<false, true>(vcur, kcur, o, pf, vpre, kpre, sA0, sA1, st, RELARGS, [&]() { midB(0); });
    tailB(0);
    WAIT_BAR(0);
#define STEP(n, C0, C1, P0, P1) do { \
        attn_A<true, true>(kcur, vcur, qf, st.negm, kpre, vpre, C0, C1, P0, P1, pf, st.l, dj, vlin, [&]() { midA(n); }); \
        SEG(0); \
        BARRIER_L(); \
        SEG(2); \
        headB(n); \
        attn_B<true, true>(vcur, kcur, o, pf, vpre, kpre, C0, C1, st, RELARGS, [&]() { midB(n); }); \
        tailB(n); \
        SEG(3); \
        if ((n) < 16) { WAIT_BAR(0); } else { BARRIER_L(); }     \
        SEG(4); \
    } while (0)
    unsigned pfd;
    for (int n = 1; n < 17; n += 2) {
        STEP(n, sB0, sB1, sA0, sA1);
        STEP(n + 1, sA0, sA1, sB0, sB1);
    }
#undef STEP
    asm volatile("global_load_dword %0, %1, %2" : "=v"(pfd) : "v"(vlin * 8),
                 "s"((const char*)wo + (size_t)(wid < 2 ? bid * 2 + wid : (bid >> 3) * 4 + ((wid - 2) & 3)) * 8192));
    attn_A<false, true>(kcur, vcur, qf, st.negm, kpre, vpre, sB0, sB1, sA0, sA1, pf, st.l, dj, vlin, [&]() { midA(17); });
    BARRIER_L();
    attn_B<true, false>(vcur, kcur, o, pf, vpre, kpre, sB0, sB1, st, false, false, 0, pend, fpend, [&]() {});
    BARRIER_L();
    SEG_OUT(1);
    if (grp == 0) { BARRIER_L(); }
    STAMP(1, 3);
    unsigned cfl = 0;
    const int ort = (bid & 7) * 4 + ((bid >> 3) & 3);
    if (wid == 0) asm volatile("global_load_dword %0, %1, %2 sc1" : "=v"(cfl) : "v"((lane & 31) * 4), "s"(C.sy + SY_CTX + (ort >> 1) * 32));
    BARRIER_L();
    {
        const float lsum = half_sum(st.l);
        LAS char* xch = (LAS char*)lds_;
        LAS char* mine = xch + wid * 8192;
        auto put = [&](int dd, const f32x16& v) {
#pragma unroll
            for (int q4 = 0; q4 < 4; ++q4)
                *(LAS f32x4*)(mine + (dd * 4 + q4) * 1024 + lane * 16) = (f32x4){v[4 * q4], v[4 * q4 + 1], v[4 * q4 + 2], v[4 * q4 + 3]};
        };
        if (grp) { put(0, o[0]); put(1, o[1]); } else { put(0, o[2]); put(1, o[3]); }
        LAS float* ml = (LAS float*)(xch + 65536);
        ml[wid * 128 + lane] = st.mhat; ml[wid * 128 + 64 + lane] = lsum;
        BARRIER_L();
        const int ow = wid ^ 4;
        const float m2 = ml[ow * 128 + lane], l2 = ml[ow * 128 + 64 + lane];
        const float mm = fmaxf(st.mhat, m2);
        const float f1 = __builtin_amdgcn_exp2f(st.mhat - mm), f2 = __builtin_amdgcn_exp2f(m2 - mm);
        const float inv = 1.0f / (lsum * f1 + l2 * f2);
        const float c1 = f1 * inv, c2 = f2 * inv;
        const LAS char* theirs = xch + ow * 8192;
        auto fin = [&](int dd, f32x16 v) {
#pragma unroll
            for (int q4 = 0; q4 < 4; ++q4) {
                const f32x4 t = *(const LAS f32x4*)(theirs + (dd * 4 + q4) * 1024 + lane * 16);
#pragma unroll
                for (int e = 0; e < 4; ++e) v[4 * q4 + e] = v[4 * q4 + e] * c1 + t[e] * c2;
            }
            store_ctx(blkB, 2 * grp + dd, v, 1.0f);
        };
        if (grp) { fin(0, o[2]); fin(1, o[3]); } else { fin(0, o[0]); fin(1, o[1]); }
    }
    asm volatile("s_waitcnt vmcnt(0)" : "+v"(pfd), "+v"(cfl));
    if (wid == 0) {
        const unsigned bal = (unsigned)__ballot(cfl == C.tag);
        unsigned m0 = 0;
#pragma unroll
        for (int g = 0; g < 8; ++g) m0 |= (((bal >> (4 * g)) & 15u) == 15u ? 1u : 0u) << g;
        if (__builtin_popcount(m0) < 2) m0 = 0;
        if (lane == 0) *(LAS unsigned*)((LAS char*)lds_ + 163840 - 16) = m0;
    }
    BARRIER_L();
    if (tid == 0) { flag_store(C.sy + SY_CTX + blkB * 32 + h, C.tag); flag_store(C.sy + SY_CTX + blkA * 32 + h, C.tag); }
    STAMP(1, 4);
}

constexpr int K3_SLOT = 32768, K3_NS = 5;
DI void phase_oproj(const Params& P, const Ctx& C, const Routed& R) {
    char* ws = P.ws; char* lds_ = C.lds_;
    const lptr lds = C.lds;
    const unsigned lds0 = C.lds0;
    const int lane = C.lane, l31 = lane & 31, hi = lane >> 5, wid = C.wid;
    const int bid = C.bid, rt = (bid & 7) * 4 + ((bid >> 3) & 3), ct = bid >> 5;
    STAMP(2, 0);
    const unsigned m0r = __builtin_amdgcn_readfirstlane(*(const LAS unsigned*)((LAS char*)lds_ + 163840 - 16));
    const unsigned m0 = m0r ? m0r : 0xFFu;
    const int nAg = __builtin_popcount(m0);
    const int rot = 0;
    unsigned ord = 0;
    { int u = 0;
#pragma unroll
      for (int k = 0; k < 8; ++k) { const int g = (k + rot) & 7; if ((m0 >> g) & 1) { ord |= (unsigned)g << (4 * u); ++u; } }
#pragma unroll
      for (int k = 0; k < 8; ++k) { const int g = (k + rot) & 7; if (!((m0 >> g) & 1)) { ord |= (unsigned)g << (4 * u); ++u; } } }
    if (wid >= 4) {
        const int lw = wid - 4;
        const char* csrc = ws + WS_CTX + (size_t)(2 * rt + (lw >> 1)) * 32 * 8192 + (lw & 1) * 4096;
        const char* wsrc = (const char*)P.wo + ct * 128;
        unsigned voffW[4];
#pragma unroll
        for (int j = 0; j < 4; ++j) { const int row = (lw * 4 + j) * 8 + (lane >> 3);
            voffW[j] = row * 1024 + (((lane & 7) ^ (((row >> 1) & 1) << 2)) << 4); }
        const unsigned vlin = lane * 16;
        auto head = [&](int t) { return (int)(4 * ((ord >> (4 * (t >> 2))) & 7) + (t & 3)); };
        auto stageW = [&](int t, int slot) {
            const unsigned b = lds0 + slot * K3_SLOT + lw * 4096;
#pragma unroll
            for (int j = 0; j < 4; ++j) glds16(wsrc + (size_t)head(t) * 131072, voffW[j], b + 16384 + j * 1024);
        };
        auto stageC = [&](int t, int slot) { glds16x4_dev(csrc + (size_t)head(t) * 8192, vlin, lds0 + slot * K3_SLOT + lw * 4096); };
        if (!m0r) asm volatile("s_barrier" ::: "memory");
#pragma unroll
        for (int t = 0; t < 4; ++t) { stageC(t, t); stageW(t, t); }
        int slot = 4;
        for (int hh = 0; hh < 28; ++hh) {
            WAIT_BAR(24);
            stageC(hh + 4, slot); stageW(hh + 4, slot);
            slot = slot == K3_NS - 1 ? 0 : slot + 1;
        }
        WAIT_BAR(24); WAIT_BAR(16); WAIT_BAR(8); WAIT_BAR(0);
        return;
    }
    const int qg2 = wid >> 1, cb = wid & 1;
    const int i0 = R.sel0, i1 = R.sel1;
    const bool has0 = (i0 >> 6) == rt, has1 = (i1 >> 6) == rt;
    const int q = rt * 64 + qg2 * 32 + l31;
    const unsigned* cflags = C.sy + SY_CTX + (rt >> 1) * 32;
    if (!m0r) { if (wid == 0) wait_flags<1>(cflags, 32, C.tag, lane); asm volatile("s_barrier" ::: "memory"); }
    const float g = __uint_as_float(flag_load((const unsigned*)(ws + WS_G) + q));
    s16x4 xb[4];
#pragma unroll
    for (int q4 = 0; q4 < 4; ++q4) xb[q4] = *(const s16x4*)(P.ax + (size_t)q * DM + ct * 64 + cb * 32 + 8 * q4 + 4 * hi);
    const int tq = (lane & 15) >> 2, tp = lane & 3, tblk = (lane >> 4) & 1;
    const lptr wA = lds + 16384 + (4 * hi + tq) * 128 + ((cb ^ ((tq >> 1) & 1)) << 6) + tblk * 32 + tp * 8;
    const lptr cA = lds + qg2 * 8192 + lane * 16;
    f32x16 accs[8] = {};
    int bo = 0;
    auto run4 = [&](f32x16& acc) {
#pragma unroll 1
        for (int hh = 0; hh < 4; ++hh) {
            asm volatile("s_waitcnt lgkmcnt(0)\n\ts_barrier" ::: "memory");
            bf16x8 cf[8], wf[8];
#pragma unroll
            for (int f = 0; f < 4; ++f) { wf[f] = cat(ldstr(wA + bo + f * 2048), ldstr(wA + bo + f * 2048 + 1024)); cf[f] = ldsr128(cA + bo + f * 1024); }
            SBAR();
#pragma unroll
            for (int f = 0; f < 8; ++f) {
                acc = MFMA(wf[f], cf[f], acc);
                if (f + 4 < 8) { wf[f + 4] = cat(ldstr(wA + bo + (f + 4) * 2048), ldstr(wA + bo + (f + 4) * 2048 + 1024)); cf[f + 4] = ldsr128(cA + bo + (f + 4) * 1024); }
                SBAR();
            }
            bo = bo == (K3_NS - 1) * K3_SLOT ? 0 : bo + K3_SLOT;
        }
    };
    auto slots = [&](int u0, int u1) {
        for (int u = u0; u < u1; ++u) {
            switch ((ord >> (4 * u)) & 7) {
                case 0: run4(accs[0]); break; case 1: run4(accs[1]); break; case 2: run4(accs[2]); break; case 3: run4(accs[3]); break;
                case 4: run4(accs[4]); break; case 5: run4(accs[5]); break; case 6: run4(accs[6]); break; default: run4(accs[7]); break;
            }
            if (wid == 0 && u == nAg - 2 && nAg < 8) wait_flags<1>(cflags, 32, C.tag, lane);
        }
    };
    unsigned yf[4]; f32x4 yv4[4];
    if (wid == 1) {
#pragma unroll
        for (int j = 0; j < 4; ++j) yf[j] = flag_load(C.sy + SY_Y + lane + 64 * j);
    }
    slots(0, 1);
    STAMP(2, 2);
    if (wid == 1) {
        if (!__all(yf[0] == C.tag && yf[1] == C.tag && yf[2] == C.tag && yf[3] == C.tag)) wait_flags<4>(C.sy + SY_Y, 256, C.tag, lane);
        const rsrc_t ypr = make_rsrc(ws + WS_YP, MOE_WGS * 4096);
#pragma unroll
        for (int j = 0; j < 4; ++j)
            yv4[j] = __builtin_bit_cast(f32x4, __builtin_amdgcn_raw_buffer_load_b128(ypr, (lane + 64 * j) * 4096 + bid * 16, 0, 16));
    }
    slots(1, 3);
    if (wid == 1) {
        f32x4 y = (yv4[0] + yv4[1]) + (yv4[2] + yv4[3]);
#pragma unroll
        for (int e = 0; e < 4; ++e) y[e] = wave_sum(y[e], lane);
        if (lane < 4) store_f32_wt((float*)(ws + WS_Y) + bid * 4 + lane, lane == 0 ? y[0] : lane == 1 ? y[1] : lane == 2 ? y[2] : y[3]);
    }
    slots(3, 6);
    if (wid == 1) {
        asm volatile("s_waitcnt vmcnt(0)" ::: "memory");
        if (lane == 0) flag_store(C.sy + SY_Y2 + bid, C.tag);
    }
    slots(6, 8);
    STAMP(2, 3);
    f32x16 acc = accs[0];
#pragma unroll
    for (int gg = 1; gg < 8; ++gg) acc += accs[gg];
    if (has0 || has1) {
        wait_flags<1>(C.sy + SY_Y2 + ct * 16, 16, C.tag, lane);
        wait_flags<1>(C.sy + SY_Y2 + 128 + ct * 16, 16, C.tag, lane);
    }
    {
        const bool r0 = (q == i0), r1 = (q == i1);
        const float* yv = (const float*)(ws + WS_Y) + (r1 ? 512 : 0) + ct * 64;
        u32x2 w2[4];
#pragma unroll
        for (int q4 = 0; q4 < 4; ++q4) {
            const int ml = cb * 32 + 8 * q4 + 4 * hi;
            float res[4];
#pragma unroll
            for (int e = 0; e < 4; ++e) {
                float b = bf2f((u16)xb[q4][e]);
                if (r0 || r1) b += __uint_as_float(flag_load((const unsigned*)(yv + ml + e))) * (r1 ? R.sig1 : R.sig0);
                res[e] = g * acc[4 * q4 + e] + (1.0f - g) * b;
            }
            w2[q4] = (u32x2){cvtpk(res[0], res[1]), cvtpk(res[2], res[3])};
        }
        const rsrc_t outr = make_rsrc(P.out, SEQ * DM * 2);
#pragma unroll
        for (int k = 0; k < 4; k += 2) {
            auto rx = __builtin_amdgcn_permlane32_swap(w2[k][0], w2[k + 1][0], false, false);
            auto ry = __builtin_amdgcn_permlane32_swap(w2[k][1], w2[k + 1][1], false, false);
            const u32x4 v = {rx[0], ry[0], rx[1], ry[1]};
            __builtin_amdgcn_raw_buffer_store_b128(v, outr, (q * DM + ct * 64 + cb * 32 + 8 * k + 8 * hi) * 2, 0, 16);
        }
    }
    STAMP(2, 4);
}

__global__ __launch_bounds__(512, 2)
void mega_kernel(Params Parg) {
    extern __shared__ __attribute__((aligned(16))) char lds_[];
    const int wid = __builtin_amdgcn_readfirstlane(threadIdx.x >> 6);
    unsigned tag;
    Routed R;
    QPre Q;
    {
        const Params P = load_params();
        unsigned tagv;
        asm volatile("global_load_dword %0, %1, %2 sc1" : "=v"(tagv) : "v"(0), "s"((const unsigned*)(P.ws + WS_SYNC) + SY_EPOCH));
        Ctx C = make_ctx(lds_, P.ws, wid, 0u);
        phase_kv(P, C, Q, tagv);
        tag = __builtin_amdgcn_readfirstlane(C.tag);
    }
    { const Params P = load_params(); phase_attn(P, make_ctx(lds_, P.ws, wid, tag), Q, R); }
    {
        const Params P = load_params();
        const Ctx C = make_ctx(lds_, P.ws, wid, tag);
        phase_oproj(P, C, R);
        if (C.tid == 0) __hip_atomic_fetch_add(C.sy + SY_EPOCH, 1u, __ATOMIC_RELAXED, __HIP_MEMORY_SCOPE_AGENT);
    }
}

extern "C" void kernel_launch(void* const* d_in, const int* in_sizes, int n_in, void* d_out, int out_size, void* d_ws, size_t ws_size,
                              hipStream_t stream) {
    const u16* ax = (const u16*)d_in[0]; const u16* ckv = (const u16*)d_in[1]; const u16* wq = (const u16*)d_in[2];
    const u16* wuk = (const u16*)d_in[3]; const u16* wuv = (const u16*)d_in[4]; const u16* wo = (const u16*)d_in[5];
      const u16* wr = (const u16*)d_in[7]; const u16* wg = (const u16*)d_in[8];
    const u16* wu = (const u16*)d_in[9]; const u16* wd = (const u16*)d_in[10]; const u16* gw = (const u16*)d_in[11];
    char* ws = (char*)d_ws;
    (void)hipFuncSetAttribute((const void*)mega_kernel, hipFuncAttributeMaxDynamicSharedMemorySize, 163840);
    Params P{ax, ckv, wq, wuk, wuv, wo, wr, wg, wu, wd, gw, ws, (u16*)d_out};
    mega_kernel<<<256, 512, 163840, stream>>>(P);
}
```

```cpp
#include <hip/hip_runtime.h>
#include <hip/hip_bf16.h>
#include <stdint.h>
#include <utility>

typedef unsigned short u16;
typedef short bf16x8 __attribute__((ext_vector_type(8)));
typedef short s16x4 __attribute__((ext_vector_type(4)));
typedef short v4i16_t __attribute__((ext_vector_type(4)));
typedef float f32x16 __attribute__((ext_vector_type(16)));
typedef float f32x4 __attribute__((ext_vector_type(4)));
typedef unsigned u32x4 __attribute__((ext_vector_type(4)));
typedef unsigned u32x2 __attribute__((ext_vector_type(2)));
#define LAS __attribute__((address_space(3)))
#define DI __device__ __forceinline__
#define MFMA(a, b, c) __builtin_amdgcn_mfma_f32_32x32x16_bf16((a), (b), (c), 0, 0, 0)
#define MFMA16(a, b, c) __builtin_amdgcn_mfma_f32_16x16x32_bf16((a), (b), (c), 0, 0, 0)
#define SBAR() __builtin_amdgcn_sched_barrier(0)

constexpr int SEQ = 2048, DM = 512, NH = 32, HD = 128, NHD = NH * HD;
constexpr int FF = 1792;
constexpr int MOE_WGS = 224, MOE_COLS = 8;

constexpr size_t WS_KF = 0;
constexpr size_t WS_VF = WS_KF + (size_t)SEQ * NHD * 2;
constexpr size_t WS_CTX = WS_VF + (size_t)SEQ * NHD * 2;
constexpr size_t WS_R = WS_CTX + (size_t)SEQ * NHD * 2;
constexpr size_t WS_G = WS_R + SEQ * 4;
constexpr size_t WS_Y = WS_G + SEQ * 4;
constexpr size_t WS_YP = WS_Y + 4096;
constexpr size_t WS_SYNC = WS_YP + 224 * 4096;
constexpr int SY_EPOCH = 0, SY_KV = 256, SY_R = 512, SY_Y = 768, SY_Y2 = 1024, SY_CTX = 1280;

constexpr size_t WS_DBG = 128u << 20;
#define STAMP(K, i) do {} while (0)
#define SEG_DECL
#define SEG_PARAMS
#define SEG_ARGS
#define SEG_START() do {} while (0)
#define SEG(i) do {} while (0)
#define SEG_OUT(K) do {} while (0)
template <int... Is, class F> DI void static_for_(std::integer_sequence<int, Is...>, F&& f) { (f(std::integral_constant<int, Is>{}), ...); }
template <int N, class F> DI void static_for(F&& f) { static_for_(std::make_integer_sequence<int, N>{}, f); }
DI unsigned cvtpk(float lo, float hi) {
    typedef float f32x2 __attribute__((ext_vector_type(2)));
    typedef __bf16 bf16x2 __attribute__((ext_vector_type(2)));
    f32x2 v = {lo, hi}; bf16x2 b = __builtin_convertvector(v, bf16x2); return __builtin_bit_cast(unsigned, b);
}
DI float bf2f(u16 v) { return __uint_as_float((unsigned)v << 16); }
DI float bfround(float x) { return __uint_as_float(cvtpk(x, x) << 16); }
DI bf16x8 pack8(const f32x16& x, int s, float sc) {
    u32x4 w = {cvtpk(x[8 * s] * sc, x[8 * s + 1] * sc), cvtpk(x[8 * s + 2] * sc, x[8 * s + 3] * sc),
               cvtpk(x[8 * s + 4] * sc, x[8 * s + 5] * sc), cvtpk(x[8 * s + 6] * sc, x[8 * s + 7] * sc)};
    return __builtin_bit_cast(bf16x8, w);
}
DI bf16x8 pack8(const f32x16& x, int s) {
    u32x4 w = {cvtpk(x[8 * s], x[8 * s + 1]), cvtpk(x[8 * s + 2], x[8 * s + 3]),
               cvtpk(x[8 * s + 4], x[8 * s + 5]), cvtpk(x[8 * s + 6], x[8 * s + 7])};
    return __builtin_bit_cast(bf16x8, w);
}
DI void glds16(const void* sbase, unsigned voff, unsigned lds_addr) {
    lds_addr = __builtin_amdgcn_readfirstlane(lds_addr);
    asm volatile("s_mov_b32 m0, %2\n\ts_nop 0\n\tglobal_load_lds_dwordx4 %0, %1" :: "v"(voff), "s"(sbase), "s"(lds_addr) : "memory");
}
DI void glds16x4(const void* sbase, unsigned voff, unsigned lds_addr) {
    lds_addr = __builtin_amdgcn_readfirstlane(lds_addr);
    asm volatile("s_mov_b32 m0, %2\n\ts_nop 0\n\tglobal_load_lds_dwordx4 %0, %1\n\tglobal_load_lds_dwordx4 %0, %1 offset:1024\n\t"
                 "global_load_lds_dwordx4 %0, %1 offset:2048\n\tglobal_load_lds_dwordx4 %0, %1 offset:3072"
                 :: "v"(voff), "s"(sbase), "s"(lds_addr) : "memory");
}
DI unsigned flag_load(const unsigned* p) { return __hip_atomic_load(p, __ATOMIC_RELAXED, __HIP_MEMORY_SCOPE_AGENT); }
DI void flag_store(unsigned* p, unsigned v) { __hip_atomic_store(p, v, __ATOMIC_RELAXED, __HIP_MEMORY_SCOPE_AGENT); }
DI void store_f32_wt(float* p, float v) { __hip_atomic_store((unsigned*)p, __float_as_uint(v), __ATOMIC_RELAXED, __HIP_MEMORY_SCOPE_AGENT); }
template <int NW> DI void wait_flags(const unsigned* f, int n, unsigned tag, int lane) {
    const unsigned long long t0 = __builtin_amdgcn_s_memrealtime();
    for (;;) {
        bool ok = true;
#pragma unroll
        for (int j = 0; j < NW; ++j) { const int i = lane + 64 * j; ok &= (i < n ? flag_load(f + i) : tag) == tag; }
        if (__all(ok)) break;
        if (__builtin_amdgcn_s_memrealtime() - t0 > 5000000ull) break;
        __builtin_amdgcn_s_sleep(2);
    }
}
DI void acquire_agent() { __builtin_amdgcn_fence(__ATOMIC_ACQUIRE, "agent"); }
DI void glds16x4_dev(const void* sbase, unsigned voff, unsigned lds_addr) {
    lds_addr = __builtin_amdgcn_readfirstlane(lds_addr);
    asm volatile("s_mov_b32 m0, %2\n\ts_nop 0\n\tglobal_load_lds_dwordx4 %0, %1 sc1\n\tglobal_load_lds_dwordx4 %0, %1 offset:1024 sc1\n\t"
                 "global_load_lds_dwordx4 %0, %1 offset:2048 sc1\n\tglobal_load_lds_dwordx4 %0, %1 offset:3072 sc1"
                 :: "v"(voff), "s"(sbase), "s"(lds_addr) : "memory");
}
#define WAIT_BAR(N) asm volatile("s_waitcnt vmcnt(" #N ") lgkmcnt(0)\n\ts_barrier" ::: "memory")
#define WAIT_CNT(c) do { if ((c) == 8) { WAIT_BAR(8); } else if ((c) == 4) { WAIT_BAR(4); } else { WAIT_BAR(0); } } while (0)
DI int lane_now() { int l; asm volatile("v_mbcnt_lo_u32_b32 %0, -1, 0\n\tv_mbcnt_hi_u32_b32 %0, -1, %0" : "=v"(l)); return l; }
typedef LAS const char* lptr;
DI bf16x8 ldsr128(lptr p) { return *(const LAS bf16x8*)p; }
DI bf16x8 ldsr128a(lptr p) { return *(const LAS bf16x8*)p; }
DI s16x4 ldstr(lptr p) { return __builtin_bit_cast(s16x4, __builtin_amdgcn_ds_read_tr16_b64_v4i16((LAS v4i16_t*)p)); }
DI bf16x8 cat(s16x4 a, s16x4 b) { return __builtin_shufflevector(a, b, 0, 1, 2, 3, 4, 5, 6, 7); }
DI unsigned shx(unsigned v, int m, int lane) { return (unsigned)__builtin_amdgcn_ds_bpermute((lane ^ m) << 2, (int)v); }
DI float shx(float v, int m, int lane) { return __uint_as_float(shx(__float_as_uint(v), m, lane)); }
template <int CTRL> DI unsigned dppu(unsigned v) { return (unsigned)__builtin_amdgcn_update_dpp(0, (int)v, CTRL, 0xF, 0xF, true); }
template <int CTRL> DI float dppf(float v) { return __uint_as_float(dppu<CTRL>(__float_as_uint(v))); }
DI float wave_sum(float v, int lane) {
#pragma unroll
    for (int o = 32; o >= 1; o >>= 1) v += shx(v, o, lane);
    return v;
}
DI float sigmoidf_(float x) { return 1.0f / (1.0f + __expf(-x)); }

constexpr int K1_STAGE = 49152, K1_LDS = 163840;
typedef __amdgpu_buffer_rsrc_t rsrc_t;
DI rsrc_t make_rsrc(const void* p, unsigned bytes) { return __builtin_amdgcn_make_buffer_rsrc((void*)p, (short)0, (int)bytes, 0x00020000); }
DI void store_wt(rsrc_t r, unsigned off, bf16x8 v) {
    __builtin_amdgcn_raw_buffer_store_b128(__builtin_bit_cast(u32x4, v), r, (int)off, 0, 16);
}
#define WAIT_VM(N) asm volatile("s_waitcnt vmcnt(" #N ")" ::: "memory")

template <bool ISV, class Pre, class Mid, class Tail, class St>
DI void kv_body(const char* cbase, const char* kbase, const char* vbase, rsrc_t outr, unsigned outoff, lptr lds, unsigned lds0, int wid, int lane,
                Pre&& pre, Mid&& mid, Tail&& tail, St&& stamp2) {
    const int r16 = lane & 15, q4 = lane >> 4;
    unsigned voffW[2], voffC[2], voffG[2];
#pragma unroll
    for (int j = 0; j < 2; ++j) {
        const int n = 2 * wid + j;
        { const int row = 4 * n + (lane >> 4); voffW[j] = row * 8192 + (((lane & 15) ^ ((row & 3) << 2) ^ (((row >> 3) & 1) << 1)) << 4); }
        { const int row = 8 * n + (lane >> 3); voffC[j] = row * 1024 + (((lane & 7) ^ ((row >> 1) & 7)) << 4); }
        voffG[j] = (lane ^ ((wid + 8 * j) & 15)) << 4;
    }
    auto stage = [&](int s) {
        const unsigned b = lds0 + (s % 3) * K1_STAGE + 2 * wid * 1024;
#pragma unroll
        for (int j = 0; j < 2; ++j) glds16(kbase + (size_t)s * 64 * 8192, voffW[j], b + j * 1024);
#pragma unroll
        for (int j = 0; j < 2; ++j) glds16(vbase + (size_t)s * 64 * 8192, voffW[j], b + 16384 + j * 1024);
#pragma unroll
        for (int j = 0; j < 2; ++j) glds16(cbase + s * 128, voffC[j], b + 32768 + j * 1024);
    };
    auto gbuf = [&](int k) { return k == 4 ? 98304 : k == 5 ? 131072 : k == 6 ? 0 : 49152; };
    const int tq = (lane & 15) >> 2, tp = lane & 3;
    lptr wA[2], cA[2];
#pragma unroll
    for (int b = 0; b < 2; ++b) {
        wA[b] = lds + (ISV ? 16384 : 0) + (8 * q4 + tq) * 256 + (((wid & 3) ^ tq) << 6) + ((b ^ (q4 & 1)) << 5) + tp * 8;
        cA[b] = lds + 32768 + r16 * 128 + (((4 * b + q4) ^ (r16 >> 1)) << 4);
    }
    auto mm = [&](const bf16x8& w, const bf16x8& c, const f32x4& acc) { return ISV ? MFMA16(c, w, acc) : MFMA16(w, c, acc); };
    const unsigned olane = outoff + (q4 >> 1) * 1024 + (ISV ? (r16 + 32 * (q4 & 1)) * 16 : (16 * (r16 >> 3) + (r16 & 7) + 32 * (q4 & 1)) * 16);
    auto out_tile = [&](int kg, const f32x4& t00, const f32x4& t01, const f32x4& t10, const f32x4& t11) {
        auto pk = [](const f32x4& x, const f32x4& y) { u32x4 w = {cvtpk(x[0], x[1]), cvtpk(x[2], x[3]), cvtpk(y[0], y[1]), cvtpk(y[2], y[3])}; return __builtin_bit_cast(bf16x8, w); };
        if (ISV) { store_wt(outr, olane + kg * 8192, pk(t00, t10)); store_wt(outr, olane + kg * 8192 + 256, pk(t01, t11)); }
        else     { store_wt(outr, olane + kg * 8192, pk(t00, t01)); store_wt(outr, olane + kg * 8192 + 128, pk(t10, t11)); }
    };

    bf16x8 wf[16][2];
    f32x4 acc[8][2] = {};
    lptr gA[4];
    stage(0); stage(1); stage(2);
    pre();
    auto rdW = [&](int s, int sl) { const int o = (s % 3) * K1_STAGE + sl * 8192;
#pragma unroll
        for (int b = 0; b < 2; ++b) wf[2 * s + sl][b] = cat(ldstr(wA[b] + o), ldstr(wA[b] + o + 1024)); };
    auto rdC = [&](int s, int i) { return ldsr128(cA[i >> 3] + (s % 3) * K1_STAGE + (i & 7) * 2048); };
    auto rdG = [&](int k, int ks) { return ldsr128(gA[(ks >> 1) & 3] + gbuf(k) + (ks & 1) * 16384 + (ks >> 3) * 256); };
    auto dmaS = [&](int s, int j) {
        const unsigned b = lds0 + (s % 3) * K1_STAGE + 2 * wid * 1024;
        if (j < 2) glds16(kbase + (size_t)s * 64 * 8192, voffW[j], b + j * 1024);
        else if (j < 4) glds16(vbase + (size_t)s * 64 * 8192, voffW[j - 2], b + 16384 + (j - 2) * 1024);
        else glds16(cbase + s * 128, voffC[j - 4], b + 32768 + (j - 4) * 1024);
    };
    auto dmaG = [&](int k, int j) { glds16(cbase + (size_t)(32 * k + wid + 8 * j) * 1024, voffG[j & 1], lds0 + gbuf(k) + (wid + 8 * j) * 1024); };
    bf16x8 cf[8];
    WAIT_BAR(12);
    rdW(0, 0);
#pragma unroll
    for (int i = 0; i < 4; ++i) cf[i] = rdC(0, i);
    static_for<128>([&](auto SI) {
        {
            constexpr int s = SI / 16, i = SI % 16;
            if (i == 12) {
                if (s < 6) { WAIT_BAR(6); } else if (s == 6) { WAIT_BAR(8); } else { WAIT_BAR(8); }
            }
            if (s == 7 && i == 11) {
                const int ln = lane_now();
#pragma unroll
                for (int j = 0; j < 4; ++j) gA[j] = lds + (ln & 15) * 1024 + (((4 * j + (ln >> 4)) ^ (ln & 15)) << 4);
            }
            if (s == 1 && i == 13) mid();
            constexpr int nx = i + 4;
            if constexpr (nx < 16) { if (nx == 8) rdW(s, 1); cf[nx & 7] = rdC(s, nx); }
            else if constexpr (s < 7) { if (nx == 16) rdW(s + 1, 0); cf[nx & 7] = rdC(s + 1, nx - 16); }
            else cf[nx & 7] = rdG(4, nx - 16);
            acc[i & 7][0] = mm(wf[2 * s + (i >> 3)][0], cf[i & 7], acc[i & 7][0]);
            acc[i & 7][1] = mm(wf[2 * s + (i >> 3)][1], cf[i & 7], acc[i & 7][1]);
            if (s + 3 < 8) { if (i == 12 || i == 14) dmaS(s + 3, (i - 12) / 2); }
            else if (i >= 12) {
                const int j = i - 12;
                if (s == 5) dmaG(4, j);
                else if (s == 6) dmaG(6, j);
                else dmaG(7, j);
            }
            if (i < 12 && i % 3 == 0) { if (s >= 1 && s + 2 < 8) dmaS(s + 2, 2 + i / 3); }
            if (s == 6 && i >= 2 && i < 10 && !(i & 1)) dmaG(5, (i - 2) / 2);
            if (s == 7 && i == 15) out_tile(0, acc[0][0], acc[0][1], acc[1][0], acc[1][1]);
            SBAR();
        }
    });
    stamp2();
    f32x4 a2[2][2][2];
    static_for<128>([&](auto KI) {
        {
            constexpr int k = 4 + KI / 32, ks = KI % 32;
            if (ks == 28 && k < 7) {
                if (k == 4) { WAIT_BAR(16); } else if (k == 5) { WAIT_BAR(16); } else { WAIT_BAR(21); }
            }
            constexpr int nx = ks + 4;
            if constexpr (nx < 32) cf[nx & 7] = rdG(k, nx); else if constexpr (k < 7) cf[nx & 7] = rdG(k + 1, nx - 32);
#pragma unroll
            for (int b = 0; b < 2; ++b) a2[k & 1][ks & 1][b] = mm(wf[ks >> 1][b], cf[ks & 7], ks < 2 ? f32x4{} : a2[k & 1][ks & 1][b]);
            if constexpr (k == 4) { if constexpr (ks < 3) out_tile(ks + 1, acc[2 * ks + 2][0], acc[2 * ks + 2][1], acc[2 * ks + 3][0], acc[2 * ks + 3][1]); }
            else if constexpr (ks == 3) out_tile(k - 1, a2[(k - 1) & 1][0][0], a2[(k - 1) & 1][0][1], a2[(k - 1) & 1][1][0], a2[(k - 1) & 1][1][1]);
            tail(KI);
            SBAR();
        }
    });
    out_tile(7, a2[1][0][0], a2[1][0][1], a2[1][1][0], a2[1][1][1]);
}

DI void ld_u16(unsigned& d, const void* sbase, unsigned voff) { asm volatile("global_load_ushort %0, %1, %2" : "=v"(d) : "v"(voff), "s"(sbase)); }
DI void ld_x4(u32x4& d, const void* sbase, unsigned voff) { asm volatile("global_load_dwordx4 %0, %1, %2" : "=v"(d) : "v"(voff), "s"(sbase)); }
DI void ld_u16_nop(unsigned& d, const void* sbase, unsigned voff) { asm volatile("s_nop 4\n\tglobal_load_ushort %0, %1, %2" : "=v"(d) : "v"(voff), "s"(sbase)); }
DI void ld_x4_nop(u32x4& d, const void* sbase, unsigned voff) { asm volatile("s_nop 4\n\tglobal_load_dwordx4 %0, %1, %2" : "=v"(d) : "v"(voff), "s"(sbase)); }
struct Params {
    const u16 *ax, *ckv, *wq, *wuk, *wuv, *wo, *wr, *wg, *wu, *wd, *gw;
    char* ws; u16* out;
};
struct Ctx { lptr lds; char* lds_; unsigned lds0; int tid, lane, wid, bid; unsigned tag; unsigned* sy; };
typedef __attribute__((address_space(4))) const Params* kparams_t;
DI Params load_params() {
    kparams_t pp = (kparams_t)__builtin_amdgcn_kernarg_segment_ptr();
    asm volatile("" : "+s"(pp));
    Params P;
    P.ax = pp->ax; P.ckv = pp->ckv; P.wq = pp->wq; P.wuk = pp->wuk; P.wuv = pp->wuv; P.wo = pp->wo; P.wr = pp->wr; P.wg = pp->wg; P.wu = pp->wu;
    P.wd = pp->wd; P.gw = pp->gw; P.ws = pp->ws; P.out = pp->out;
    return P;
}
DI Ctx make_ctx(char* lds_, char* ws, int wid, unsigned tag) {
    Ctx C;
    C.lds = (lptr)lds_; C.lds_ = lds_; C.lds0 = (unsigned)(uintptr_t)C.lds;
    int lane; asm volatile("v_mbcnt_lo_u32_b32 %0, -1, 0\n\tv_mbcnt_hi_u32_b32 %0, -1, %0" : "=v"(lane));
    asm volatile("" : "+s"(wid), "+s"(tag));
    C.lane = lane; C.wid = wid; C.tid = wid * 64 + lane; C.bid = blockIdx.x; C.tag = tag; C.sy = (unsigned*)(ws + WS_SYNC);
    return C;
}

#define GU_K 5
#define GU_KS 8
constexpr int QST = 49152;
struct QPre { unsigned voffX[4], voffW[2]; u32x4 rv, wgv, wuv; };
constexpr int MOE_WD = 147456 + 2048;
DI int moe_f0(int bid) { return ((bid & 7) * 28 + ((bid >> 3) < 28 ? (bid >> 3) : 27)) * MOE_COLS; }
DI void q_dma(const QPre& Q, const Params& P, const Ctx& C, int s, int j) {
    const int wid = C.wid, h = (C.bid & 7) * 4 + ((C.bid >> 3) & 3), bi = C.bid >> 5;
    const char* xbase = (const char*)P.ax + (size_t)(((wid >> 2) ? 15 - bi : bi) * 128 + (wid & 3) * 32) * 1024;
    const char* wbase = (const char*)P.wq + h * 256;
    const unsigned b = C.lds0 + ((s + 2) % 3) * QST;
    if (j < 2) glds16(wbase + (size_t)s * 64 * 8192, Q.voffW[j], b + 32768 + (2 * wid + j) * 1024);
    else glds16(xbase + s * 128, Q.voffX[j - 2], b + (4 * wid + j - 2) * 1024);
}
DI void phase_kv(const Params& P, Ctx& C, QPre& Q, unsigned tagv) {
    char* ws = P.ws;
    const int lane = C.lane, wid = C.wid, bid = C.bid, h = (bid & 7) * 4 + ((bid >> 3) & 3), p = bid >> 5;
    STAMP(0, 0);
    const int tok = bid * 8 + wid;
    const bf16x8 bxv = *(const bf16x8*)(P.ax + tok * DM + lane * 8), wrv = *(const bf16x8*)(P.wr + lane * 8);
    const bf16x8 gwv = *(const bf16x8*)(P.gw + lane * 8);
    auto pre = [&]() {
        float sr = 0.f, sg = 0.f;
#pragma unroll
        for (int j = 0; j < 8; ++j) { sr += bf2f((u16)bxv[j]) * bf2f((u16)wrv[j]); sg += bf2f((u16)bxv[j]) * bf2f((u16)gwv[j]); }
        sr = wave_sum(sr, lane); sg = wave_sum(sg, lane);
        { unsigned t;
          asm volatile("s_waitcnt vmcnt(18)\n\tv_readfirstlane_b32 %0, %1" : "=s"(t) : "v"(tagv));
          C.tag = t + 1u; }
        if (lane == 0) { store_f32_wt((float*)(ws + WS_R) + tok, bfround(sr)); store_f32_wt((float*)(ws + WS_G) + tok, sigmoidf_(sg)); }
    };
    auto mid = [&]() { if (C.tid == 0) flag_store(C.sy + SY_R + bid, C.tag); };
    const char* cbase = (const char*)P.ckv + (size_t)p * 256 * 1024;
    const char* kbase = (const char*)P.wuk + h * 256;
    const char* vbase = (const char*)P.wuv + h * 256;
    const unsigned outoff = (unsigned)((h * 64 + 8 * p) * 8 + 2 * (wid & 3)) * 1024;
    const unsigned kvbytes = (unsigned)((size_t)SEQ * NHD * 2);
    unsigned rp[4];
    auto tail = [&](auto KI) {
        constexpr int k = 4 + KI / 32, ks = KI % 32;
        if constexpr (k == 4 && ks == 20) {
            const int ln = lane_now();
#pragma unroll
            for (int j = 0; j < 4; ++j) { const int r = 8 * j + (ln >> 3); Q.voffX[j] = r * 1024 + (((ln & 7) ^ ((r >> 1) & 7)) << 4); }
#pragma unroll
            for (int j = 0; j < 2; ++j) { const int row = 4 * (2 * wid + j) + (ln >> 4); Q.voffW[j] = row * 8192 + (((ln & 15) ^ ((row & 3) << 2)) << 4); }
            if (wid == 0) {
#pragma unroll
                for (int j = 0; j < 4; ++j) asm volatile("global_load_dword %0, %1, %2 sc1" : "=v"(rp[j]) : "v"(ln * 4), "s"(C.sy + SY_R + 64 * j));
            }
        }
        if constexpr (k == GU_K && ks == GU_KS) ld_x4(Q.wgv, (const char*)P.wg + moe_f0(bid) * 2, (wid * 64 + lane_now()) * (FF * 2));
        if constexpr (k == GU_K && ks == GU_KS + 1) ld_x4(Q.wuv, (const char*)P.wu + moe_f0(bid) * 2, (wid * 64 + lane_now()) * (FF * 2));
        if constexpr (k == 6 && ks == 8) glds16((const char*)P.wd + (size_t)(moe_f0(bid) + wid) * 1024, lane_now() * 16, C.lds0 + MOE_WD + wid * 1024);
        if constexpr (k == 5 && ks >= 28) q_dma(Q, P, C, 0, ks - 28);
        if constexpr (k == 6 && ks < 2) q_dma(Q, P, C, 0, 4 + ks);
        if constexpr (k == 6 && ks == 20) {
            if (wid == 0) {
                asm volatile("s_waitcnt vmcnt(13)" : "+v"(rp[0]), "+v"(rp[1]), "+v"(rp[2]), "+v"(rp[3]));
                if (__builtin_expect(!__all(rp[0] == C.tag && rp[1] == C.tag && rp[2] == C.tag && rp[3] == C.tag), 0)) wait_flags<4>(C.sy + SY_R, 256, C.tag, lane_now());
            }
        }
        if constexpr (k == 6 && ks >= 28) q_dma(Q, P, C, 1, ks - 28);
        if constexpr (k == 7 && ks < 2) q_dma(Q, P, C, 1, 4 + ks);
        if constexpr (k == 7 && ks == 5) asm volatile("global_load_dwordx4 %0, %1, %2 sc1" : "=v"(Q.rv) : "v"((wid * 64 + lane_now()) * 16), "s"(P.ws + WS_R));
    };
    auto st2 = [&]() { };
    if (wid < 4) kv_body<false>(cbase, kbase, vbase, make_rsrc(ws + WS_KF, kvbytes), outoff, C.lds, C.lds0, wid, lane, pre, mid, tail, st2);
    else         kv_body<true>(cbase, kbase, vbase, make_rsrc(ws + WS_VF, kvbytes), outoff, C.lds, C.lds0, wid, lane, pre, mid, tail, st2);
    STAMP(0, 3);
}

DI unsigned umax_(unsigned a, unsigned b) { return a > b ? a : b; }
DI unsigned umin_(unsigned a, unsigned b) { return a < b ? a : b; }
DI float lo16(unsigned v) { return __uint_as_float(v << 16); }
DI float hi16(unsigned v) { return __uint_as_float(v & 0xFFFF0000u); }

constexpr int K2_QST = 49152, K2_SLOT = 32768, K2_QB = 131072, K2_LDS = 163840;
constexpr float QSCALE = 0.08838834764831845f * 1.4426950408889634f;
constexpr float THR = 8.0f;
constexpr int RING0 = 3;

#define SETPRIO(x) __builtin_amdgcn_s_setprio(x)
struct SMState { float mhat, l; f32x16 negm; };
#define PIN(x) asm volatile("" : "+v"(x))
DI float max3(float a, float b, float c) { return fmaxf(fmaxf(a, b), c); }

#define EXP2(x) __builtin_amdgcn_exp2f(x)
#define VOFF(i) (((i) >> 3) * 8192 + (2 * ((i) & 3) + (((i) >> 2) & 1)) * 1024)
#define KOFF(i) (((i) & 1) * 8192 + ((i) >> 1) * 1024)
DI float fadd_s(float a, float b) { float r; asm("v_add_f32_e32 %0, %1, %2" : "=v"(r) : "v"(a), "v"(b)); return r; }
DI float max3_s(float a, float b, float c) { float r; asm("v_max3_f32 %0, %1, %2, %3" : "=v"(r) : "v"(a), "v"(b), "v"(c)); return r; }
DI float max2_s(float a, float b) { float r; asm("v_max_f32_e32 %0, %1, %2" : "=v"(r) : "v"(a), "v"(b)); return r; }
template <int OFF> DI void glds_step(const void* sbase, unsigned voff, unsigned lds_addr) {
    if (OFF == 0) asm volatile("s_mov_b32 m0, %2\n\ts_nop 0\n\tglobal_load_lds_dwordx4 %0, %1 sc1" :: "v"(voff), "s"(sbase), "s"(lds_addr));
    else asm volatile("global_load_lds_dwordx4 %0, %1 offset:%c2 sc1" :: "v"(voff), "s"(sbase), "i"(OFF));
}
DI f32x16 mfma_c(bf16x8 a, bf16x8 b, const f32x16& c) {
    f32x16 d; asm volatile("v_mfma_f32_32x32x16_bf16 %0, %1, %2, %3" : "=&v"(d) : "v"(a), "v"(b), "v"(c)); return d;
}
struct DmaJob { const char* src0; const char* src1; unsigned dst0, dst1; bool has0, has1; };

#define LA 4
template <bool QK, bool FIN, class Mid>
DI void attn_A(lptr kp, const lptr& vpn, const bf16x8 (&qf)[8], const f32x16& negm, const bf16x8 (&kpre)[LA], bf16x8 (&vpre)[LA], f32x16& s0, f32x16& s1,
               const f32x16& p0, const f32x16& p1, bf16x8 (&pf)[4], float& l, const DmaJob& dj, unsigned vlin, Mid&& mid) {
    bf16x8 fr[16];
    unsigned pw[16];
    float sa = 0.f, sb = 0.f;
#pragma unroll
    for (int i = 0; i < LA; ++i) fr[i] = kpre[i];
#pragma unroll
    for (int i = 0; i < 16; ++i) {
        if (QK && i + LA < 16) fr[i + LA] = ldsr128a(kp + KOFF(i + LA));
        if (i + LA >= 16) vpre[i + LA - 16] = ldsr128a(vpn + VOFF(i + LA - 16));
        if (QK) {
            if (i == 0) s0 = mfma_c(fr[0], qf[0], negm);
            else if (i == 1) s1 = mfma_c(fr[1], qf[0], negm);
            else if (i & 1) s1 = MFMA(fr[i], qf[i >> 1], s1);
            else s0 = MFMA(fr[i], qf[i >> 1], s0);
        }
        if (FIN) {
            const float e0 = i < 8 ? p0[2 * i] : p1[2 * i - 16], e1 = i < 8 ? p0[2 * i + 1] : p1[2 * i - 15];
            if (i == 0) { sa = e0; sb = e1; } else { sa = fadd_s(sa, e0); sb = fadd_s(sb, e1); PIN(sa); PIN(sb); }
            pw[i] = cvtpk(e0, e1);
            PIN(pw[i]);
        }
        if (i == 3) mid();
        if (QK) {
            if (i == 1 && dj.has0) { glds_step<0>(dj.src0, vlin, dj.dst0); glds_step<1024>(dj.src0, vlin, dj.dst0); }
            if (i == 5 && dj.has0) { glds_step<2048>(dj.src0, vlin, dj.dst0); glds_step<3072>(dj.src0, vlin, dj.dst0); }
            if (i == 9 && dj.has1) { glds_step<0>(dj.src1, vlin, dj.dst1); glds_step<1024>(dj.src1, vlin, dj.dst1); }
            if (i == 13 && dj.has1) { glds_step<2048>(dj.src1, vlin, dj.dst1); glds_step<3072>(dj.src1, vlin, dj.dst1); }
        }
        SBAR();
    }
    if (FIN) {
        l += sa + sb;
#pragma unroll
        for (int i = 0; i < 4; ++i) { u32x4 w = {pw[4 * i], pw[4 * i + 1], pw[4 * i + 2], pw[4 * i + 3]}; pf[i] = __builtin_bit_cast(bf16x8, w); }
    }
}
DI void dma_plain(const DmaJob& dj, unsigned vlin) {
    if (dj.has0) glds16x4_dev(dj.src0, vlin, dj.dst0);
    if (dj.has1) glds16x4_dev(dj.src1, vlin, dj.dst1);
}
template <bool PV, bool SM, class Mid>
DI void attn_B(lptr vp, const lptr& kpn, f32x16 (&o)[4], const bf16x8 (&pf)[4], const bf16x8 (&vpre)[LA], bf16x8 (&kpre)[LA],
               f32x16& s0, f32x16& s1, SMState& st, bool first, bool mask, int qrel  , bool& pend, float& fpend, Mid&& mid) {
    bf16x8 fr[16];
    float ma = 0.f, mb = 0.f, mc = 0.f;
#pragma unroll
    for (int i = 0; i < LA; ++i) fr[i] = vpre[i];
    if (SM && __builtin_expect(mask, 0)) {
        const float NEG = -__builtin_inff();
#pragma unroll
        for (int r = 0; r < 16; ++r) { const int c = (r & 3) + 8 * (r >> 3) + 16 * ((r >> 2) & 1);
            if (c > qrel) s0[r] = NEG;
            if (c + 32 > qrel) s1[r] = NEG; }
    }
    SETPRIO(1);
    static_for<16>([&](auto I) {
        constexpr int i = I;
        if (PV && i + LA < 16) fr[i + LA] = ldsr128a(vp + VOFF(i + LA));
        if (i + LA >= 16) kpre[i + LA - 16] = ldsr128a(kpn + KOFF(i + LA - 16));
        if (PV) o[i & 3] = MFMA(fr[i], pf[i >> 2], o[i & 3]);
        if (SM) {
            if (i == 0) { ma = max3(s0[0], s0[1], s0[2]); mb = max3(s1[0], s1[1], s1[2]); mc = max3(s0[3], s0[4], s1[3]); PIN(ma); PIN(mb); PIN(mc); }
            if (i == 1) { ma = max3_s(ma, s0[5], s0[6]); mb = max3_s(mb, s1[4], s1[5]); mc = max3_s(mc, s0[7], s1[6]); PIN(ma); PIN(mb); PIN(mc); }
            if (i == 2) { ma = max3_s(ma, s0[8], s0[9]); mb = max3_s(mb, s1[7], s1[8]); mc = max3_s(mc, s0[10], s1[9]); PIN(ma); PIN(mb); PIN(mc); }
            if (i == 3) { ma = max3_s(ma, s0[11], s0[12]); mb = max3_s(mb, s1[10], s1[11]); mc = max3_s(mc, s0[13], s1[12]); PIN(ma); PIN(mb); PIN(mc); }
            if (i == 4) { ma = max3_s(ma, s0[14], s0[15]); mb = max3_s(mb, s1[13], s1[14]); mc = max2_s(mc, s1[15]); PIN(ma); PIN(mb); PIN(mc); }
            if (i == 5) {
                float rm = max3_s(ma, mb, mc);
                auto rr = __builtin_amdgcn_permlane32_swap(__float_as_uint(rm), __float_as_uint(rm), false, false);
                rm = max2_s(__uint_as_float(rr[0]), __uint_as_float(rr[1]));
                if (__builtin_expect(first || __any(rm > THR), 0)) {
                    const float dl = first ? rm : fmaxf(rm, 0.f), f = first ? 1.0f : __builtin_amdgcn_exp2f(-dl);
                    st.mhat += dl; st.l *= f;
#pragma unroll
                    for (int r = 0; r < 16; ++r) { s0[r] -= dl; s1[r] -= dl; st.negm[r] = -st.mhat; }
                    pend = !first; fpend = f;
                }
            }
            if (i >= 6) {
                constexpr int e0 = i < 14 ? 3 * (i - 6) : 24 + 4 * (i - 14), e1 = i < 14 ? e0 + 3 : e0 + 4;
#pragma unroll
                for (int e = e0; e < e1; ++e) { if (e < 16) { s0[e] = EXP2(s0[e]); } else { s1[e - 16] = EXP2(s1[e - 16]); } }
                PIN(s0); PIN(s1);
            }
        }
        if (i == 7) mid();
        SBAR();
    });
    SETPRIO(0);
}

struct Routed { int sel0, sel1; float sig0, sig1; };
DI void phase_attn(const Params& P, const Ctx& C, QPre& Q, Routed& R) {
    char* ws = P.ws; char* lds_ = C.lds_;
    const u16 *x = P.ax, *wq = P.wq, *wo = P.wo, *bx = P.ax, *wg = P.wg, *wu = P.wu, *wd = P.wd;
    const lptr lds = C.lds;
    const unsigned lds0 = C.lds0;
    const int tid = C.tid, lane = C.lane, l31 = lane & 31, hi = lane >> 5, wid = C.wid;
    const int bid = C.bid, h = (bid & 7) * 4 + ((bid >> 3) & 3), bi = bid >> 5;
    const int grp = wid >> 2, rg = wid & 3;
    const int blkA = bi, blkB = 15 - bi;

    STAMP(1, 0);
    LAS unsigned* sk = (LAS unsigned*)((LAS char*)lds_ + 147456);
    LAS float* hs = (LAS float*)((LAS char*)lds_ + 147456 + 128);
    LAS float* red = (LAS float*)((LAS char*)lds_ + 147456 + 256);
    const int cg = moe_f0(bid) / MOE_COLS;
    unsigned k1 = 0, k2 = 0, xs0, xs1;
    u32x4 skv[4];
    float mv[32], ma0, ma1, hsv[16];
    unsigned wdv[8];
    auto top2 = [&](unsigned b1, unsigned b2) { k2 = umax_(umin_(k1, b1), umax_(k2, b2)); k1 = umax_(k1, b1); };
    auto ins = [&](unsigned key) { k2 = umax_(k2, umin_(k1, key)); k1 = umax_(k1, key); };
    auto moe_gap = [&](auto GI) {
        constexpr int G = GI, s = G / 16, i = G % 16;
        if constexpr (G == 13 || G == 14) {
            if constexpr (G == 13) asm volatile("" : "+v"(Q.rv));
#pragma unroll
            for (int j = 2 * (G - 13); j < 2 * (G - 13) + 2; ++j) {
                const unsigned u = Q.rv[j] >> 16;
                const unsigned o = (u & 0x8000u) ? (~u & 0xFFFFu) : (u | 0x8000u);
                ins((o << 16) | (unsigned)(0xFFFF - (tid * 4 + j)));
            }
            PIN(k1); PIN(k2);
        }
        if constexpr (G == 15) { top2(dppu<0x128>(k1), dppu<0x128>(k2)); PIN(k1); PIN(k2); }
        if constexpr (G == 16) { top2(dppu<0x124>(k1), dppu<0x124>(k2)); PIN(k1); PIN(k2); }
        if constexpr (G == 17) { top2(dppu<0x122>(k1), dppu<0x122>(k2)); PIN(k1); PIN(k2); }
        if constexpr (G == 18) { top2(dppu<0x121>(k1), dppu<0x121>(k2)); PIN(k1); PIN(k2); }
        if constexpr (G == 19) { auto a = __builtin_amdgcn_permlane16_swap(k1, k1, false, false); auto b = __builtin_amdgcn_permlane16_swap(k2, k2, false, false);
            k1 = a[0]; k2 = b[0]; top2(a[1], b[1]); PIN(k1); PIN(k2); }
        if constexpr (G == 20) { auto a = __builtin_amdgcn_permlane32_swap(k1, k1, false, false); auto b = __builtin_amdgcn_permlane32_swap(k2, k2, false, false);
            k1 = a[0]; k2 = b[0]; top2(a[1], b[1]);
            if (lane == 0) { sk[wid * 2] = k1; sk[wid * 2 + 1] = k2; } }
        if constexpr (G == 29) {
#pragma unroll
            for (int j = 0; j < 4; ++j) skv[j] = *(const LAS u32x4*)(sk + 4 * j);
            k1 = 0; k2 = 0;
        }
        if constexpr (G == 30 || G == 31) {
#pragma unroll
            for (int j = 8 * (G - 30); j < 8 * (G - 30) + 8; j += 2) top2(skv[j >> 2][j & 3], skv[j >> 2][(j & 3) + 1]);
            PIN(k1); PIN(k2);
        }
        if constexpr (G == 32) {
            k1 = __builtin_amdgcn_readfirstlane(k1); k2 = __builtin_amdgcn_readfirstlane(k2);
            R.sel0 = 0xFFFF - (int)(k1 & 0xFFFFu); R.sel1 = 0xFFFF - (int)(k2 & 0xFFFFu);
            ld_u16_nop(xs0, (const char*)bx + R.sel0 * 1024, tid * 2);
            ld_u16_nop(xs1, (const char*)bx + R.sel1 * 1024, tid * 2);
        }
        if constexpr (G == 33) {
            const unsigned o0 = k1 >> 16, o1 = k2 >> 16;
            const float r0 = __uint_as_float(((o0 & 0x8000u) ? (o0 & 0x7FFFu) : (~o0 & 0xFFFFu)) << 16);
            const float r1 = __uint_as_float(((o1 & 0x8000u) ? (o1 & 0x7FFFu) : (~o1 & 0xFFFFu)) << 16);
            R.sig0 = __uint_as_float(__builtin_amdgcn_readfirstlane(__float_as_uint(sigmoidf_(r0))));
            R.sig1 = __uint_as_float(__builtin_amdgcn_readfirstlane(__float_as_uint(sigmoidf_(r1))));
        }
        if constexpr (G >= 61 && G <= 64) {
            if constexpr (G == 61) { asm volatile("" : "+v"(xs0), "+v"(xs1), "+v"(Q.wgv), "+v"(Q.wuv)); ma0 = lo16(xs0); ma1 = lo16(xs1); }
            constexpr int c = 2 * (G - 61);
            const float g0 = lo16(Q.wgv[c >> 1]), g1 = hi16(Q.wgv[c >> 1]), u0 = lo16(Q.wuv[c >> 1]), u1 = hi16(Q.wuv[c >> 1]);
            mv[c] = ma0 * g0; mv[c + 1] = ma0 * g1; mv[8 + c] = ma0 * u0; mv[9 + c] = ma0 * u1;
            mv[16 + c] = ma1 * g0; mv[17 + c] = ma1 * g1; mv[24 + c] = ma1 * u0; mv[25 + c] = ma1 * u1;
            PIN(mv[c]); PIN(mv[c + 1]); PIN(mv[8 + c]); PIN(mv[9 + c]); PIN(mv[16 + c]); PIN(mv[17 + c]); PIN(mv[24 + c]); PIN(mv[25 + c]);
        }
        if constexpr (G >= 65 && G <= 68) {
#pragma unroll
            for (int j = 4 * (G - 65); j < 4 * (G - 65) + 4; ++j) { auto r = __builtin_amdgcn_permlane32_swap(__float_as_uint(mv[j]), __float_as_uint(mv[j + 16]), false, false);
                mv[j] = __uint_as_float(r[0]) + __uint_as_float(r[1]); PIN(mv[j]); }
        }
        if constexpr (G == 69 || G == 70) {
#pragma unroll
            for (int j = 4 * (G - 69); j < 4 * (G - 69) + 4; ++j) { auto r = __builtin_amdgcn_permlane16_swap(__float_as_uint(mv[j]), __float_as_uint(mv[j + 8]), false, false);
                mv[j] = __uint_as_float(r[0]) + __uint_as_float(r[1]); PIN(mv[j]); }
        }
        if constexpr (G == 71) { const bool up_ = lane & 8;
#pragma unroll
            for (int j = 0; j < 4; ++j) { const float send = up_ ? mv[j] : mv[j + 4], keep = up_ ? mv[j + 4] : mv[j]; mv[j] = keep + dppf<0x128>(send); PIN(mv[j]); } }
        if constexpr (G == 72) { const bool up_ = lane & 4;
#pragma unroll
            for (int j = 0; j < 2; ++j) { const float send = up_ ? mv[j] : mv[j + 2], keep = up_ ? mv[j + 2] : mv[j]; mv[j] = keep + dppf<0x1B>(dppf<0x141>(send)); PIN(mv[j]); } }
        if constexpr (G == 73) { const bool up_ = lane & 2;
            const float send = up_ ? mv[0] : mv[1], keep = up_ ? mv[1] : mv[0]; mv[0] = keep + dppf<0x4E>(send);
            mv[0] += dppf<0xB1>(mv[0]);
            if (!(lane & 1)) red[wid * 32 + (lane >> 1)] = mv[0];
        }
        if constexpr (G == 77) {
            if (tid < 16) {
                const int c = tid & 7, t = tid >> 3;
                float g = 0.f, u = 0.f;
#pragma unroll
                for (int w = 0; w < 8; ++w) { g += red[w * 32 + 16 * t + c]; u += red[w * 32 + 16 * t + 8 + c]; }
                g = bfround(g); u = bfround(u);
                hs[t * 8 + c] = bfround(bfround(g * sigmoidf_(g)) * u);
            }
        }
        if constexpr (G == 93) {
#pragma unroll
            for (int j = 0; j < 8; ++j) wdv[j] = *(const LAS u16*)((LAS char*)lds_ + MOE_WD + j * 1024 + tid * 2);
#pragma unroll
            for (int j = 0; j < 4; ++j) { const f32x4 t = *(const LAS f32x4*)(hs + 4 * j); hsv[4 * j] = t[0]; hsv[4 * j + 1] = t[1]; hsv[4 * j + 2] = t[2]; hsv[4 * j + 3] = t[3]; }
        }
        if constexpr (G == 95) {
            float y0 = 0.f, y1 = 0.f;
#pragma unroll
            for (int j = 0; j < 8; ++j) { const float w = lo16(wdv[j]); y0 += hsv[j] * w; y1 += hsv[8 + j] * w; }
            float* yp = (float*)(ws + WS_YP) + (size_t)cg * 1024;
            store_f32_wt(yp + tid, y0); store_f32_wt(yp + 512 + tid, y1);
        }
        (void)s; (void)i;
    };

    const char* Kh = ws + WS_KF + (size_t)h * 64 * 8192;
    const unsigned vlin = lane * 16;
    bf16x8 qf[8];
    {
        lptr xA[4];
#pragma unroll
        for (int ks = 0; ks < 4; ++ks) xA[ks] = lds + (32 * wid + l31) * 128 + (((2 * ks + hi) ^ ((l31 >> 1) & 7)) << 4);
        const int tq = (lane & 15) >> 2, tp = lane & 3, tblk = (lane >> 4) & 1;
        lptr wA[4];
#pragma unroll
        for (int c = 0; c < 4; ++c) wA[c] = lds + 32768 + (8 * hi + tq) * 256 + ((c ^ tq) << 6) + (tp >> 1) * 32 + tblk * 16 + (tp & 1) * 8;
        auto dmaS = [&](int s, int j) { q_dma(Q, P, C, s, j); };
        auto rdW = [&](int s, int i) { const int o = ((s + 2) % 3) * QST + (i >> 2) * 4096; return cat(ldstr(wA[i & 3] + o), ldstr(wA[i & 3] + o + 1024)); };
        auto rdX = [&](int s, int ks) { return ldsr128(xA[ks] + ((s + 2) % 3) * QST); };
        f32x16 acc[4] = {};
        bf16x8 wfr[8], xfr[2];
        unsigned kvf = 0;
        WAIT_BAR(14);
        STAMP(1, 1);
        xfr[0] = rdX(0, 0);
#pragma unroll
        for (int i = 0; i < 4; ++i) wfr[i] = rdW(0, i);
        static_for<128>([&](auto SI) {
            {
                constexpr int s = SI / 16, i = SI % 16;
                if (i == 12 && s < 7) {
                    if (s == 0) { WAIT_BAR(8); } else if (s == 1) { WAIT_BAR(6); } else if (s == 2) { WAIT_BAR(8); } else if (s < 6) { WAIT_BAR(6); } else { WAIT_BAR(0); }
                }
                const int nx = i + 4;
                if (nx < 16) { if ((nx & 3) == 0) xfr[(nx >> 2) & 1] = rdX(s, nx >> 2); wfr[nx & 7] = rdW(s, nx); }
                else if (s < 7) { if (nx == 16) xfr[0] = rdX(s + 1, 0); wfr[nx & 7] = rdW(s + 1, nx - 16); }
                acc[i & 3] = MFMA(wfr[i & 7], xfr[(i >> 2) & 1], acc[i & 3]);
                if (i == 12 || i == 14) { if (s + 3 < 8) dmaS(s + 3, (i - 12) / 2); }
                else if (s == 0) { if (i < 6) dmaS(2, i); }
                else if (i < 12 && i % 3 == 0) { if (s + 2 < 8) dmaS(s + 2, 2 + i / 3); }
                if (s == 6 && grp == 0) {
                    if (i == 12) glds16x4_dev(Kh + rg * 4096, vlin, lds0 + RING0 * 16384 + rg * 4096);
                    if (i == 14) glds16x4_dev(Kh + 16384 + rg * 4096, vlin, lds0 + (RING0 + 2) * 16384 + rg * 4096);
                }
                moe_gap(SI);
                if (s == 1 && i == 13 && tid == 0) flag_store(C.sy + SY_KV + h * 8 + bi, C.tag);
                if (s == 4 && i == 3 && wid == 0)
                    asm volatile("global_load_dword %0, %1, %2 sc1" : "=v"(kvf) : "v"((lane & 7) * 4), "s"(C.sy + SY_KV + h * 8));
                if (s == 5 && i == 13 && wid == 0) {
                    asm volatile("" : "+v"(kvf));
                    if (__builtin_expect(!__all(kvf == C.tag), 0)) wait_flags<1>(C.sy + SY_KV + h * 8, 8, C.tag, lane);

                }
                if (s == 6 && i == 13 && tid == 0) flag_store(C.sy + SY_Y + bid, C.tag);
                SBAR();
            }
        });
#pragma unroll
        for (int c = 0; c < 4; ++c) { qf[2 * c] = pack8(acc[c], 0, QSCALE); qf[2 * c + 1] = pack8(acc[c], 1, QSCALE); }
    }

    STAMP(1, 2);
    const int n1 = 2 * bi + 2;
    auto tile_of = [&](int n, int g) { return n < n1 ? n : n1 + 2 * (n - n1) + g; };
    auto jstar = [&](int tn) { return tn <= 2 * n1 ? tn : tn <= 2 * n1 + 2 ? tn + 1 : 2 * tn - 2 * n1 - 2; };
    auto jg1 = [&](int tn) { return tn == 2 * n1 + 1 ? tn : 2 * tn - 2 * n1 - 1; };
    int tb_d0, tb_d1, tb_kv, tb_rel;
    auto make_job = [&](int tn, int& d0, int& d1) {
        const int n = tn >> 1;
        int a_off, b_off; bool a_ok, b_ok;
        const int kb = (int)WS_KF + h * 64 * 8192 + rg * 4096, vb = (int)WS_VF + h * 64 * 8192 + rg * 4096;
        if (tn & 1) { a_ok = n >= 1 && n <= 17; a_off = vb + tile_of(n - 1, 0) * 16384; b_ok = n >= n1 && n <= 16; b_off = kb + tile_of(n, 1) * 16384; }
        else        { a_ok = n <= 16;           a_off = kb + tile_of(n, 0) * 16384;     b_ok = n - 2 >= n1 && n <= 18; b_off = vb + tile_of(n - 2, 1) * 16384; }
        const int da = a_off | ((jstar(tn) + RING0) & 7) | 8, db = b_off | ((jg1(tn) + RING0) & 7) | 8;
        d0 = a_ok ? da : b_ok ? db : 0; d1 = (a_ok && b_ok) ? db : 0;
    };
    {
        const int n = lane < 19 ? lane : 18;
        make_job(2 * n + grp + 3, tb_d0, tb_d1);
        const int ks = ((n < n1 || grp == 0 ? jstar(2 * n) : jg1(2 * n + 1)) + RING0) & 7;
        const int vs = ((n < n1 || grp == 0 ? jstar(2 * n + 3) : jg1(2 * n + 4)) + RING0) & 7;
        tb_kv = ks | vs << 8;
        tb_rel = 128 * ((grp || n >= n1) ? blkB : blkA) + 32 * rg - 64 * tile_of(n, grp);
    }
    auto job_from = [&](int d0, int d1) -> DmaJob {
        DmaJob d;
        d.has0 = d0 & 8; d.has1 = d1 & 8;
        d.src0 = ws + (unsigned)(d0 & ~4095); d.src1 = ws + (unsigned)(d1 & ~4095);
        d.dst0 = lds0 + (d0 & 7) * 16384 + rg * 4096; d.dst1 = lds0 + (d1 & 7) * 16384 + rg * 4096;
        return d;
    };
    auto job = [&](int n) { return job_from(__builtin_amdgcn_readlane(tb_d0, n), __builtin_amdgcn_readlane(tb_d1, n)); };
    if (grp == 1) {
#pragma unroll
        for (int c = 0; c < 8; ++c) *(LAS bf16x8*)((LAS char*)lds_ + K2_QB + rg * 8192 + c * 1024 + lane * 16) = qf[c];
    }
    f32x16 o[4] = {};
    SMState st; st.mhat = 0.f; st.l = 0.f; st.negm = f32x16{};
    const lptr kp0 = lds + lane * 16;
    char* ctx = ws + WS_CTX;
    const rsrc_t ctxr = make_rsrc(ctx, (unsigned)((size_t)SEQ * NHD * 2));
    auto store_ctx = [&](int blk, int d, const f32x16& v, float inv) {
        const unsigned off = (unsigned)(((4 * blk + rg) * 32 + h) * 8 + 2 * d) * 1024 + lane * 16;
        store_wt(ctxr, off, pack8(v, 0, inv));
        store_wt(ctxr, off + 1024, pack8(v, 1, inv));
    };
    auto half_sum = [&](float v) { auto rr = __builtin_amdgcn_permlane32_swap(__float_as_uint(v), __float_as_uint(v), false, false);
                                   return __uint_as_float(rr[0]) + __uint_as_float(rr[1]); };
    auto kslot = [&](int n) { return kp0 + (__builtin_amdgcn_readlane(tb_kv, n) & 7) * 16384; };
    auto vslot = [&](int n) { return kp0 + ((__builtin_amdgcn_readlane(tb_kv, n) >> 8) & 7) * 16384; };
    WAIT_BAR(0);
    bool first = true, pend = false;
    float invA = 0.f, fpend = 1.f;
    f32x16 sA0, sA1, sB0, sB1; bf16x8 pf[4], vpre[LA], kpre[LA];
#pragma unroll
    for (int i = 0; i < LA; ++i) kpre[i] = ldsr128a(kslot(0) + KOFF(i));
    if (grp == 1) { WAIT_BAR(0); }
    SEG_DECL; SEG_START();
    const int nsw = grp == 0 ? n1 - 1 : 100;
    auto headB = [&](int n) {
        if (__builtin_expect(n == nsw + 1, 0)) { invA = 1.0f / half_sum(st.l); st.mhat = 0.f; st.l = 0.f; first = true; }
    };
    auto tailB = [&](int n) {
        first = false;
        if (__builtin_expect(pend || (unsigned)(n - nsw) < 3u, 0)) {
            if (n == nsw + 2 && tid == 0) flag_store(C.sy + SY_CTX + blkA * 32 + h, C.tag);
            if (pend) {
#pragma unroll
                for (int d = 0; d < 4; ++d)
#pragma unroll
                    for (int r = 0; r < 16; ++r) o[d][r] *= fpend;
                pend = false;
            }
            if (n == nsw + 1) {
#pragma unroll
                for (int d = 0; d < 4; ++d) store_ctx(blkA, d, o[d], invA);
#pragma unroll
                for (int d = 0; d < 4; ++d) o[d] = f32x16{};
            }
            if (n == nsw) {
#pragma unroll
                for (int c = 0; c < 8; ++c) qf[c] = ldsr128(lds + K2_QB + rg * 8192 + c * 1024 + lane * 16);
                st.negm = f32x16{};
            }
        }
    };
#define BARRIER_L() asm volatile("s_waitcnt lgkmcnt(0)\n\ts_barrier" ::: "memory")
    lptr kcur = kslot(0), vcur = kp0;
    DmaJob dj = job(0);
    int rel = 0;
    auto pinjob = [&]() { asm volatile("" : "+s"(dj.src0), "+s"(dj.src1), "+s"(dj.dst0), "+s"(dj.dst1)); };
    auto midA = [&](int n) { vcur = vslot(n > 0 ? n - 1 : 0); rel = __builtin_amdgcn_readlane(tb_rel, n); asm volatile("" : "+v"(vcur), "+s"(rel)); };
    auto midB = [&](int n) { kcur = kslot(n + 1); dj = job(n + 1); asm volatile("" : "+v"(kcur)); pinjob(); };
#define RELARGS first, rel < 63, rel + l31 - 4 * hi, pend, fpend
    attn_A<true, false>(kcur, vcur, qf, st.negm, kpre, vpre, sA0, sA1, sA0, sA1, pf, st.l, dj, vlin, [&]() { midA(0); });
    BARRIER_L();
    attn_B<false, true>(vcur, kcur, o, pf, vpre, kpre, sA0, sA1, st, RELARGS, [&]() { midB(0); });
    tailB(0);
    WAIT_BAR(0);
#define STEP(n, C0, C1, P0, P1) do { \
        attn_A<true, true>(kcur, vcur, qf, st.negm, kpre, vpre, C0, C1, P0, P1, pf, st.l, dj, vlin, [&]() { midA(n); }); \
        SEG(0); \
        BARRIER_L(); \
        SEG(2); \
        headB(n); \
        attn_B<true, true>(vcur, kcur, o, pf, vpre, kpre, C0, C1, st, RELARGS, [&]() { midB(n); }); \
        tailB(n); \
        SEG(3); \
        if ((n) < 16) { WAIT_BAR(0); } else { BARRIER_L(); }     \
        SEG(4); \
    } while (0)
    unsigned pfd;
    for (int n = 1; n < 17; n += 2) {
        STEP(n, sB0, sB1, sA0, sA1);
        STEP(n + 1, sA0, sA1, sB0, sB1);
    }
#undef STEP
    asm volatile("global_load_dword %0, %1, %2" : "=v"(pfd) : "v"(vlin * 8),
                 "s"((const char*)wo + (size_t)(wid < 2 ? bid * 2 + wid : (bid >> 3) * 4 + ((wid - 2) & 3)) * 8192));
    attn_A<false, true>(kcur, vcur, qf, st.negm, kpre, vpre, sB0, sB1, sA0, sA1, pf, st.l, dj, vlin, [&]() { midA(17); });
    BARRIER_L();
    attn_B<true, false>(vcur, kcur, o, pf, vpre, kpre, sB0, sB1, st, false, false, 0, pend, fpend, [&]() {});
    BARRIER_L();
    SEG_OUT(1);
    if (grp == 0) { BARRIER_L(); }
    STAMP(1, 3);
    unsigned cfl = 0;
    const int ort = (bid & 7) * 4 + ((bid >> 3) & 3);
    if (wid == 0) asm volatile("global_load_dword %0, %1, %2 sc1" : "=v"(cfl) : "v"((lane & 31) * 4), "s"(C.sy + SY_CTX + (ort >> 1) * 32));
    BARRIER_L();
    {
        const float lsum = half_sum(st.l);
        LAS char* xch = (LAS char*)lds_;
        LAS char* mine = xch + wid * 8192;
        auto put = [&](int dd, const f32x16& v) {
#pragma unroll
            for (int q4 = 0; q4 < 4; ++q4)
                *(LAS f32x4*)(mine + (dd * 4 + q4) * 1024 + lane * 16) = (f32x4){v[4 * q4], v[4 * q4 + 1], v[4 * q4 + 2], v[4 * q4 + 3]};
        };
        if (grp) { put(0, o[0]); put(1, o[1]); } else { put(0, o[2]); put(1, o[3]); }
        LAS float* ml = (LAS float*)(xch + 65536);
        ml[wid * 128 + lane] = st.mhat; ml[wid * 128 + 64 + lane] = lsum;
        BARRIER_L();
        const int ow = wid ^ 4;
        const float m2 = ml[ow * 128 + lane], l2 = ml[ow * 128 + 64 + lane];
        const float mm = fmaxf(st.mhat, m2);
        const float f1 = __builtin_amdgcn_exp2f(st.mhat - mm), f2 = __builtin_amdgcn_exp2f(m2 - mm);
        const float inv = 1.0f / (lsum * f1 + l2 * f2);
        const float c1 = f1 * inv, c2 = f2 * inv;
        const LAS char* theirs = xch + ow * 8192;
        auto fin = [&](int dd, f32x16 v) {
#pragma unroll
            for (int q4 = 0; q4 < 4; ++q4) {
                const f32x4 t = *(const LAS f32x4*)(theirs + (dd * 4 + q4) * 1024 + lane * 16);
#pragma unroll
                for (int e = 0; e < 4; ++e) v[4 * q4 + e] = v[4 * q4 + e] * c1 + t[e] * c2;
            }
            store_ctx(blkB, 2 * grp + dd, v, 1.0f);
        };
        if (grp) { fin(0, o[2]); fin(1, o[3]); } else { fin(0, o[0]); fin(1, o[1]); }
    }
    asm volatile("s_waitcnt vmcnt(0)" : "+v"(pfd), "+v"(cfl));
    if (wid == 0) {
        const unsigned bal = (unsigned)__ballot(cfl == C.tag);
        unsigned m0 = 0;
#pragma unroll
        for (int g = 0; g < 8; ++g) m0 |= (((bal >> (4 * g)) & 15u) == 15u ? 1u : 0u) << g;
        if (__builtin_popcount(m0) < 2) m0 = 0;
        if (lane == 0) *(LAS unsigned*)((LAS char*)lds_ + 163840 - 16) = m0;
    }
    BARRIER_L();
    if (tid == 0) { flag_store(C.sy + SY_CTX + blkB * 32 + h, C.tag); flag_store(C.sy + SY_CTX + blkA * 32 + h, C.tag); }
    STAMP(1, 4);
}

constexpr int K3_SLOT = 32768, K3_NS = 5;
DI void phase_oproj(const Params& P, const Ctx& C, const Routed& R) {
    char* ws = P.ws; char* lds_ = C.lds_;
    const lptr lds = C.lds;
    const unsigned lds0 = C.lds0;
    const int lane = C.lane, l31 = lane & 31, hi = lane >> 5, wid = C.wid;
    const int bid = C.bid, rt = (bid & 7) * 4 + ((bid >> 3) & 3), ct = bid >> 5;
    STAMP(2, 0);
    unsigned m0 = __builtin_amdgcn_readfirstlane(*(const LAS unsigned*)((LAS char*)lds_ + 163840 - 16));
    if (!m0) {
        if (wid == 0) {
            const unsigned* cf_ = C.sy + SY_CTX + (rt >> 1) * 32;
            const unsigned long long t0 = __builtin_amdgcn_s_memrealtime();
            unsigned m = 0;
            for (;;) {
                const unsigned bal = (unsigned)__ballot(flag_load(cf_ + (lane & 31)) == C.tag);
                m = 0;
#pragma unroll
                for (int g = 0; g < 8; ++g) m |= (((bal >> (4 * g)) & 15u) == 15u ? 1u : 0u) << g;
                if (__builtin_popcount(m) >= 2) break;
                if (__builtin_amdgcn_s_memrealtime() - t0 > 5000000ull) { m = 0xFFu; break; }
                __builtin_amdgcn_s_sleep(2);
            }
            if (lane == 0) *(LAS unsigned*)((LAS char*)lds_ + 163840 - 32) = m;
        }
        asm volatile("s_waitcnt lgkmcnt(0)\n\ts_barrier" ::: "memory");
        m0 = __builtin_amdgcn_readfirstlane(*(const LAS unsigned*)((LAS char*)lds_ + 163840 - 32));
        asm volatile("s_waitcnt lgkmcnt(0)\n\ts_barrier" ::: "memory");
    }
    const int nAg = __builtin_popcount(m0);
    const int rot = 0;
    unsigned ord = 0;
    { int u = 0;
#pragma unroll
      for (int k = 0; k < 8; ++k) { const int g = (k + rot) & 7; if ((m0 >> g) & 1) { ord |= (unsigned)g << (4 * u); ++u; } }
#pragma unroll
      for (int k = 0; k < 8; ++k) { const int g = (k + rot) & 7; if (!((m0 >> g) & 1)) { ord |= (unsigned)g << (4 * u); ++u; } } }
    if (wid >= 4) {
        const int lw = wid - 4;
        const char* csrc = ws + WS_CTX + (size_t)(2 * rt + (lw >> 1)) * 32 * 8192 + (lw & 1) * 4096;
        const char* wsrc = (const char*)P.wo + ct * 128;
        unsigned voffW[4];
#pragma unroll
        for (int j = 0; j < 4; ++j) { const int row = (lw * 4 + j) * 8 + (lane >> 3);
            voffW[j] = row * 1024 + (((lane & 7) ^ (((row >> 1) & 1) << 2)) << 4); }
        const unsigned vlin = lane * 16;
        auto head = [&](int t) { return (int)(4 * ((ord >> (4 * (t >> 2))) & 7) + (t & 3)); };
        auto stageW = [&](int t, int slot) {
            const unsigned b = lds0 + slot * K3_SLOT + lw * 4096;
#pragma unroll
            for (int j = 0; j < 4; ++j) glds16(wsrc + (size_t)head(t) * 131072, voffW[j], b + 16384 + j * 1024);
        };
        auto stageC = [&](int t, int slot) { glds16x4_dev(csrc + (size_t)head(t) * 8192, vlin, lds0 + slot * K3_SLOT + lw * 4096); };
#pragma unroll
        for (int t = 0; t < 4; ++t) { stageC(t, t); stageW(t, t); }
        int slot = 4;
        for (int hh = 0; hh < 28; ++hh) {
            WAIT_BAR(24);
            stageC(hh + 4, slot); stageW(hh + 4, slot);
            slot = slot == K3_NS - 1 ? 0 : slot + 1;
        }
        WAIT_BAR(24); WAIT_BAR(16); WAIT_BAR(8); WAIT_BAR(0);
        return;
    }
    const int qg2 = wid >> 1, cb = wid & 1;
    const int i0 = R.sel0, i1 = R.sel1;
    const bool has0 = (i0 >> 6) == rt, has1 = (i1 >> 6) == rt;
    const int q = rt * 64 + qg2 * 32 + l31;
    const unsigned* cflags = C.sy + SY_CTX + (rt >> 1) * 32;
    const float g = __uint_as_float(flag_load((const unsigned*)(ws + WS_G) + q));
    s16x4 xb[4];
#pragma unroll
    for (int q4 = 0; q4 < 4; ++q4) xb[q4] = *(const s16x4*)(P.ax + (size_t)q * DM + ct * 64 + cb * 32 + 8 * q4 + 4 * hi);
    const int tq = (lane & 15) >> 2, tp = lane & 3, tblk = (lane >> 4) & 1;
    const lptr wA = lds + 16384 + (4 * hi + tq) * 128 + ((cb ^ ((tq >> 1) & 1)) << 6) + tblk * 32 + tp * 8;
    const lptr cA = lds + qg2 * 8192 + lane * 16;
    f32x16 accs[8] = {};
    int bo = 0;
    auto run4 = [&](f32x16& acc) {
#pragma unroll 1
        for (int hh = 0; hh < 4; ++hh) {
            asm volatile("s_waitcnt lgkmcnt(0)\n\ts_barrier" ::: "memory");
            bf16x8 cf[8], wf[8];
#pragma unroll
            for (int f = 0; f < 4; ++f) { wf[f] = cat(ldstr(wA + bo + f * 2048), ldstr(wA + bo + f * 2048 + 1024)); cf[f] = ldsr128(cA + bo + f * 1024); }
            SBAR();
#pragma unroll
            for (int f = 0; f < 8; ++f) {
                acc = MFMA(wf[f], cf[f], acc);
                if (f + 4 < 8) { wf[f + 4] = cat(ldstr(wA + bo + (f + 4) * 2048), ldstr(wA + bo + (f + 4) * 2048 + 1024)); cf[f + 4] = ldsr128(cA + bo + (f + 4) * 1024); }
                SBAR();
            }
            bo = bo == (K3_NS - 1) * K3_SLOT ? 0 : bo + K3_SLOT;
        }
    };
    auto slots = [&](int u0, int u1) {
        for (int u = u0; u < u1; ++u) {
            switch ((ord >> (4 * u)) & 7) {
                case 0: run4(accs[0]); break; case 1: run4(accs[1]); break; case 2: run4(accs[2]); break; case 3: run4(accs[3]); break;
                case 4: run4(accs[4]); break; case 5: run4(accs[5]); break; case 6: run4(accs[6]); break; default: run4(accs[7]); break;
            }
            if (wid == 0 && u == nAg - 2 && nAg < 8) wait_flags<1>(cflags, 32, C.tag, lane);
        }
    };
    unsigned yf[4]; f32x4 yv4[4];
    if (wid == 1) {
#pragma unroll
        for (int j = 0; j < 4; ++j) yf[j] = flag_load(C.sy + SY_Y + lane + 64 * j);
    }
    slots(0, 1);
    STAMP(2, 2);
    if (wid == 1) {
        if (!__all(yf[0] == C.tag && yf[1] == C.tag && yf[2] == C.tag && yf[3] == C.tag)) wait_flags<4>(C.sy + SY_Y, 256, C.tag, lane);
        const rsrc_t ypr = make_rsrc(ws + WS_YP, MOE_WGS * 4096);
#pragma unroll
        for (int j = 0; j < 4; ++j)
            yv4[j] = __builtin_bit_cast(f32x4, __builtin_amdgcn_raw_buffer_load_b128(ypr, (lane + 64 * j) * 4096 + bid * 16, 0, 16));
    }
    slots(1, 3);
    if (wid == 1) {
        f32x4 y = (yv4[0] + yv4[1]) + (yv4[2] + yv4[3]);
#pragma unroll
        for (int e = 0; e < 4; ++e) y[e] = wave_sum(y[e], lane);
        if (lane < 4) store_f32_wt((float*)(ws + WS_Y) + bid * 4 + lane, lane == 0 ? y[0] : lane == 1 ? y[1] : lane == 2 ? y[2] : y[3]);
    }
    slots(3, 6);
    if (wid == 1) {
        asm volatile("s_waitcnt vmcnt(0)" ::: "memory");
        if (lane == 0) flag_store(C.sy + SY_Y2 + bid, C.tag);
    }
    slots(6, 8);
    STAMP(2, 3);
    f32x16 acc = accs[0];
#pragma unroll
    for (int gg = 1; gg < 8; ++gg) acc += accs[gg];
    const bool r0 = (q == i0), r1 = (q == i1);
    f32x4 yrow[4] = {};
    if (has0 || has1) {
        wait_flags<1>(C.sy + SY_Y2 + ct * 16, 16, C.tag, lane);
        wait_flags<1>(C.sy + SY_Y2 + 128 + ct * 16, 16, C.tag, lane);
        const rsrc_t yr = make_rsrc(ws + WS_Y, 4096);
#pragma unroll
        for (int q4 = 0; q4 < 4; ++q4)
            yrow[q4] = __builtin_bit_cast(f32x4, __builtin_amdgcn_raw_buffer_load_b128(yr, ((r1 ? 512 : 0) + ct * 64 + cb * 32 + 8 * q4 + 4 * hi) * 4, 0, 16));
    }
    {
        const float ysc = r0 ? R.sig0 : r1 ? R.sig1 : 0.f;
        u32x2 w2[4];
#pragma unroll
        for (int q4 = 0; q4 < 4; ++q4) {
            float res[4];
#pragma unroll
            for (int e = 0; e < 4; ++e) {
                float b = bf2f((u16)xb[q4][e]);
                if (r0 || r1) b += yrow[q4][e] * ysc;
                res[e] = g * acc[4 * q4 + e] + (1.0f - g) * b;
            }
            w2[q4] = (u32x2){cvtpk(res[0], res[1]), cvtpk(res[2], res[3])};
        }
        const rsrc_t outr = make_rsrc(P.out, SEQ * DM * 2);
#pragma unroll
        for (int k = 0; k < 4; k += 2) {
            auto rx = __builtin_amdgcn_permlane32_swap(w2[k][0], w2[k + 1][0], false, false);
            auto ry = __builtin_amdgcn_permlane32_swap(w2[k][1], w2[k + 1][1], false, false);
            const u32x4 v = {rx[0], ry[0], rx[1], ry[1]};
            __builtin_amdgcn_raw_buffer_store_b128(v, outr, (q * DM + ct * 64 + cb * 32 + 8 * k + 8 * hi) * 2, 0, 16);
        }
    }
    STAMP(2, 4);
}

__global__ __launch_bounds__(512, 2)
void mega_kernel(Params Parg) {
    extern __shared__ __attribute__((aligned(16))) char lds_[];
    const int wid = __builtin_amdgcn_readfirstlane(threadIdx.x >> 6);
    unsigned tag;
    Routed R;
    QPre Q;
    {
        const Params P = load_params();
        unsigned tagv;
        asm volatile("global_load_dword %0, %1, %2 sc1" : "=v"(tagv) : "v"(0), "s"((const unsigned*)(P.ws + WS_SYNC) + SY_EPOCH));
        Ctx C = make_ctx(lds_, P.ws, wid, 0u);
        phase_kv(P, C, Q, tagv);
        tag = __builtin_amdgcn_readfirstlane(C.tag);
    }
    { const Params P = load_params(); phase_attn(P, make_ctx(lds_, P.ws, wid, tag), Q, R); }
    {
        const Params P = load_params();
        const Ctx C = make_ctx(lds_, P.ws, wid, tag);
        phase_oproj(P, C, R);
        if (C.tid == 0) __hip_atomic_fetch_add(C.sy + SY_EPOCH, 1u, __ATOMIC_RELAXED, __HIP_MEMORY_SCOPE_AGENT);
    }
}

extern "C" void kernel_launch(void* const* d_in, const int* in_sizes, int n_in, void* d_out, int out_size, void* d_ws, size_t ws_size,
                              hipStream_t stream) {
    const u16* ax = (const u16*)d_in[0]; const u16* ckv = (const u16*)d_in[1]; const u16* wq = (const u16*)d_in[2];
    const u16* wuk = (const u16*)d_in[3]; const u16* wuv = (const u16*)d_in[4]; const u16* wo = (const u16*)d_in[5];
      const u16* wr = (const u16*)d_in[7]; const u16* wg = (const u16*)d_in[8];
    const u16* wu = (const u16*)d_in[9]; const u16* wd = (const u16*)d_in[10]; const u16* gw = (const u16*)d_in[11];
    char* ws = (char*)d_ws;
    (void)hipFuncSetAttribute((const void*)mega_kernel, hipFuncAttributeMaxDynamicSharedMemorySize, 163840);
    Params P{ax, ckv, wq, wuk, wuv, wo, wr, wg, wu, wd, gw, ws, (u16*)d_out};
    mega_kernel<<<256, 512, 163840, stream>>>(P);
}
```

```cpp
#include <hip/hip_runtime.h>
#include <hip/hip_bf16.h>
#include <stdint.h>
#include <utility>

typedef unsigned short u16;
typedef short bf16x8 __attribute__((ext_vector_type(8)));
typedef short s16x4 __attribute__((ext_vector_type(4)));
typedef short v4i16_t __attribute__((ext_vector_type(4)));
typedef float f32x16 __attribute__((ext_vector_type(16)));
typedef float f32x4 __attribute__((ext_vector_type(4)));
typedef unsigned u32x4 __attribute__((ext_vector_type(4)));
typedef unsigned u32x2 __attribute__((ext_vector_type(2)));
#define LAS __attribute__((address_space(3)))
#define DI __device__ __forceinline__
#define MFMA(a, b, c) __builtin_amdgcn_mfma_f32_32x32x16_bf16((a), (b), (c), 0, 0, 0)
#define MFMA16(a, b, c) __builtin_amdgcn_mfma_f32_16x16x32_bf16((a), (b), (c), 0, 0, 0)
#define SBAR() __builtin_amdgcn_sched_barrier(0)

constexpr int SEQ = 2048, DM = 512, NH = 32, HD = 128, NHD = NH * HD;
constexpr int FF = 1792;
constexpr int MOE_WGS = 224, MOE_COLS = 8;

constexpr size_t WS_KF = 0;
constexpr size_t WS_VF = WS_KF + (size_t)SEQ * NHD * 2;
constexpr size_t WS_CTX = WS_VF + (size_t)SEQ * NHD * 2;
constexpr size_t WS_R = WS_CTX + (size_t)SEQ * NHD * 2;
constexpr size_t WS_G = WS_R + SEQ * 4;
constexpr size_t WS_Y = WS_G + SEQ * 4;
constexpr size_t WS_YP = WS_Y + 4096;
constexpr size_t WS_SYNC = WS_YP + 224 * 4096;
constexpr int SY_EPOCH = 0, SY_KV = 256, SY_R = 512, SY_Y = 768, SY_Y2 = 1024, SY_CTX = 1280;

constexpr size_t WS_DBG = 128u << 20;
#define STAMP(K, i) do {} while (0)
#define SEG_DECL
#define SEG_PARAMS
#define SEG_ARGS
#define SEG_START() do {} while (0)
#define SEG(i) do {} while (0)
#define SEG_OUT(K) do {} while (0)
template <int... Is, class F> DI void static_for_(std::integer_sequence<int, Is...>, F&& f) { (f(std::integral_constant<int, Is>{}), ...); }
template <int N, class F> DI void static_for(F&& f) { static_for_(std::make_integer_sequence<int, N>{}, f); }
DI unsigned cvtpk(float lo, float hi) {
    typedef float f32x2 __attribute__((ext_vector_type(2)));
    typedef __bf16 bf16x2 __attribute__((ext_vector_type(2)));
    f32x2 v = {lo, hi}; bf16x2 b = __builtin_convertvector(v, bf16x2); return __builtin_bit_cast(unsigned, b);
}
DI float bf2f(u16 v) { return __uint_as_float((unsigned)v << 16); }
DI float bfround(float x) { return __uint_as_float(cvtpk(x, x) << 16); }
DI bf16x8 pack8(const f32x16& x, int s, float sc) {
    u32x4 w = {cvtpk(x[8 * s] * sc, x[8 * s + 1] * sc), cvtpk(x[8 * s + 2] * sc, x[8 * s + 3] * sc),
               cvtpk(x[8 * s + 4] * sc, x[8 * s + 5] * sc), cvtpk(x[8 * s + 6] * sc, x[8 * s + 7] * sc)};
    return __builtin_bit_cast(bf16x8, w);
}
DI bf16x8 pack8(const f32x16& x, int s) {
    u32x4 w = {cvtpk(x[8 * s], x[8 * s + 1]), cvtpk(x[8 * s + 2], x[8 * s + 3]),
               cvtpk(x[8 * s + 4], x[8 * s + 5]), cvtpk(x[8 * s + 6], x[8 * s + 7])};
    return __builtin_bit_cast(bf16x8, w);
}
DI void glds16(const void* sbase, unsigned voff, unsigned lds_addr) {
    lds_addr = __builtin_amdgcn_readfirstlane(lds_addr);
    asm volatile("s_mov_b32 m0, %2\n\ts_nop 0\n\tglobal_load_lds_dwordx4 %0, %1" :: "v"(voff), "s"(sbase), "s"(lds_addr) : "memory");
}
DI void glds16x4(const void* sbase, unsigned voff, unsigned lds_addr) {
    lds_addr = __builtin_amdgcn_readfirstlane(lds_addr);
    asm volatile("s_mov_b32 m0, %2\n\ts_nop 0\n\tglobal_load_lds_dwordx4 %0, %1\n\tglobal_load_lds_dwordx4 %0, %1 offset:1024\n\t"
                 "global_load_lds_dwordx4 %0, %1 offset:2048\n\tglobal_load_lds_dwordx4 %0, %1 offset:3072"
                 :: "v"(voff), "s"(sbase), "s"(lds_addr) : "memory");
}
DI unsigned flag_load(const unsigned* p) { return __hip_atomic_load(p, __ATOMIC_RELAXED, __HIP_MEMORY_SCOPE_AGENT); }
DI void flag_store(unsigned* p, unsigned v) { __hip_atomic_store(p, v, __ATOMIC_RELAXED, __HIP_MEMORY_SCOPE_AGENT); }
DI void store_f32_wt(float* p, float v) { __hip_atomic_store((unsigned*)p, __float_as_uint(v), __ATOMIC_RELAXED, __HIP_MEMORY_SCOPE_AGENT); }
template <int NW> DI void wait_flags(const unsigned* f, int n, unsigned tag, int lane) {
    const unsigned long long t0 = __builtin_amdgcn_s_memrealtime();
    for (;;) {
        bool ok = true;
#pragma unroll
        for (int j = 0; j < NW; ++j) { const int i = lane + 64 * j; ok &= (i < n ? flag_load(f + i) : tag) == tag; }
        if (__all(ok)) break;
        if (__builtin_amdgcn_s_memrealtime() - t0 > 5000000ull) break;
        __builtin_amdgcn_s_sleep(2);
    }
}
DI void acquire_agent() { __builtin_amdgcn_fence(__ATOMIC_ACQUIRE, "agent"); }
DI void glds16x4_dev(const void* sbase, unsigned voff, unsigned lds_addr) {
    lds_addr = __builtin_amdgcn_readfirstlane(lds_addr);
    asm volatile("s_mov_b32 m0, %2\n\ts_nop 0\n\tglobal_load_lds_dwordx4 %0, %1 sc1\n\tglobal_load_lds_dwordx4 %0, %1 offset:1024 sc1\n\t"
                 "global_load_lds_dwordx4 %0, %1 offset:2048 sc1\n\tglobal_load_lds_dwordx4 %0, %1 offset:3072 sc1"
                 :: "v"(voff), "s"(sbase), "s"(lds_addr) : "memory");
}
#define WAIT_BAR(N) asm volatile("s_waitcnt vmcnt(" #N ") lgkmcnt(0)\n\ts_barrier" ::: "memory")
#define WAIT_CNT(c) do { if ((c) == 8) { WAIT_BAR(8); } else if ((c) == 4) { WAIT_BAR(4); } else { WAIT_BAR(0); } } while (0)
DI int lane_now() { int l; asm volatile("v_mbcnt_lo_u32_b32 %0, -1, 0\n\tv_mbcnt_hi_u32_b32 %0, -1, %0" : "=v"(l)); return l; }
typedef LAS const char* lptr;
DI bf16x8 ldsr128(lptr p) { return *(const LAS bf16x8*)p; }
DI bf16x8 ldsr128a(lptr p) { return *(const LAS bf16x8*)p; }
DI s16x4 ldstr(lptr p) { return __builtin_bit_cast(s16x4, __builtin_amdgcn_ds_read_tr16_b64_v4i16((LAS v4i16_t*)p)); }
DI bf16x8 cat(s16x4 a, s16x4 b) { return __builtin_shufflevector(a, b, 0, 1, 2, 3, 4, 5, 6, 7); }
DI unsigned shx(unsigned v, int m, int lane) { return (unsigned)__builtin_amdgcn_ds_bpermute((lane ^ m) << 2, (int)v); }
DI float shx(float v, int m, int lane) { return __uint_as_float(shx(__float_as_uint(v), m, lane)); }
template <int CTRL> DI unsigned dppu(unsigned v) { return (unsigned)__builtin_amdgcn_update_dpp(0, (int)v, CTRL, 0xF, 0xF, true); }
template <int CTRL> DI float dppf(float v) { return __uint_as_float(dppu<CTRL>(__float_as_uint(v))); }
DI float wave_sum(float v, int lane) {
#pragma unroll
    for (int o = 32; o >= 1; o >>= 1) v += shx(v, o, lane);
    return v;
}
DI float sigmoidf_(float x) { return 1.0f / (1.0f + __expf(-x)); }

constexpr int K1_STAGE = 49152, K1_LDS = 163840;
typedef __amdgpu_buffer_rsrc_t rsrc_t;
DI rsrc_t make_rsrc(const void* p, unsigned bytes) { return __builtin_amdgcn_make_buffer_rsrc((void*)p, (short)0, (int)bytes, 0x00020000); }
DI void store_wt(rsrc_t r, unsigned off, bf16x8 v) {
    __builtin_amdgcn_raw_buffer_store_b128(__builtin_bit_cast(u32x4, v), r, (int)off, 0, 16);
}
#define WAIT_VM(N) asm volatile("s_waitcnt vmcnt(" #N ")" ::: "memory")

template <bool ISV, class Pre, class Mid, class Tail, class St>
DI void kv_body(const char* cbase, const char* kbase, const char* vbase, rsrc_t outr, unsigned outoff, lptr lds, unsigned lds0, int wid, int lane,
                Pre&& pre, Mid&& mid, Tail&& tail, St&& stamp2) {
    const int r16 = lane & 15, q4 = lane >> 4;
    unsigned voffW[2], voffC[2], voffG[2];
#pragma unroll
    for (int j = 0; j < 2; ++j) {
        const int n = 2 * wid + j;
        { const int row = 4 * n + (lane >> 4); voffW[j] = row * 8192 + (((lane & 15) ^ ((row & 3) << 2) ^ (((row >> 3) & 1) << 1)) << 4); }
        { const int row = 8 * n + (lane >> 3); voffC[j] = row * 1024 + (((lane & 7) ^ ((row >> 1) & 7)) << 4); }
        voffG[j] = (lane ^ ((wid + 8 * j) & 15)) << 4;
    }
    auto stage = [&](int s) {
        const unsigned b = lds0 + (s % 3) * K1_STAGE + 2 * wid * 1024;
#pragma unroll
        for (int j = 0; j < 2; ++j) glds16(kbase + (size_t)s * 64 * 8192, voffW[j], b + j * 1024);
#pragma unroll
        for (int j = 0; j < 2; ++j) glds16(vbase + (size_t)s * 64 * 8192, voffW[j], b + 16384 + j * 1024);
#pragma unroll
        for (int j = 0; j < 2; ++j) glds16(cbase + s * 128, voffC[j], b + 32768 + j * 1024);
    };
    auto gbuf = [&](int k) { return k == 4 ? 98304 : k == 5 ? 131072 : k == 6 ? 0 : 49152; };
    const int tq = (lane & 15) >> 2, tp = lane & 3;
    lptr wA[2], cA[2];
#pragma unroll
    for (int b = 0; b < 2; ++b) {
        wA[b] = lds + (ISV ? 16384 : 0) + (8 * q4 + tq) * 256 + (((wid & 3) ^ tq) << 6) + ((b ^ (q4 & 1)) << 5) + tp * 8;
        cA[b] = lds + 32768 + r16 * 128 + (((4 * b + q4) ^ (r16 >> 1)) << 4);
    }
    auto mm = [&](const bf16x8& w, const bf16x8& c, const f32x4& acc) { return ISV ? MFMA16(c, w, acc) : MFMA16(w, c, acc); };
    const unsigned olane = outoff + (q4 >> 1) * 1024 + (ISV ? (r16 + 32 * (q4 & 1)) * 16 : (16 * (r16 >> 3) + (r16 & 7) + 32 * (q4 & 1)) * 16);
    auto out_tile = [&](int kg, const f32x4& t00, const f32x4& t01, const f32x4& t10, const f32x4& t11) {
        auto pk = [](const f32x4& x, const f32x4& y) { u32x4 w = {cvtpk(x[0], x[1]), cvtpk(x[2], x[3]), cvtpk(y[0], y[1]), cvtpk(y[2], y[3])}; return __builtin_bit_cast(bf16x8, w); };
        if (ISV) { store_wt(outr, olane + kg * 8192, pk(t00, t10)); store_wt(outr, olane + kg * 8192 + 256, pk(t01, t11)); }
        else     { store_wt(outr, olane + kg * 8192, pk(t00, t01)); store_wt(outr, olane + kg * 8192 + 128, pk(t10, t11)); }
    };

    bf16x8 wf[16][2];
    f32x4 acc[8][2] = {};
    lptr gA[4];
    stage(0); stage(1); stage(2);
    pre();
    auto rdW = [&](int s, int sl) { const int o = (s % 3) * K1_STAGE + sl * 8192;
#pragma unroll
        for (int b = 0; b < 2; ++b) wf[2 * s + sl][b] = cat(ldstr(wA[b] + o), ldstr(wA[b] + o + 1024)); };
    auto rdC = [&](int s, int i) { return ldsr128(cA[i >> 3] + (s % 3) * K1_STAGE + (i & 7) * 2048); };
    auto rdG = [&](int k, int ks) { return ldsr128(gA[(ks >> 1) & 3] + gbuf(k) + (ks & 1) * 16384 + (ks >> 3) * 256); };
    auto dmaS = [&](int s, int j) {
        const unsigned b = lds0 + (s % 3) * K1_STAGE + 2 * wid * 1024;
        if (j < 2) glds16(kbase + (size_t)s * 64 * 8192, voffW[j], b + j * 1024);
        else if (j < 4) glds16(vbase + (size_t)s * 64 * 8192, voffW[j - 2], b + 16384 + (j - 2) * 1024);
        else glds16(cbase + s * 128, voffC[j - 4], b + 32768 + (j - 4) * 1024);
    };
    auto dmaG = [&](int k, int j) { glds16(cbase + (size_t)(32 * k + wid + 8 * j) * 1024, voffG[j & 1], lds0 + gbuf(k) + (wid + 8 * j) * 1024); };
    bf16x8 cf[8];
    WAIT_BAR(12);
    rdW(0, 0);
#pragma unroll
    for (int i = 0; i < 4; ++i) cf[i] = rdC(0, i);
    static_for<128>([&](auto SI) {
        {
            constexpr int s = SI / 16, i = SI % 16;
            if (i == 12) {
                if (s < 6) { WAIT_BAR(6); } else if (s == 6) { WAIT_BAR(8); } else { WAIT_BAR(8); }
            }
            if (s == 7 && i == 11) {
                const int ln = lane_now();
#pragma unroll
                for (int j = 0; j < 4; ++j) gA[j] = lds + (ln & 15) * 1024 + (((4 * j + (ln >> 4)) ^ (ln & 15)) << 4);
            }
            if (s == 1 && i == 13) mid();
            constexpr int nx = i + 4;
            if constexpr (nx < 16) { if (nx == 8) rdW(s, 1); cf[nx & 7] = rdC(s, nx); }
            else if constexpr (s < 7) { if (nx == 16) rdW(s + 1, 0); cf[nx & 7] = rdC(s + 1, nx - 16); }
            else cf[nx & 7] = rdG(4, nx - 16);
            acc[i & 7][0] = mm(wf[2 * s + (i >> 3)][0], cf[i & 7], acc[i & 7][0]);
            acc[i & 7][1] = mm(wf[2 * s + (i >> 3)][1], cf[i & 7], acc[i & 7][1]);
            if (s + 3 < 8) { if (i == 12 || i == 14) dmaS(s + 3, (i - 12) / 2); }
            else if (i >= 12) {
                const int j = i - 12;
                if (s == 5) dmaG(4, j);
                else if (s == 6) dmaG(6, j);
                else dmaG(7, j);
            }
            if (i < 12 && i % 3 == 0) { if (s >= 1 && s + 2 < 8) dmaS(s + 2, 2 + i / 3); }
            if (s == 6 && i >= 2 && i < 10 && !(i & 1)) dmaG(5, (i - 2) / 2);
            if (s == 7 && i == 15) out_tile(0, acc[0][0], acc[0][1], acc[1][0], acc[1][1]);
            SBAR();
        }
    });
    stamp2();
    f32x4 a2[2][2][2];
    static_for<128>([&](auto KI) {
        {
            constexpr int k = 4 + KI / 32, ks = KI % 32;
            if (ks == 28 && k < 7) {
                if (k == 4) { WAIT_BAR(16); } else if (k == 5) { WAIT_BAR(16); } else { WAIT_BAR(21); }
            }
            constexpr int nx = ks + 4;
            if constexpr (nx < 32) cf[nx & 7] = rdG(k, nx); else if constexpr (k < 7) cf[nx & 7] = rdG(k + 1, nx - 32);
#pragma unroll
            for (int b = 0; b < 2; ++b) a2[k & 1][ks & 1][b] = mm(wf[ks >> 1][b], cf[ks & 7], ks < 2 ? f32x4{} : a2[k & 1][ks & 1][b]);
            if constexpr (k == 4) { if constexpr (ks < 3) out_tile(ks + 1, acc[2 * ks + 2][0], acc[2 * ks + 2][1], acc[2 * ks + 3][0], acc[2 * ks + 3][1]); }
            else if constexpr (ks == 3) out_tile(k - 1, a2[(k - 1) & 1][0][0], a2[(k - 1) & 1][0][1], a2[(k - 1) & 1][1][0], a2[(k - 1) & 1][1][1]);
            tail(KI);
            SBAR();
        }
    });
    out_tile(7, a2[1][0][0], a2[1][0][1], a2[1][1][0], a2[1][1][1]);
}

DI void ld_u16(unsigned& d, const void* sbase, unsigned voff) { asm volatile("global_load_ushort %0, %1, %2" : "=v"(d) : "v"(voff), "s"(sbase)); }
DI void ld_x4(u32x4& d, const void* sbase, unsigned voff) { asm volatile("global_load_dwordx4 %0, %1, %2" : "=v"(d) : "v"(voff), "s"(sbase)); }
DI void ld_u16_nop(unsigned& d, const void* sbase, unsigned voff) { asm volatile("s_nop 4\n\tglobal_load_ushort %0, %1, %2" : "=v"(d) : "v"(voff), "s"(sbase)); }
DI void ld_x4_nop(u32x4& d, const void* sbase, unsigned voff) { asm volatile("s_nop 4\n\tglobal_load_dwordx4 %0, %1, %2" : "=v"(d) : "v"(voff), "s"(sbase)); }
DI float lo16(unsigned v) { return __uint_as_float(v << 16); }
DI float hi16(unsigned v) { return __uint_as_float(v & 0xFFFF0000u); }
struct Params {
    const u16 *ax, *ckv, *wq, *wuk, *wuv, *wo, *wr, *wg, *wu, *wd, *gw;
    char* ws; u16* out;
};
struct Ctx { lptr lds; char* lds_; unsigned lds0; int tid, lane, wid, bid; unsigned tag; unsigned* sy; };
typedef __attribute__((address_space(4))) const Params* kparams_t;
DI Params load_params() {
    kparams_t pp = (kparams_t)__builtin_amdgcn_kernarg_segment_ptr();
    asm volatile("" : "+s"(pp));
    Params P;
    P.ax = pp->ax; P.ckv = pp->ckv; P.wq = pp->wq; P.wuk = pp->wuk; P.wuv = pp->wuv; P.wo = pp->wo; P.wr = pp->wr; P.wg = pp->wg; P.wu = pp->wu;
    P.wd = pp->wd; P.gw = pp->gw; P.ws = pp->ws; P.out = pp->out;
    return P;
}
DI Ctx make_ctx(char* lds_, char* ws, int wid, unsigned tag) {
    Ctx C;
    C.lds = (lptr)lds_; C.lds_ = lds_; C.lds0 = (unsigned)(uintptr_t)C.lds;
    int lane; asm volatile("v_mbcnt_lo_u32_b32 %0, -1, 0\n\tv_mbcnt_hi_u32_b32 %0, -1, %0" : "=v"(lane));
    asm volatile("" : "+s"(wid), "+s"(tag));
    C.lane = lane; C.wid = wid; C.tid = wid * 64 + lane; C.bid = blockIdx.x; C.tag = tag; C.sy = (unsigned*)(ws + WS_SYNC);
    return C;
}

#define GU_K 5
#define GU_KS 8
constexpr int QST = 49152;
struct QPre { unsigned voffX[4], voffW[2]; u32x4 rv, wgv, wuv; };
constexpr int MOE_WD = 147456 + 2048;
DI int moe_f0(int bid) { return ((bid & 7) * 28 + ((bid >> 3) < 28 ? (bid >> 3) : 27)) * MOE_COLS; }
DI void q_dma(const QPre& Q, const Params& P, const Ctx& C, int s, int j) {
    const int wid = C.wid, h = (C.bid & 7) * 4 + ((C.bid >> 3) & 3), bi = C.bid >> 5;
    const char* xbase = (const char*)P.ax + (size_t)(((wid >> 2) ? 15 - bi : bi) * 128 + (wid & 3) * 32) * 1024;
    const char* wbase = (const char*)P.wq + h * 256;
    const unsigned b = C.lds0 + ((s + 2) % 3) * QST;
    if (j < 2) glds16(wbase + (size_t)s * 64 * 8192, Q.voffW[j], b + 32768 + (2 * wid + j) * 1024);
    else glds16(xbase + s * 128, Q.voffX[j - 2], b + (4 * wid + j - 2) * 1024);
}
DI void phase_kv(const Params& P, Ctx& C, QPre& Q, unsigned tagv) {
    char* ws = P.ws;
    const int lane = C.lane, wid = C.wid, bid = C.bid, h = (bid & 7) * 4 + ((bid >> 3) & 3), p = bid >> 5;
    STAMP(0, 0);
    const int tok = bid * 8 + wid;
    u32x4 bxv, wrv, gwv;
    ld_x4(bxv, (const char*)P.ax + (size_t)tok * DM * 2, lane * 16); ld_x4(wrv, P.wr, lane * 16); ld_x4(gwv, P.gw, lane * 16);
    auto pre = [&]() {
        unsigned t, xb_[4], wr_[4], gw_[4];
        asm volatile("s_waitcnt vmcnt(18)\n\tv_readfirstlane_b32 %0, %13\n\t"
                     "v_mov_b32 %1, %14\n\tv_mov_b32 %2, %15\n\tv_mov_b32 %3, %16\n\tv_mov_b32 %4, %17\n\t"
                     "v_mov_b32 %5, %18\n\tv_mov_b32 %6, %19\n\tv_mov_b32 %7, %20\n\tv_mov_b32 %8, %21\n\t"
                     "v_mov_b32 %9, %22\n\tv_mov_b32 %10, %23\n\tv_mov_b32 %11, %24\n\tv_mov_b32 %12, %25"
                     : "=&s"(t), "=&v"(xb_[0]), "=&v"(xb_[1]), "=&v"(xb_[2]), "=&v"(xb_[3]), "=&v"(wr_[0]), "=&v"(wr_[1]), "=&v"(wr_[2]), "=&v"(wr_[3]),
                       "=&v"(gw_[0]), "=&v"(gw_[1]), "=&v"(gw_[2]), "=&v"(gw_[3])
                     : "v"(tagv), "v"(bxv[0]), "v"(bxv[1]), "v"(bxv[2]), "v"(bxv[3]), "v"(wrv[0]), "v"(wrv[1]), "v"(wrv[2]), "v"(wrv[3]),
                       "v"(gwv[0]), "v"(gwv[1]), "v"(gwv[2]), "v"(gwv[3]));
        C.tag = t + 1u;
        float sr = 0.f, sg = 0.f;
#pragma unroll
        for (int j = 0; j < 4; ++j) {
            const float x0 = lo16(xb_[j]), x1 = hi16(xb_[j]);
            sr += x0 * lo16(wr_[j]); sr += x1 * hi16(wr_[j]); sg += x0 * lo16(gw_[j]); sg += x1 * hi16(gw_[j]);
        }
        sr = wave_sum(sr, lane); sg = wave_sum(sg, lane);
        if (lane == 0) { store_f32_wt((float*)(ws + WS_R) + tok, bfround(sr)); store_f32_wt((float*)(ws + WS_G) + tok, sigmoidf_(sg)); }
    };
    auto mid = [&]() { if (C.tid == 0) flag_store(C.sy + SY_R + bid, C.tag); };
    const char* cbase = (const char*)P.ckv + (size_t)p * 256 * 1024;
    const char* kbase = (const char*)P.wuk + h * 256;
    const char* vbase = (const char*)P.wuv + h * 256;
    const unsigned outoff = (unsigned)((h * 64 + 8 * p) * 8 + 2 * (wid & 3)) * 1024;
    const unsigned kvbytes = (unsigned)((size_t)SEQ * NHD * 2);
    unsigned rp[4];
    auto tail = [&](auto KI) {
        constexpr int k = 4 + KI / 32, ks = KI % 32;
        if constexpr (k == 4 && ks == 20) {
            const int ln = lane_now();
#pragma unroll
            for (int j = 0; j < 4; ++j) { const int r = 8 * j + (ln >> 3); Q.voffX[j] = r * 1024 + (((ln & 7) ^ ((r >> 1) & 7)) << 4); }
#pragma unroll
            for (int j = 0; j < 2; ++j) { const int row = 4 * (2 * wid + j) + (ln >> 4); Q.voffW[j] = row * 8192 + (((ln & 15) ^ ((row & 3) << 2)) << 4); }
            if (wid == 0) {
#pragma unroll
                for (int j = 0; j < 4; ++j) asm volatile("global_load_dword %0, %1, %2 sc1" : "=v"(rp[j]) : "v"(ln * 4), "s"(C.sy + SY_R + 64 * j));
            }
        }
        if constexpr (k == GU_K && ks == GU_KS) ld_x4(Q.wgv, (const char*)P.wg + moe_f0(bid) * 2, (wid * 64 + lane_now()) * (FF * 2));
        if constexpr (k == GU_K && ks == GU_KS + 1) ld_x4(Q.wuv, (const char*)P.wu + moe_f0(bid) * 2, (wid * 64 + lane_now()) * (FF * 2));
        if constexpr (k == 6 && ks == 8) glds16((const char*)P.wd + (size_t)(moe_f0(bid) + wid) * 1024, lane_now() * 16, C.lds0 + MOE_WD + wid * 1024);
        if constexpr (k == 5 && ks >= 28) q_dma(Q, P, C, 0, ks - 28);
        if constexpr (k == 6 && ks < 2) q_dma(Q, P, C, 0, 4 + ks);
        if constexpr (k == 6 && ks == 20) {
            if (wid == 0) {
                asm volatile("s_waitcnt vmcnt(13)" : "+v"(rp[0]), "+v"(rp[1]), "+v"(rp[2]), "+v"(rp[3]));
                if (__builtin_expect(!__all(rp[0] == C.tag && rp[1] == C.tag && rp[2] == C.tag && rp[3] == C.tag), 0)) wait_flags<4>(C.sy + SY_R, 256, C.tag, lane_now());
            }
        }
        if constexpr (k == 6 && ks >= 28) q_dma(Q, P, C, 1, ks - 28);
        if constexpr (k == 7 && ks < 2) q_dma(Q, P, C, 1, 4 + ks);
        if constexpr (k == 7 && ks == 5) asm volatile("global_load_dwordx4 %0, %1, %2 sc1" : "=v"(Q.rv) : "v"((wid * 64 + lane_now()) * 16), "s"(P.ws + WS_R));
    };
    auto st2 = [&]() { };
    if (wid < 4) kv_body<false>(cbase, kbase, vbase, make_rsrc(ws + WS_KF, kvbytes), outoff, C.lds, C.lds0, wid, lane, pre, mid, tail, st2);
    else         kv_body<true>(cbase, kbase, vbase, make_rsrc(ws + WS_VF, kvbytes), outoff, C.lds, C.lds0, wid, lane, pre, mid, tail, st2);
    STAMP(0, 3);
}

DI unsigned umax_(unsigned a, unsigned b) { return a > b ? a : b; }
DI unsigned umin_(unsigned a, unsigned b) { return a < b ? a : b; }

constexpr int K2_QST = 49152, K2_SLOT = 32768, K2_QB = 131072, K2_LDS = 163840;
constexpr float QSCALE = 0.08838834764831845f * 1.4426950408889634f;
constexpr float THR = 8.0f;
constexpr int RING0 = 3;

#define SETPRIO(x) __builtin_amdgcn_s_setprio(x)
struct SMState { float mhat, l; f32x16 negm; };
#define PIN(x) asm volatile("" : "+v"(x))
DI float max3(float a, float b, float c) { return fmaxf(fmaxf(a, b), c); }

#define EXP2(x) __builtin_amdgcn_exp2f(x)
#define VOFF(i) (((i) >> 3) * 8192 + (2 * ((i) & 3) + (((i) >> 2) & 1)) * 1024)
#define KOFF(i) (((i) & 1) * 8192 + ((i) >> 1) * 1024)
DI float fadd_s(float a, float b) { float r; asm("v_add_f32_e32 %0, %1, %2" : "=v"(r) : "v"(a), "v"(b)); return r; }
DI float max3_s(float a, float b, float c) { float r; asm("v_max3_f32 %0, %1, %2, %3" : "=v"(r) : "v"(a), "v"(b), "v"(c)); return r; }
DI float max2_s(float a, float b) { float r; asm("v_max_f32_e32 %0, %1, %2" : "=v"(r) : "v"(a), "v"(b)); return r; }
template <int OFF> DI void glds_step(const void* sbase, unsigned voff, unsigned lds_addr) {
    if (OFF == 0) asm volatile("s_mov_b32 m0, %2\n\ts_nop 0\n\tglobal_load_lds_dwordx4 %0, %1 sc1" :: "v"(voff), "s"(sbase), "s"(lds_addr));
    else asm volatile("global_load_lds_dwordx4 %0, %1 offset:%c2 sc1" :: "v"(voff), "s"(sbase), "i"(OFF));
}
DI f32x16 mfma_c(bf16x8 a, bf16x8 b, const f32x16& c) {
    f32x16 d; asm volatile("v_mfma_f32_32x32x16_bf16 %0, %1, %2, %3" : "=&v"(d) : "v"(a), "v"(b), "v"(c)); return d;
}
struct DmaJob { const char* src0; const char* src1; unsigned dst0, dst1; bool has0, has1; };

#define LA 4
template <bool QK, bool FIN, class Mid>
DI void attn_A(lptr kp, const lptr& vpn, const bf16x8 (&qf)[8], const f32x16& negm, const bf16x8 (&kpre)[LA], bf16x8 (&vpre)[LA], f32x16& s0, f32x16& s1,
               const f32x16& p0, const f32x16& p1, bf16x8 (&pf)[4], float& l, const DmaJob& dj, unsigned vlin, Mid&& mid) {
    bf16x8 fr[16];
    unsigned pw[16];
    float sa = 0.f, sb = 0.f;
#pragma unroll
    for (int i = 0; i < LA; ++i) fr[i] = kpre[i];
#pragma unroll
    for (int i = 0; i < 16; ++i) {
        if (QK && i + LA < 16) fr[i + LA] = ldsr128a(kp + KOFF(i + LA));
        if (i + LA >= 16) vpre[i + LA - 16] = ldsr128a(vpn + VOFF(i + LA - 16));
        if (QK) {
            if (i == 0) s0 = mfma_c(fr[0], qf[0], negm);
            else if (i == 1) s1 = mfma_c(fr[1], qf[0], negm);
            else if (i & 1) s1 = MFMA(fr[i], qf[i >> 1], s1);
            else s0 = MFMA(fr[i], qf[i >> 1], s0);
        }
        if (FIN) {
            const float e0 = i < 8 ? p0[2 * i] : p1[2 * i - 16], e1 = i < 8 ? p0[2 * i + 1] : p1[2 * i - 15];
            if (i == 0) { sa = e0; sb = e1; } else { sa = fadd_s(sa, e0); sb = fadd_s(sb, e1); PIN(sa); PIN(sb); }
            pw[i] = cvtpk(e0, e1);
            PIN(pw[i]);
        }
        if (i == 3) mid();
        if (QK) {
            if (i == 1 && dj.has0) { glds_step<0>(dj.src0, vlin, dj.dst0); glds_step<1024>(dj.src0, vlin, dj.dst0); }
            if (i == 5 && dj.has0) { glds_step<2048>(dj.src0, vlin, dj.dst0); glds_step<3072>(dj.src0, vlin, dj.dst0); }
            if (i == 9 && dj.has1) { glds_step<0>(dj.src1, vlin, dj.dst1); glds_step<1024>(dj.src1, vlin, dj.dst1); }
            if (i == 13 && dj.has1) { glds_step<2048>(dj.src1, vlin, dj.dst1); glds_step<3072>(dj.src1, vlin, dj.dst1); }
        }
        SBAR();
    }
    if (FIN) {
        l += sa + sb;
#pragma unroll
        for (int i = 0; i < 4; ++i) { u32x4 w = {pw[4 * i], pw[4 * i + 1], pw[4 * i + 2], pw[4 * i + 3]}; pf[i] = __builtin_bit_cast(bf16x8, w); }
    }
}
DI void dma_plain(const DmaJob& dj, unsigned vlin) {
    if (dj.has0) glds16x4_dev(dj.src0, vlin, dj.dst0);
    if (dj.has1) glds16x4_dev(dj.src1, vlin, dj.dst1);
}
template <bool PV, bool SM, class Mid>
DI void attn_B(lptr vp, const lptr& kpn, f32x16 (&o)[4], const bf16x8 (&pf)[4], const bf16x8 (&vpre)[LA], bf16x8 (&kpre)[LA],
               f32x16& s0, f32x16& s1, SMState& st, bool first, bool mask, int qrel  , bool& pend, float& fpend, Mid&& mid) {
    bf16x8 fr[16];
    float ma = 0.f, mb = 0.f, mc = 0.f;
#pragma unroll
    for (int i = 0; i < LA; ++i) fr[i] = vpre[i];
    if (SM && __builtin_expect(mask, 0)) {
        const float NEG = -__builtin_inff();
#pragma unroll
        for (int r = 0; r < 16; ++r) { const int c = (r & 3) + 8 * (r >> 3) + 16 * ((r >> 2) & 1);
            if (c > qrel) s0[r] = NEG;
            if (c + 32 > qrel) s1[r] = NEG; }
    }
    SETPRIO(1);
    static_for<16>([&](auto I) {
        constexpr int i = I;
        if (PV && i + LA < 16) fr[i + LA] = ldsr128a(vp + VOFF(i + LA));
        if (i + LA >= 16) kpre[i + LA - 16] = ldsr128a(kpn + KOFF(i + LA - 16));
        if (PV) o[i & 3] = MFMA(fr[i], pf[i >> 2], o[i & 3]);
        if (SM) {
            if (i == 0) { ma = max3(s0[0], s0[1], s0[2]); mb = max3(s1[0], s1[1], s1[2]); mc = max3(s0[3], s0[4], s1[3]); PIN(ma); PIN(mb); PIN(mc); }
            if (i == 1) { ma = max3_s(ma, s0[5], s0[6]); mb = max3_s(mb, s1[4], s1[5]); mc = max3_s(mc, s0[7], s1[6]); PIN(ma); PIN(mb); PIN(mc); }
            if (i == 2) { ma = max3_s(ma, s0[8], s0[9]); mb = max3_s(mb, s1[7], s1[8]); mc = max3_s(mc, s0[10], s1[9]); PIN(ma); PIN(mb); PIN(mc); }
            if (i == 3) { ma = max3_s(ma, s0[11], s0[12]); mb = max3_s(mb, s1[10], s1[11]); mc = max3_s(mc, s0[13], s1[12]); PIN(ma); PIN(mb); PIN(mc); }
            if (i == 4) { ma = max3_s(ma, s0[14], s0[15]); mb = max3_s(mb, s1[13], s1[14]); mc = max2_s(mc, s1[15]); PIN(ma); PIN(mb); PIN(mc); }
            if (i == 5) {
                float rm = max3_s(ma, mb, mc);
                auto rr = __builtin_amdgcn_permlane32_swap(__float_as_uint(rm), __float_as_uint(rm), false, false);
                rm = max2_s(__uint_as_float(rr[0]), __uint_as_float(rr[1]));
                if (__builtin_expect(first || __any(rm > THR), 0)) {
                    const float dl = first ? rm : fmaxf(rm, 0.f), f = first ? 1.0f : __builtin_amdgcn_exp2f(-dl);
                    st.mhat += dl; st.l *= f;
#pragma unroll
                    for (int r = 0; r < 16; ++r) { s0[r] -= dl; s1[r] -= dl; st.negm[r] = -st.mhat; }
                    pend = !first; fpend = f;
                }
            }
            if (i >= 6) {
                constexpr int e0 = i < 14 ? 3 * (i - 6) : 24 + 4 * (i - 14), e1 = i < 14 ? e0 + 3 : e0 + 4;
#pragma unroll
                for (int e = e0; e < e1; ++e) { if (e < 16) { s0[e] = EXP2(s0[e]); } else { s1[e - 16] = EXP2(s1[e - 16]); } }
                PIN(s0); PIN(s1);
            }
        }
        if (i == 7) mid();
        SBAR();
    });
    SETPRIO(0);
}

struct Routed { int sel0, sel1; float sig0, sig1; };
DI void phase_attn(const Params& P, const Ctx& C, QPre& Q, Routed& R) {
    char* ws = P.ws; char* lds_ = C.lds_;
    const u16 *x = P.ax, *wq = P.wq, *wo = P.wo, *bx = P.ax, *wg = P.wg, *wu = P.wu, *wd = P.wd;
    const lptr lds = C.lds;
    const unsigned lds0 = C.lds0;
    const int tid = C.tid, lane = C.lane, l31 = lane & 31, hi = lane >> 5, wid = C.wid;
    const int bid = C.bid, h = (bid & 7) * 4 + ((bid >> 3) & 3), bi = bid >> 5;
    const int grp = wid >> 2, rg = wid & 3;
    const int blkA = bi, blkB = 15 - bi;

    STAMP(1, 0);
    LAS unsigned* sk = (LAS unsigned*)((LAS char*)lds_ + 147456);
    LAS float* hs = (LAS float*)((LAS char*)lds_ + 147456 + 128);
    LAS float* red = (LAS float*)((LAS char*)lds_ + 147456 + 256);
    const int cg = moe_f0(bid) / MOE_COLS;
    unsigned k1 = 0, k2 = 0, xs0, xs1;
    u32x4 skv[4];
    float mv[32], ma0, ma1, hsv[16];
    unsigned wdv[8];
    auto top2 = [&](unsigned b1, unsigned b2) { k2 = umax_(umin_(k1, b1), umax_(k2, b2)); k1 = umax_(k1, b1); };
    auto ins = [&](unsigned key) { k2 = umax_(k2, umin_(k1, key)); k1 = umax_(k1, key); };
    auto moe_gap = [&](auto GI) {
        constexpr int G = GI, s = G / 16, i = G % 16;
        if constexpr (G == 13 || G == 14) {
            if constexpr (G == 13) asm volatile("" : "+v"(Q.rv));
#pragma unroll
            for (int j = 2 * (G - 13); j < 2 * (G - 13) + 2; ++j) {
                const unsigned u = Q.rv[j] >> 16;
                const unsigned o = (u & 0x8000u) ? (~u & 0xFFFFu) : (u | 0x8000u);
                ins((o << 16) | (unsigned)(0xFFFF - (tid * 4 + j)));
            }
            PIN(k1); PIN(k2);
        }
        if constexpr (G == 15) { top2(dppu<0x128>(k1), dppu<0x128>(k2)); PIN(k1); PIN(k2); }
        if constexpr (G == 16) { top2(dppu<0x124>(k1), dppu<0x124>(k2)); PIN(k1); PIN(k2); }
        if constexpr (G == 17) { top2(dppu<0x122>(k1), dppu<0x122>(k2)); PIN(k1); PIN(k2); }
        if constexpr (G == 18) { top2(dppu<0x121>(k1), dppu<0x121>(k2)); PIN(k1); PIN(k2); }
        if constexpr (G == 19) { auto a = __builtin_amdgcn_permlane16_swap(k1, k1, false, false); auto b = __builtin_amdgcn_permlane16_swap(k2, k2, false, false);
            k1 = a[0]; k2 = b[0]; top2(a[1], b[1]); PIN(k1); PIN(k2); }
        if constexpr (G == 20) { auto a = __builtin_amdgcn_permlane32_swap(k1, k1, false, false); auto b = __builtin_amdgcn_permlane32_swap(k2, k2, false, false);
            k1 = a[0]; k2 = b[0]; top2(a[1], b[1]);
            if (lane == 0) { sk[wid * 2] = k1; sk[wid * 2 + 1] = k2; } }
        if constexpr (G == 29) {
#pragma unroll
            for (int j = 0; j < 4; ++j) skv[j] = *(const LAS u32x4*)(sk + 4 * j);
            k1 = 0; k2 = 0;
        }
        if constexpr (G == 30 || G == 31) {
#pragma unroll
            for (int j = 8 * (G - 30); j < 8 * (G - 30) + 8; j += 2) top2(skv[j >> 2][j & 3], skv[j >> 2][(j & 3) + 1]);
            PIN(k1); PIN(k2);
        }
        if constexpr (G == 32) {
            k1 = __builtin_amdgcn_readfirstlane(k1); k2 = __builtin_amdgcn_readfirstlane(k2);
            R.sel0 = 0xFFFF - (int)(k1 & 0xFFFFu); R.sel1 = 0xFFFF - (int)(k2 & 0xFFFFu);
            ld_u16_nop(xs0, (const char*)bx + R.sel0 * 1024, tid * 2);
            ld_u16_nop(xs1, (const char*)bx + R.sel1 * 1024, tid * 2);
        }
        if constexpr (G == 33) {
            const unsigned o0 = k1 >> 16, o1 = k2 >> 16;
            const float r0 = __uint_as_float(((o0 & 0x8000u) ? (o0 & 0x7FFFu) : (~o0 & 0xFFFFu)) << 16);
            const float r1 = __uint_as_float(((o1 & 0x8000u) ? (o1 & 0x7FFFu) : (~o1 & 0xFFFFu)) << 16);
            R.sig0 = __uint_as_float(__builtin_amdgcn_readfirstlane(__float_as_uint(sigmoidf_(r0))));
            R.sig1 = __uint_as_float(__builtin_amdgcn_readfirstlane(__float_as_uint(sigmoidf_(r1))));
        }
        if constexpr (G >= 61 && G <= 64) {
            if constexpr (G == 61) { asm volatile("" : "+v"(xs0), "+v"(xs1), "+v"(Q.wgv), "+v"(Q.wuv)); ma0 = lo16(xs0); ma1 = lo16(xs1); }
            constexpr int c = 2 * (G - 61);
            const float g0 = lo16(Q.wgv[c >> 1]), g1 = hi16(Q.wgv[c >> 1]), u0 = lo16(Q.wuv[c >> 1]), u1 = hi16(Q.wuv[c >> 1]);
            mv[c] = ma0 * g0; mv[c + 1] = ma0 * g1; mv[8 + c] = ma0 * u0; mv[9 + c] = ma0 * u1;
            mv[16 + c] = ma1 * g0; mv[17 + c] = ma1 * g1; mv[24 + c] = ma1 * u0; mv[25 + c] = ma1 * u1;
            PIN(mv[c]); PIN(mv[c + 1]); PIN(mv[8 + c]); PIN(mv[9 + c]); PIN(mv[16 + c]); PIN(mv[17 + c]); PIN(mv[24 + c]); PIN(mv[25 + c]);
        }
        if constexpr (G >= 65 && G <= 68) {
#pragma unroll
            for (int j = 4 * (G - 65); j < 4 * (G - 65) + 4; ++j) { auto r = __builtin_amdgcn_permlane32_swap(__float_as_uint(mv[j]), __float_as_uint(mv[j + 16]), false, false);
                mv[j] = __uint_as_float(r[0]) + __uint_as_float(r[1]); PIN(mv[j]); }
        }
        if constexpr (G == 69 || G == 70) {
#pragma unroll
            for (int j = 4 * (G - 69); j < 4 * (G - 69) + 4; ++j) { auto r = __builtin_amdgcn_permlane16_swap(__float_as_uint(mv[j]), __float_as_uint(mv[j + 8]), false, false);
                mv[j] = __uint_as_float(r[0]) + __uint_as_float(r[1]); PIN(mv[j]); }
        }
        if constexpr (G == 71) { const bool up_ = lane & 8;
#pragma unroll
            for (int j = 0; j < 4; ++j) { const float send = up_ ? mv[j] : mv[j + 4], keep = up_ ? mv[j + 4] : mv[j]; mv[j] = keep + dppf<0x128>(send); PIN(mv[j]); } }
        if constexpr (G == 72) { const bool up_ = lane & 4;
#pragma unroll
            for (int j = 0; j < 2; ++j) { const float send = up_ ? mv[j] : mv[j + 2], keep = up_ ? mv[j + 2] : mv[j]; mv[j] = keep + dppf<0x1B>(dppf<0x141>(send)); PIN(mv[j]); } }
        if constexpr (G == 73) { const bool up_ = lane & 2;
            const float send = up_ ? mv[0] : mv[1], keep = up_ ? mv[1] : mv[0]; mv[0] = keep + dppf<0x4E>(send);
            mv[0] += dppf<0xB1>(mv[0]);
            if (!(lane & 1)) red[wid * 32 + (lane >> 1)] = mv[0];
        }
        if constexpr (G == 77) {
            if (tid < 16) {
                const int c = tid & 7, t = tid >> 3;
                float g = 0.f, u = 0.f;
#pragma unroll
                for (int w = 0; w < 8; ++w) { g += red[w * 32 + 16 * t + c]; u += red[w * 32 + 16 * t + 8 + c]; }
                g = bfround(g); u = bfround(u);
                hs[t * 8 + c] = bfround(bfround(g * sigmoidf_(g)) * u);
            }
        }
        if constexpr (G == 93) {
#pragma unroll
            for (int j = 0; j < 8; ++j) wdv[j] = *(const LAS u16*)((LAS char*)lds_ + MOE_WD + j * 1024 + tid * 2);
#pragma unroll
            for (int j = 0; j < 4; ++j) { const f32x4 t = *(const LAS f32x4*)(hs + 4 * j); hsv[4 * j] = t[0]; hsv[4 * j + 1] = t[1]; hsv[4 * j + 2] = t[2]; hsv[4 * j + 3] = t[3]; }
        }
        if constexpr (G == 95) {
            float y0 = 0.f, y1 = 0.f;
#pragma unroll
            for (int j = 0; j < 8; ++j) { const float w = lo16(wdv[j]); y0 += hsv[j] * w; y1 += hsv[8 + j] * w; }
            float* yp = (float*)(ws + WS_YP) + (size_t)cg * 1024;
            store_f32_wt(yp + tid, y0); store_f32_wt(yp + 512 + tid, y1);
        }
        (void)s; (void)i;
    };

    const char* Kh = ws + WS_KF + (size_t)h * 64 * 8192;
    const unsigned vlin = lane * 16;
    bf16x8 qf[8];
    {
        lptr xA[4];
#pragma unroll
        for (int ks = 0; ks < 4; ++ks) xA[ks] = lds + (32 * wid + l31) * 128 + (((2 * ks + hi) ^ ((l31 >> 1) & 7)) << 4);
        const int tq = (lane & 15) >> 2, tp = lane & 3, tblk = (lane >> 4) & 1;
        lptr wA[4];
#pragma unroll
        for (int c = 0; c < 4; ++c) wA[c] = lds + 32768 + (8 * hi + tq) * 256 + ((c ^ tq) << 6) + (tp >> 1) * 32 + tblk * 16 + (tp & 1) * 8;
        auto dmaS = [&](int s, int j) { q_dma(Q, P, C, s, j); };
        auto rdW = [&](int s, int i) { const int o = ((s + 2) % 3) * QST + (i >> 2) * 4096; return cat(ldstr(wA[i & 3] + o), ldstr(wA[i & 3] + o + 1024)); };
        auto rdX = [&](int s, int ks) { return ldsr128(xA[ks] + ((s + 2) % 3) * QST); };
        f32x16 acc[4] = {};
        bf16x8 wfr[8], xfr[2];
        unsigned kvf = 0;
        WAIT_BAR(14);
        STAMP(1, 1);
        xfr[0] = rdX(0, 0);
#pragma unroll
        for (int i = 0; i < 4; ++i) wfr[i] = rdW(0, i);
        static_for<128>([&](auto SI) {
            {
                constexpr int s = SI / 16, i = SI % 16;
                if (i == 12 && s < 7) {
                    if (s == 0) { WAIT_BAR(8); } else if (s == 1) { WAIT_BAR(6); } else if (s == 2) { WAIT_BAR(8); } else if (s < 6) { WAIT_BAR(6); } else { WAIT_BAR(0); }
                }
                const int nx = i + 4;
                if (nx < 16) { if ((nx & 3) == 0) xfr[(nx >> 2) & 1] = rdX(s, nx >> 2); wfr[nx & 7] = rdW(s, nx); }
                else if (s < 7) { if (nx == 16) xfr[0] = rdX(s + 1, 0); wfr[nx & 7] = rdW(s + 1, nx - 16); }
                acc[i & 3] = MFMA(wfr[i & 7], xfr[(i >> 2) & 1], acc[i & 3]);
                if (i == 12 || i == 14) { if (s + 3 < 8) dmaS(s + 3, (i - 12) / 2); }
                else if (s == 0) { if (i < 6) dmaS(2, i); }
                else if (i < 12 && i % 3 == 0) { if (s + 2 < 8) dmaS(s + 2, 2 + i / 3); }
                if (s == 6 && grp == 0) {
                    if (i == 12) glds16x4_dev(Kh + rg * 4096, vlin, lds0 + RING0 * 16384 + rg * 4096);
                    if (i == 14) glds16x4_dev(Kh + 16384 + rg * 4096, vlin, lds0 + (RING0 + 2) * 16384 + rg * 4096);
                }
                moe_gap(SI);
                if (s == 1 && i == 13 && tid == 0) flag_store(C.sy + SY_KV + h * 8 + bi, C.tag);
                if (s == 4 && i == 3 && wid == 0)
                    asm volatile("global_load_dword %0, %1, %2 sc1" : "=v"(kvf) : "v"((lane & 7) * 4), "s"(C.sy + SY_KV + h * 8));
                if (s == 5 && i == 13 && wid == 0) {
                    asm volatile("" : "+v"(kvf));
                    if (__builtin_expect(!__all(kvf == C.tag), 0)) wait_flags<1>(C.sy + SY_KV + h * 8, 8, C.tag, lane);

                }
                if (s == 6 && i == 13 && tid == 0) flag_store(C.sy + SY_Y + bid, C.tag);
                SBAR();
            }
        });
#pragma unroll
        for (int c = 0; c < 4; ++c) { qf[2 * c] = pack8(acc[c], 0, QSCALE); qf[2 * c + 1] = pack8(acc[c], 1, QSCALE); }
    }

    STAMP(1, 2);
    const int n1 = 2 * bi + 2;
    auto tile_of = [&](int n, int g) { return n < n1 ? n : n1 + 2 * (n - n1) + g; };
    auto jstar = [&](int tn) { return tn <= 2 * n1 ? tn : tn <= 2 * n1 + 2 ? tn + 1 : 2 * tn - 2 * n1 - 2; };
    auto jg1 = [&](int tn) { return tn == 2 * n1 + 1 ? tn : 2 * tn - 2 * n1 - 1; };
    int tb_d0, tb_d1, tb_kv, tb_rel;
    auto make_job = [&](int tn, int& d0, int& d1) {
        const int n = tn >> 1;
        int a_off, b_off; bool a_ok, b_ok;
        const int kb = (int)WS_KF + h * 64 * 8192 + rg * 4096, vb = (int)WS_VF + h * 64 * 8192 + rg * 4096;
        if (tn & 1) { a_ok = n >= 1 && n <= 17; a_off = vb + tile_of(n - 1, 0) * 16384; b_ok = n >= n1 && n <= 16; b_off = kb + tile_of(n, 1) * 16384; }
        else        { a_ok = n <= 16;           a_off = kb + tile_of(n, 0) * 16384;     b_ok = n - 2 >= n1 && n <= 18; b_off = vb + tile_of(n - 2, 1) * 16384; }
        const int da = a_off | ((jstar(tn) + RING0) & 7) | 8, db = b_off | ((jg1(tn) + RING0) & 7) | 8;
        d0 = a_ok ? da : b_ok ? db : 0; d1 = (a_ok && b_ok) ? db : 0;
    };
    {
        const int n = lane < 19 ? lane : 18;
        make_job(2 * n + grp + 3, tb_d0, tb_d1);
        const int ks = ((n < n1 || grp == 0 ? jstar(2 * n) : jg1(2 * n + 1)) + RING0) & 7;
        const int vs = ((n < n1 || grp == 0 ? jstar(2 * n + 3) : jg1(2 * n + 4)) + RING0) & 7;
        tb_kv = ks | vs << 8;
        tb_rel = 128 * ((grp || n >= n1) ? blkB : blkA) + 32 * rg - 64 * tile_of(n, grp);
    }
    auto job_from = [&](int d0, int d1) -> DmaJob {
        DmaJob d;
        d.has0 = d0 & 8; d.has1 = d1 & 8;
        d.src0 = ws + (unsigned)(d0 & ~4095); d.src1 = ws + (unsigned)(d1 & ~4095);
        d.dst0 = lds0 + (d0 & 7) * 16384 + rg * 4096; d.dst1 = lds0 + (d1 & 7) * 16384 + rg * 4096;
        return d;
    };
    auto job = [&](int n) { return job_from(__builtin_amdgcn_readlane(tb_d0, n), __builtin_amdgcn_readlane(tb_d1, n)); };
    if (grp == 1) {
#pragma unroll
        for (int c = 0; c < 8; ++c) *(LAS bf16x8*)((LAS char*)lds_ + K2_QB + rg * 8192 + c * 1024 + lane * 16) = qf[c];
    }
    f32x16 o[4] = {};
    SMState st; st.mhat = 0.f; st.l = 0.f; st.negm = f32x16{};
    const lptr kp0 = lds + lane * 16;
    char* ctx = ws + WS_CTX;
    const rsrc_t ctxr = make_rsrc(ctx, (unsigned)((size_t)SEQ * NHD * 2));
    auto store_ctx = [&](int blk, int d, const f32x16& v, float inv) {
        const unsigned off = (unsigned)(((4 * blk + rg) * 32 + h) * 8 + 2 * d) * 1024 + lane * 16;
        store_wt(ctxr, off, pack8(v, 0, inv));
        store_wt(ctxr, off + 1024, pack8(v, 1, inv));
    };
    auto half_sum = [&](float v) { auto rr = __builtin_amdgcn_permlane32_swap(__float_as_uint(v), __float_as_uint(v), false, false);
                                   return __uint_as_float(rr[0]) + __uint_as_float(rr[1]); };
    auto kslot = [&](int n) { return kp0 + (__builtin_amdgcn_readlane(tb_kv, n) & 7) * 16384; };
    auto vslot = [&](int n) { return kp0 + ((__builtin_amdgcn_readlane(tb_kv, n) >> 8) & 7) * 16384; };
    WAIT_BAR(0);
    bool first = true, pend = false;
    float invA = 0.f, fpend = 1.f;
    f32x16 sA0, sA1, sB0, sB1; bf16x8 pf[4], vpre[LA], kpre[LA];
#pragma unroll
    for (int i = 0; i < LA; ++i) kpre[i] = ldsr128a(kslot(0) + KOFF(i));
    if (grp == 1) { WAIT_BAR(0); }
    SEG_DECL; SEG_START();
    const int nsw = grp == 0 ? n1 - 1 : 100;
    auto headB = [&](int n) {
        if (__builtin_expect(n == nsw + 1, 0)) { invA = 1.0f / half_sum(st.l); st.mhat = 0.f; st.l = 0.f; first = true; }
    };
    auto tailB = [&](int n) {
        first = false;
        if (__builtin_expect(pend || (unsigned)(n - nsw) < 3u, 0)) {
            if (n == nsw + 2 && tid == 0) flag_store(C.sy + SY_CTX + blkA * 32 + h, C.tag);
            if (pend) {
#pragma unroll
                for (int d = 0; d < 4; ++d)
#pragma unroll
                    for (int r = 0; r < 16; ++r) o[d][r] *= fpend;
                pend = false;
            }
            if (n == nsw + 1) {
#pragma unroll
                for (int d = 0; d < 4; ++d) store_ctx(blkA, d, o[d], invA);
#pragma unroll
                for (int d = 0; d < 4; ++d) o[d] = f32x16{};
            }
            if (n == nsw) {
#pragma unroll
                for (int c = 0; c < 8; ++c) qf[c] = ldsr128(lds + K2_QB + rg * 8192 + c * 1024 + lane * 16);
                st.negm = f32x16{};
            }
        }
    };
#define BARRIER_L() asm volatile("s_waitcnt lgkmcnt(0)\n\ts_barrier" ::: "memory")
    lptr kcur = kslot(0), vcur = kp0;
    DmaJob dj = job(0);
    int rel = 0;
    auto pinjob = [&]() { asm volatile("" : "+s"(dj.src0), "+s"(dj.src1), "+s"(dj.dst0), "+s"(dj.dst1)); };
    auto midA = [&](int n) { vcur = vslot(n > 0 ? n - 1 : 0); rel = __builtin_amdgcn_readlane(tb_rel, n); asm volatile("" : "+v"(vcur), "+s"(rel)); };
    auto midB = [&](int n) { kcur = kslot(n + 1); dj = job(n + 1); asm volatile("" : "+v"(kcur)); pinjob(); };
#define RELARGS first, rel < 63, rel + l31 - 4 * hi, pend, fpend
    attn_A<true, false>(kcur, vcur, qf, st.negm, kpre, vpre, sA0, sA1, sA0, sA1, pf, st.l, dj, vlin, [&]() { midA(0); });
    BARRIER_L();
    attn_B<false, true>(vcur, kcur, o, pf, vpre, kpre, sA0, sA1, st, RELARGS, [&]() { midB(0); });
    tailB(0);
    WAIT_BAR(0);
#define STEP(n, C0, C1, P0, P1) do { \
        attn_A<true, true>(kcur, vcur, qf, st.negm, kpre, vpre, C0, C1, P0, P1, pf, st.l, dj, vlin, [&]() { midA(n); }); \
        SEG(0); \
        BARRIER_L(); \
        SEG(2); \
        headB(n); \
        attn_B<true, true>(vcur, kcur, o, pf, vpre, kpre, C0, C1, st, RELARGS, [&]() { midB(n); }); \
        tailB(n); \
        SEG(3); \
        if ((n) < 16) { WAIT_BAR(0); } else { BARRIER_L(); }     \
        SEG(4); \
    } while (0)
    unsigned pfd;
    for (int n = 1; n < 17; n += 2) {
        STEP(n, sB0, sB1, sA0, sA1);
        STEP(n + 1, sA0, sA1, sB0, sB1);
    }
#undef STEP
    asm volatile("global_load_dword %0, %1, %2" : "=v"(pfd) : "v"(vlin * 8),
                 "s"((const char*)wo + (size_t)(wid < 2 ? bid * 2 + wid : (bid >> 3) * 4 + ((wid - 2) & 3)) * 8192));
    attn_A<false, true>(kcur, vcur, qf, st.negm, kpre, vpre, sB0, sB1, sA0, sA1, pf, st.l, dj, vlin, [&]() { midA(17); });
    BARRIER_L();
    attn_B<true, false>(vcur, kcur, o, pf, vpre, kpre, sB0, sB1, st, false, false, 0, pend, fpend, [&]() {});
    BARRIER_L();
    SEG_OUT(1);
    if (grp == 0) { BARRIER_L(); }
    STAMP(1, 3);
    unsigned cfl = 0;
    const int ort = (bid & 7) * 4 + ((bid >> 3) & 3);
    if (wid == 0) asm volatile("global_load_dword %0, %1, %2 sc1" : "=v"(cfl) : "v"((lane & 31) * 4), "s"(C.sy + SY_CTX + (ort >> 1) * 32));
    BARRIER_L();
    {
        const float lsum = half_sum(st.l);
        LAS char* xch = (LAS char*)lds_;
        LAS char* mine = xch + wid * 8192;
        auto put = [&](int dd, const f32x16& v) {
#pragma unroll
            for (int q4 = 0; q4 < 4; ++q4)
                *(LAS f32x4*)(mine + (dd * 4 + q4) * 1024 + lane * 16) = (f32x4){v[4 * q4], v[4 * q4 + 1], v[4 * q4 + 2], v[4 * q4 + 3]};
        };
        if (grp) { put(0, o[0]); put(1, o[1]); } else { put(0, o[2]); put(1, o[3]); }
        LAS float* ml = (LAS float*)(xch + 65536);
        ml[wid * 128 + lane] = st.mhat; ml[wid * 128 + 64 + lane] = lsum;
        BARRIER_L();
        const int ow = wid ^ 4;
        const float m2 = ml[ow * 128 + lane], l2 = ml[ow * 128 + 64 + lane];
        const float mm = fmaxf(st.mhat, m2);
        const float f1 = __builtin_amdgcn_exp2f(st.mhat - mm), f2 = __builtin_amdgcn_exp2f(m2 - mm);
        const float inv = 1.0f / (lsum * f1 + l2 * f2);
        const float c1 = f1 * inv, c2 = f2 * inv;
        const LAS char* theirs = xch + ow * 8192;
        auto fin = [&](int dd, f32x16 v) {
#pragma unroll
            for (int q4 = 0; q4 < 4; ++q4) {
                const f32x4 t = *(const LAS f32x4*)(theirs + (dd * 4 + q4) * 1024 + lane * 16);
#pragma unroll
                for (int e = 0; e < 4; ++e) v[4 * q4 + e] = v[4 * q4 + e] * c1 + t[e] * c2;
            }
            store_ctx(blkB, 2 * grp + dd, v, 1.0f);
        };
        if (grp) { fin(0, o[2]); fin(1, o[3]); } else { fin(0, o[0]); fin(1, o[1]); }
    }
    asm volatile("s_waitcnt vmcnt(0)" : "+v"(pfd), "+v"(cfl));
    if (wid == 0) {
        const unsigned bal = (unsigned)__ballot(cfl == C.tag);
        unsigned m0 = 0;
#pragma unroll
        for (int g = 0; g < 8; ++g) m0 |= (((bal >> (4 * g)) & 15u) == 15u ? 1u : 0u) << g;
        if (__builtin_popcount(m0) < 2) m0 = 0;
        if (lane == 0) *(LAS unsigned*)((LAS char*)lds_ + 163840 - 16) = m0;
    }
    BARRIER_L();
    if (tid == 0) { flag_store(C.sy + SY_CTX + blkB * 32 + h, C.tag); flag_store(C.sy + SY_CTX + blkA * 32 + h, C.tag); }
    STAMP(1, 4);
}

constexpr int K3_SLOT = 32768, K3_NS = 5;
DI void phase_oproj(const Params& P, const Ctx& C, const Routed& R) {
    char* ws = P.ws; char* lds_ = C.lds_;
    const lptr lds = C.lds;
    const unsigned lds0 = C.lds0;
    const int lane = C.lane, l31 = lane & 31, hi = lane >> 5, wid = C.wid;
    const int bid = C.bid, rt = (bid & 7) * 4 + ((bid >> 3) & 3), ct = bid >> 5;
    STAMP(2, 0);
    unsigned m0 = __builtin_amdgcn_readfirstlane(*(const LAS unsigned*)((LAS char*)lds_ + 163840 - 16));
    if (!m0) {
        if (wid == 0) {
            const unsigned* cf_ = C.sy + SY_CTX + (rt >> 1) * 32;
            const unsigned long long t0 = __builtin_amdgcn_s_memrealtime();
            unsigned m = 0;
            for (;;) {
                const unsigned bal = (unsigned)__ballot(flag_load(cf_ + (lane & 31)) == C.tag);
                m = 0;
#pragma unroll
                for (int g = 0; g < 8; ++g) m |= (((bal >> (4 * g)) & 15u) == 15u ? 1u : 0u) << g;
                if (__builtin_popcount(m) >= 2) break;
                if (__builtin_amdgcn_s_memrealtime() - t0 > 5000000ull) { m = 0xFFu; break; }
                __builtin_amdgcn_s_sleep(2);
            }
            if (lane == 0) *(LAS unsigned*)((LAS char*)lds_ + 163840 - 32) = m;
        }
        asm volatile("s_waitcnt lgkmcnt(0)\n\ts_barrier" ::: "memory");
        m0 = __builtin_amdgcn_readfirstlane(*(const LAS unsigned*)((LAS char*)lds_ + 163840 - 32));
        asm volatile("s_waitcnt lgkmcnt(0)\n\ts_barrier" ::: "memory");
    }
    const int nAg = __builtin_popcount(m0);
    const int rot = 0;
    unsigned ord = 0;
    { int u = 0;
#pragma unroll
      for (int k = 0; k < 8; ++k) { const int g = (k + rot) & 7; if ((m0 >> g) & 1) { ord |= (unsigned)g << (4 * u); ++u; } }
#pragma unroll
      for (int k = 0; k < 8; ++k) { const int g = (k + rot) & 7; if (!((m0 >> g) & 1)) { ord |= (unsigned)g << (4 * u); ++u; } } }
    if (wid >= 4) {
        const int lw = wid - 4;
        const char* csrc = ws + WS_CTX + (size_t)(2 * rt + (lw >> 1)) * 32 * 8192 + (lw & 1) * 4096;
        const char* wsrc = (const char*)P.wo + ct * 128;
        unsigned voffW[4];
#pragma unroll
        for (int j = 0; j < 4; ++j) { const int row = (lw * 4 + j) * 8 + (lane >> 3);
            voffW[j] = row * 1024 + (((lane & 7) ^ (((row >> 1) & 1) << 2)) << 4); }
        const unsigned vlin = lane * 16;
        auto head = [&](int t) { return (int)(4 * ((ord >> (4 * (t >> 2))) & 7) + (t & 3)); };
        auto stageW = [&](int t, int slot) {
            const unsigned b = lds0 + slot * K3_SLOT + lw * 4096;
#pragma unroll
            for (int j = 0; j < 4; ++j) glds16(wsrc + (size_t)head(t) * 131072, voffW[j], b + 16384 + j * 1024);
        };
        auto stageC = [&](int t, int slot) { glds16x4_dev(csrc + (size_t)head(t) * 8192, vlin, lds0 + slot * K3_SLOT + lw * 4096); };
#pragma unroll
        for (int t = 0; t < 4; ++t) { stageC(t, t); stageW(t, t); }
        int slot = 4;
        for (int hh = 0; hh < 28; ++hh) {
            WAIT_BAR(24);
            stageC(hh + 4, slot); stageW(hh + 4, slot);
            slot = slot == K3_NS - 1 ? 0 : slot + 1;
        }
        WAIT_BAR(24); WAIT_BAR(16); WAIT_BAR(8); WAIT_BAR(0);
        return;
    }
    const int qg2 = wid >> 1, cb = wid & 1;
    const int i0 = R.sel0, i1 = R.sel1;
    const bool has0 = (i0 >> 6) == rt, has1 = (i1 >> 6) == rt;
    const int q = rt * 64 + qg2 * 32 + l31;
    const unsigned* cflags = C.sy + SY_CTX + (rt >> 1) * 32;
    const float g = __uint_as_float(flag_load((const unsigned*)(ws + WS_G) + q));
    s16x4 xb[4];
#pragma unroll
    for (int q4 = 0; q4 < 4; ++q4) xb[q4] = *(const s16x4*)(P.ax + (size_t)q * DM + ct * 64 + cb * 32 + 8 * q4 + 4 * hi);
    const int tq = (lane & 15) >> 2, tp = lane & 3, tblk = (lane >> 4) & 1;
    const lptr wA = lds + 16384 + (4 * hi + tq) * 128 + ((cb ^ ((tq >> 1) & 1)) << 6) + tblk * 32 + tp * 8;
    const lptr cA = lds + qg2 * 8192 + lane * 16;
    f32x16 accs[8] = {};
    int bo = 0;
    auto run4 = [&](f32x16& acc) {
#pragma unroll 1
        for (int hh = 0; hh < 4; ++hh) {
            asm volatile("s_waitcnt lgkmcnt(0)\n\ts_barrier" ::: "memory");
            bf16x8 cf[8], wf[8];
#pragma unroll
            for (int f = 0; f < 4; ++f) { wf[f] = cat(ldstr(wA + bo + f * 2048), ldstr(wA + bo + f * 2048 + 1024)); cf[f] = ldsr128(cA + bo + f * 1024); }
            SBAR();
#pragma unroll
            for (int f = 0; f < 8; ++f) {
                acc = MFMA(wf[f], cf[f], acc);
                if (f + 4 < 8) { wf[f + 4] = cat(ldstr(wA + bo + (f + 4) * 2048), ldstr(wA + bo + (f + 4) * 2048 + 1024)); cf[f + 4] = ldsr128(cA + bo + (f + 4) * 1024); }
                SBAR();
            }
            bo = bo == (K3_NS - 1) * K3_SLOT ? 0 : bo + K3_SLOT;
        }
    };
    auto slots = [&](int u0, int u1) {
        for (int u = u0; u < u1; ++u) {
            switch ((ord >> (4 * u)) & 7) {
                case 0: run4(accs[0]); break; case 1: run4(accs[1]); break; case 2: run4(accs[2]); break; case 3: run4(accs[3]); break;
                case 4: run4(accs[4]); break; case 5: run4(accs[5]); break; case 6: run4(accs[6]); break; default: run4(accs[7]); break;
            }
            if (wid == 0 && u == nAg - 2 && nAg < 8) wait_flags<1>(cflags, 32, C.tag, lane);
        }
    };
    unsigned yf[4]; f32x4 yv4[4];
    if (wid == 1) {
#pragma unroll
        for (int j = 0; j < 4; ++j) yf[j] = flag_load(C.sy + SY_Y + lane + 64 * j);
    }
    slots(0, 1);
    STAMP(2, 2);
    if (wid == 1) {
        if (!__all(yf[0] == C.tag && yf[1] == C.tag && yf[2] == C.tag && yf[3] == C.tag)) wait_flags<4>(C.sy + SY_Y, 256, C.tag, lane);
        const rsrc_t ypr = make_rsrc(ws + WS_YP, MOE_WGS * 4096);
#pragma unroll
        for (int j = 0; j < 4; ++j)
            yv4[j] = __builtin_bit_cast(f32x4, __builtin_amdgcn_raw_buffer_load_b128(ypr, (lane + 64 * j) * 4096 + bid * 16, 0, 16));
    }
    slots(1, 3);
    if (wid == 1) {
        f32x4 y = (yv4[0] + yv4[1]) + (yv4[2] + yv4[3]);
#pragma unroll
        for (int e = 0; e < 4; ++e) y[e] = wave_sum(y[e], lane);
        if (lane < 4) store_f32_wt((float*)(ws + WS_Y) + bid * 4 + lane, lane == 0 ? y[0] : lane == 1 ? y[1] : lane == 2 ? y[2] : y[3]);
    }
    slots(3, 6);
    if (wid == 1) {
        asm volatile("s_waitcnt vmcnt(0)" ::: "memory");
        if (lane == 0) flag_store(C.sy + SY_Y2 + bid, C.tag);
    }
    slots(6, 8);
    STAMP(2, 3);
    f32x16 acc = accs[0];
#pragma unroll
    for (int gg = 1; gg < 8; ++gg) acc += accs[gg];
    const bool r0 = (q == i0), r1 = (q == i1);
    f32x4 yrow[4] = {};
    if (has0 || has1) {
        wait_flags<1>(C.sy + SY_Y2 + ct * 16, 16, C.tag, lane);
        wait_flags<1>(C.sy + SY_Y2 + 128 + ct * 16, 16, C.tag, lane);
        const rsrc_t yr = make_rsrc(ws + WS_Y, 4096);
#pragma unroll
        for (int q4 = 0; q4 < 4; ++q4)
            yrow[q4] = __builtin_bit_cast(f32x4, __builtin_amdgcn_raw_buffer_load_b128(yr, ((r1 ? 512 : 0) + ct * 64 + cb * 32 + 8 * q4 + 4 * hi) * 4, 0, 16));
    }
    {
        const float ysc = r0 ? R.sig0 : r1 ? R.sig1 : 0.f;
        u32x2 w2[4];
#pragma unroll
        for (int q4 = 0; q4 < 4; ++q4) {
            float res[4];
#pragma unroll
            for (int e = 0; e < 4; ++e) {
                float b = bf2f((u16)xb[q4][e]);
                if (r0 || r1) b += yrow[q4][e] * ysc;
                res[e] = g * acc[4 * q4 + e] + (1.0f - g) * b;
            }
            w2[q4] = (u32x2){cvtpk(res[0], res[1]), cvtpk(res[2], res[3])};
        }
        const rsrc_t outr = make_rsrc(P.out, SEQ * DM * 2);
#pragma unroll
        for (int k = 0; k < 4; k += 2) {
            auto rx = __builtin_amdgcn_permlane32_swap(w2[k][0], w2[k + 1][0], false, false);
            auto ry = __builtin_amdgcn_permlane32_swap(w2[k][1], w2[k + 1][1], false, false);
            const u32x4 v = {rx[0], ry[0], rx[1], ry[1]};
            __builtin_amdgcn_raw_buffer_store_b128(v, outr, (q * DM + ct * 64 + cb * 32 + 8 * k + 8 * hi) * 2, 0, 16);
        }
    }
    STAMP(2, 4);
}

__global__ __launch_bounds__(512, 2)
void mega_kernel(Params Parg) {
    extern __shared__ __attribute__((aligned(16))) char lds_[];
    const int wid = __builtin_amdgcn_readfirstlane(threadIdx.x >> 6);
    unsigned tag;
    Routed R;
    QPre Q;
    {
        const Params P = load_params();
        unsigned tagv;
        asm volatile("global_load_dword %0, %1, %2 sc1" : "=v"(tagv) : "v"(0), "s"((const unsigned*)(P.ws + WS_SYNC) + SY_EPOCH));
        Ctx C = make_ctx(lds_, P.ws, wid, 0u);
        phase_kv(P, C, Q, tagv);
        tag = __builtin_amdgcn_readfirstlane(C.tag);
    }
    { const Params P = load_params(); phase_attn(P, make_ctx(lds_, P.ws, wid, tag), Q, R); }
    {
        const Params P = load_params();
        const Ctx C = make_ctx(lds_, P.ws, wid, tag);
        phase_oproj(P, C, R);
        if (C.tid == 0) __hip_atomic_fetch_add(C.sy + SY_EPOCH, 1u, __ATOMIC_RELAXED, __HIP_MEMORY_SCOPE_AGENT);
    }
}

extern "C" void kernel_launch(void* const* d_in, const int* in_sizes, int n_in, void* d_out, int out_size, void* d_ws, size_t ws_size,
                              hipStream_t stream) {
    const u16* ax = (const u16*)d_in[0]; const u16* ckv = (const u16*)d_in[1]; const u16* wq = (const u16*)d_in[2];
    const u16* wuk = (const u16*)d_in[3]; const u16* wuv = (const u16*)d_in[4]; const u16* wo = (const u16*)d_in[5];
      const u16* wr = (const u16*)d_in[7]; const u16* wg = (const u16*)d_in[8];
    const u16* wu = (const u16*)d_in[9]; const u16* wd = (const u16*)d_in[10]; const u16* gw = (const u16*)d_in[11];
    char* ws = (char*)d_ws;
    (void)hipFuncSetAttribute((const void*)mega_kernel, hipFuncAttributeMaxDynamicSharedMemorySize, 163840);
    Params P{ax, ckv, wq, wuk, wuv, wo, wr, wg, wu, wd, gw, ws, (u16*)d_out};
    mega_kernel<<<256, 512, 163840, stream>>>(P);
}
```

```cpp
#include <hip/hip_runtime.h>
#include <hip/hip_bf16.h>
#include <stdint.h>
#include <utility>

typedef unsigned short u16;
typedef short bf16x8 __attribute__((ext_vector_type(8)));
typedef short s16x4 __attribute__((ext_vector_type(4)));
typedef short v4i16_t __attribute__((ext_vector_type(4)));
typedef float f32x16 __attribute__((ext_vector_type(16)));
typedef float f32x4 __attribute__((ext_vector_type(4)));
typedef unsigned u32x4 __attribute__((ext_vector_type(4)));
typedef unsigned u32x2 __attribute__((ext_vector_type(2)));
#define LAS __attribute__((address_space(3)))
#define DI __device__ __forceinline__
#define MFMA(a, b, c) __builtin_amdgcn_mfma_f32_32x32x16_bf16((a), (b), (c), 0, 0, 0)
#define MFMA16(a, b, c) __builtin_amdgcn_mfma_f32_16x16x32_bf16((a), (b), (c), 0, 0, 0)
#define SBAR() __builtin_amdgcn_sched_barrier(0)

constexpr int SEQ = 2048, DM = 512, NH = 32, HD = 128, NHD = NH * HD;
constexpr int FF = 1792;
constexpr int MOE_WGS = 224, MOE_COLS = 8;

constexpr size_t WS_KF = 0;
constexpr size_t WS_VF = WS_KF + (size_t)SEQ * NHD * 2;
constexpr size_t WS_CTX = WS_VF + (size_t)SEQ * NHD * 2;
constexpr size_t WS_R = WS_CTX + (size_t)SEQ * NHD * 2;
constexpr size_t WS_G = WS_R + SEQ * 4;
constexpr size_t WS_Y = WS_G + SEQ * 4;
constexpr size_t WS_YP = WS_Y + 4096;
constexpr size_t WS_SYNC = WS_YP + 224 * 4096;
constexpr int SY_EPOCH = 0, SY_KV = 256, SY_R = 512, SY_Y = 768, SY_Y2 = 1024, SY_CTX = 1280;

template <int... Is, class F> DI void static_for_(std::integer_sequence<int, Is...>, F&& f) { (f(std::integral_constant<int, Is>{}), ...); }
template <int N, class F> DI void static_for(F&& f) { static_for_(std::make_integer_sequence<int, N>{}, f); }
DI unsigned cvtpk(float lo, float hi) {
    typedef float f32x2 __attribute__((ext_vector_type(2)));
    typedef __bf16 bf16x2 __attribute__((ext_vector_type(2)));
    f32x2 v = {lo, hi}; bf16x2 b = __builtin_convertvector(v, bf16x2); return __builtin_bit_cast(unsigned, b);
}
DI float bf2f(u16 v) { return __uint_as_float((unsigned)v << 16); }
DI float bfround(float x) { return __uint_as_float(cvtpk(x, x) << 16); }
DI bf16x8 pack8(const f32x16& x, int s, float sc) {
    u32x4 w = {cvtpk(x[8 * s] * sc, x[8 * s + 1] * sc), cvtpk(x[8 * s + 2] * sc, x[8 * s + 3] * sc),
               cvtpk(x[8 * s + 4] * sc, x[8 * s + 5] * sc), cvtpk(x[8 * s + 6] * sc, x[8 * s + 7] * sc)};
    return __builtin_bit_cast(bf16x8, w);
}
DI void glds16(const void* sbase, unsigned voff, unsigned lds_addr) {
    lds_addr = __builtin_amdgcn_readfirstlane(lds_addr);
    asm volatile("s_mov_b32 m0, %2\n\ts_nop 0\n\tglobal_load_lds_dwordx4 %0, %1" :: "v"(voff), "s"(sbase), "s"(lds_addr) : "memory");
}
DI unsigned flag_load(const unsigned* p) { return __hip_atomic_load(p, __ATOMIC_RELAXED, __HIP_MEMORY_SCOPE_AGENT); }
DI void flag_store(unsigned* p, unsigned v) { __hip_atomic_store(p, v, __ATOMIC_RELAXED, __HIP_MEMORY_SCOPE_AGENT); }
DI void store_f32_wt(float* p, float v) { __hip_atomic_store((unsigned*)p, __float_as_uint(v), __ATOMIC_RELAXED, __HIP_MEMORY_SCOPE_AGENT); }
template <int NW> DI void wait_flags(const unsigned* f, int n, unsigned tag, int lane) {
    const unsigned long long t0 = __builtin_amdgcn_s_memrealtime();
    for (;;) {
        bool ok = true;
#pragma unroll
        for (int j = 0; j < NW; ++j) { const int i = lane + 64 * j; ok &= (i < n ? flag_load(f + i) : tag) == tag; }
        if (__all(ok)) break;
        if (__builtin_amdgcn_s_memrealtime() - t0 > 5000000ull) break;
        __builtin_amdgcn_s_sleep(2);
    }
}
DI void glds16x4_dev(const void* sbase, unsigned voff, unsigned lds_addr) {
    lds_addr = __builtin_amdgcn_readfirstlane(lds_addr);
    asm volatile("s_mov_b32 m0, %2\n\ts_nop 0\n\tglobal_load_lds_dwordx4 %0, %1 sc1\n\tglobal_load_lds_dwordx4 %0, %1 offset:1024 sc1\n\t"
                 "global_load_lds_dwordx4 %0, %1 offset:2048 sc1\n\tglobal_load_lds_dwordx4 %0, %1 offset:3072 sc1"
                 :: "v"(voff), "s"(sbase), "s"(lds_addr) : "memory");
}
#define WAIT_BAR(N) asm volatile("s_waitcnt vmcnt(" #N ") lgkmcnt(0)\n\ts_barrier" ::: "memory")
DI int lane_now() { int l; asm volatile("v_mbcnt_lo_u32_b32 %0, -1, 0\n\tv_mbcnt_hi_u32_b32 %0, -1, %0" : "=v"(l)); return l; }
typedef LAS const char* lptr;
DI bf16x8 ldsr128(lptr p) { return *(const LAS bf16x8*)p; }
DI s16x4 ldstr(lptr p) { return __builtin_bit_cast(s16x4, __builtin_amdgcn_ds_read_tr16_b64_v4i16((LAS v4i16_t*)p)); }
DI bf16x8 cat(s16x4 a, s16x4 b) { return __builtin_shufflevector(a, b, 0, 1, 2, 3, 4, 5, 6, 7); }
DI unsigned shx(unsigned v, int m, int lane) { return (unsigned)__builtin_amdgcn_ds_bpermute((lane ^ m) << 2, (int)v); }
DI float shx(float v, int m, int lane) { return __uint_as_float(shx(__float_as_uint(v), m, lane)); }
template <int CTRL> DI unsigned dppu(unsigned v) { return (unsigned)__builtin_amdgcn_update_dpp(0, (int)v, CTRL, 0xF, 0xF, true); }
template <int CTRL> DI float dppf(float v) { return __uint_as_float(dppu<CTRL>(__float_as_uint(v))); }
DI float wave_sum(float v, int lane) {
#pragma unroll
    for (int o = 32; o >= 1; o >>= 1) v += shx(v, o, lane);
    return v;
}
DI float sigmoidf_(float x) { return 1.0f / (1.0f + __expf(-x)); }

constexpr int K1_STAGE = 49152;
typedef __amdgpu_buffer_rsrc_t rsrc_t;
DI rsrc_t make_rsrc(const void* p, unsigned bytes) { return __builtin_amdgcn_make_buffer_rsrc((void*)p, (short)0, (int)bytes, 0x00020000); }
DI void store_wt(rsrc_t r, unsigned off, bf16x8 v) {
    __builtin_amdgcn_raw_buffer_store_b128(__builtin_bit_cast(u32x4, v), r, (int)off, 0, 16);
}

template <bool ISV, class Pre, class Mid, class Tail>
DI void kv_body(const char* cbase, const char* kbase, const char* vbase, rsrc_t outr, unsigned outoff, lptr lds, unsigned lds0, int wid, int lane,
                Pre&& pre, Mid&& mid, Tail&& tail) {
    const int r16 = lane & 15, q4 = lane >> 4;
    unsigned voffW[2], voffC[2], voffG[2];
#pragma unroll
    for (int j = 0; j < 2; ++j) {
        const int n = 2 * wid + j;
        { const int row = 4 * n + (lane >> 4); voffW[j] = row * 8192 + (((lane & 15) ^ ((row & 3) << 2) ^ (((row >> 3) & 1) << 1)) << 4); }
        { const int row = 8 * n + (lane >> 3); voffC[j] = row * 1024 + (((lane & 7) ^ ((row >> 1) & 7)) << 4); }
        voffG[j] = (lane ^ ((wid + 8 * j) & 15)) << 4;
    }
    auto stage = [&](int s) {
        const unsigned b = lds0 + (s % 3) * K1_STAGE + 2 * wid * 1024;
#pragma unroll
        for (int j = 0; j < 2; ++j) glds16(kbase + (size_t)s * 64 * 8192, voffW[j], b + j * 1024);
#pragma unroll
        for (int j = 0; j < 2; ++j) glds16(vbase + (size_t)s * 64 * 8192, voffW[j], b + 16384 + j * 1024);
#pragma unroll
        for (int j = 0; j < 2; ++j) glds16(cbase + s * 128, voffC[j], b + 32768 + j * 1024);
    };
    auto gbuf = [&](int k) { return k == 4 ? 98304 : k == 5 ? 131072 : k == 6 ? 0 : 49152; };
    const int tq = (lane & 15) >> 2, tp = lane & 3;
    lptr wA[2], cA[2];
#pragma unroll
    for (int b = 0; b < 2; ++b) {
        wA[b] = lds + (ISV ? 16384 : 0) + (8 * q4 + tq) * 256 + (((wid & 3) ^ tq) << 6) + ((b ^ (q4 & 1)) << 5) + tp * 8;
        cA[b] = lds + 32768 + r16 * 128 + (((4 * b + q4) ^ (r16 >> 1)) << 4);
    }
    auto mm = [&](const bf16x8& w, const bf16x8& c, const f32x4& acc) { return ISV ? MFMA16(c, w, acc) : MFMA16(w, c, acc); };
    const unsigned olane = outoff + (q4 >> 1) * 1024 + (ISV ? (r16 + 32 * (q4 & 1)) * 16 : (16 * (r16 >> 3) + (r16 & 7) + 32 * (q4 & 1)) * 16);
    auto out_tile = [&](int kg, const f32x4& t00, const f32x4& t01, const f32x4& t10, const f32x4& t11) {
        auto pk = [](const f32x4& x, const f32x4& y) { u32x4 w = {cvtpk(x[0], x[1]), cvtpk(x[2], x[3]), cvtpk(y[0], y[1]), cvtpk(y[2], y[3])}; return __builtin_bit_cast(bf16x8, w); };
        if (ISV) { store_wt(outr, olane + kg * 8192, pk(t00, t10)); store_wt(outr, olane + kg * 8192 + 256, pk(t01, t11)); }
        else     { store_wt(outr, olane + kg * 8192, pk(t00, t01)); store_wt(outr, olane + kg * 8192 + 128, pk(t10, t11)); }
    };

    bf16x8 wf[16][2];
    f32x4 acc[8][2] = {};
    lptr gA[4];
    stage(0); stage(1); stage(2);
    pre();
    auto rdW = [&](int s, int sl) { const int o = (s % 3) * K1_STAGE + sl * 8192;
#pragma unroll
        for (int b = 0; b < 2; ++b) wf[2 * s + sl][b] = cat(ldstr(wA[b] + o), ldstr(wA[b] + o + 1024)); };
    auto rdC = [&](int s, int i) { return ldsr128(cA[i >> 3] + (s % 3) * K1_STAGE + (i & 7) * 2048); };
    auto rdG = [&](int k, int ks) { return ldsr128(gA[(ks >> 1) & 3] + gbuf(k) + (ks & 1) * 16384 + (ks >> 3) * 256); };
    auto dmaS = [&](int s, int j) {
        const unsigned b = lds0 + (s % 3) * K1_STAGE + 2 * wid * 1024;
        if (j < 2) glds16(kbase + (size_t)s * 64 * 8192, voffW[j], b + j * 1024);
        else if (j < 4) glds16(vbase + (size_t)s * 64 * 8192, voffW[j - 2], b + 16384 + (j - 2) * 1024);
        else glds16(cbase + s * 128, voffC[j - 4], b + 32768 + (j - 4) * 1024);
    };
    auto dmaG = [&](int k, int j) { glds16(cbase + (size_t)(32 * k + wid + 8 * j) * 1024, voffG[j & 1], lds0 + gbuf(k) + (wid + 8 * j) * 1024); };
    bf16x8 cf[8];
    WAIT_BAR(12);
    rdW(0, 0);
#pragma unroll
    for (int i = 0; i < 4; ++i) cf[i] = rdC(0, i);
    static_for<128>([&](auto SI) {
        {
            constexpr int s = SI / 16, i = SI % 16;
            if (i == 12) {
                if (s < 6) { WAIT_BAR(6); } else if (s == 6) { WAIT_BAR(8); } else { WAIT_BAR(8); }
            }
            if (s == 7 && i == 11) {
                const int ln = lane_now();
#pragma unroll
                for (int j = 0; j < 4; ++j) gA[j] = lds + (ln & 15) * 1024 + (((4 * j + (ln >> 4)) ^ (ln & 15)) << 4);
            }
            if (s == 1 && i == 13) mid();
            constexpr int nx = i + 4;
            if constexpr (nx < 16) { if (nx == 8) rdW(s, 1); cf[nx & 7] = rdC(s, nx); }
            else if constexpr (s < 7) { if (nx == 16) rdW(s + 1, 0); cf[nx & 7] = rdC(s + 1, nx - 16); }
            else cf[nx & 7] = rdG(4, nx - 16);
            acc[i & 7][0] = mm(wf[2 * s + (i >> 3)][0], cf[i & 7], acc[i & 7][0]);
            acc[i & 7][1] = mm(wf[2 * s + (i >> 3)][1], cf[i & 7], acc[i & 7][1]);
            if (s + 3 < 8) { if (i == 12 || i == 14) dmaS(s + 3, (i - 12) / 2); }
            else if (i >= 12) {
                const int j = i - 12;
                if (s == 5) dmaG(4, j);
                else if (s == 6) dmaG(6, j);
                else dmaG(7, j);
            }
            if (i < 12 && i % 3 == 0) { if (s >= 1 && s + 2 < 8) dmaS(s + 2, 2 + i / 3); }
            if (s == 6 && i >= 2 && i < 10 && !(i & 1)) dmaG(5, (i - 2) / 2);
            if (s == 7 && i == 15) out_tile(0, acc[0][0], acc[0][1], acc[1][0], acc[1][1]);
            SBAR();
        }
    });
    f32x4 a2[2][2][2];
    static_for<128>([&](auto KI) {
        {
            constexpr int k = 4 + KI / 32, ks = KI % 32;
            if (ks == 28 && k < 7) {
                if (k == 4) { WAIT_BAR(16); } else if (k == 5) { WAIT_BAR(16); } else { WAIT_BAR(21); }
            }
            constexpr int nx = ks + 4;
            if constexpr (nx < 32) cf[nx & 7] = rdG(k, nx); else if constexpr (k < 7) cf[nx & 7] = rdG(k + 1, nx - 32);
#pragma unroll
            for (int b = 0; b < 2; ++b) a2[k & 1][ks & 1][b] = mm(wf[ks >> 1][b], cf[ks & 7], ks < 2 ? f32x4{} : a2[k & 1][ks & 1][b]);
            if constexpr (k == 4) { if constexpr (ks < 3) out_tile(ks + 1, acc[2 * ks + 2][0], acc[2 * ks + 2][1], acc[2 * ks + 3][0], acc[2 * ks + 3][1]); }
            else if constexpr (ks == 3) out_tile(k - 1, a2[(k - 1) & 1][0][0], a2[(k - 1) & 1][0][1], a2[(k - 1) & 1][1][0], a2[(k - 1) & 1][1][1]);
            tail(KI);
            SBAR();
        }
    });
    out_tile(7, a2[1][0][0], a2[1][0][1], a2[1][1][0], a2[1][1][1]);
}

DI void ld_x4(u32x4& d, const void* sbase, unsigned voff) { asm volatile("global_load_dwordx4 %0, %1, %2" : "=v"(d) : "v"(voff), "s"(sbase)); }
DI void ld_u16_nop(unsigned& d, const void* sbase, unsigned voff) { asm volatile("s_nop 4\n\tglobal_load_ushort %0, %1, %2" : "=v"(d) : "v"(voff), "s"(sbase)); }
DI float lo16(unsigned v) { return __uint_as_float(v << 16); }
DI float hi16(unsigned v) { return __uint_as_float(v & 0xFFFF0000u); }
struct Params {
    const u16 *ax, *ckv, *wq, *wuk, *wuv, *wo, *wr, *wg, *wu, *wd, *gw;
    char* ws; u16* out;
};
struct Ctx { lptr lds; char* lds_; unsigned lds0; int tid, lane, wid, bid; unsigned tag; unsigned* sy; };
typedef __attribute__((address_space(4))) const Params* kparams_t;
DI Params load_params() {
    kparams_t pp = (kparams_t)__builtin_amdgcn_kernarg_segment_ptr();
    asm volatile("" : "+s"(pp));
    Params P;
    P.ax = pp->ax; P.ckv = pp->ckv; P.wq = pp->wq; P.wuk = pp->wuk; P.wuv = pp->wuv; P.wo = pp->wo; P.wr = pp->wr; P.wg = pp->wg; P.wu = pp->wu;
    P.wd = pp->wd; P.gw = pp->gw; P.ws = pp->ws; P.out = pp->out;
    return P;
}
DI Ctx make_ctx(char* lds_, char* ws, int wid, unsigned tag) {
    Ctx C;
    C.lds = (lptr)lds_; C.lds_ = lds_; C.lds0 = (unsigned)(uintptr_t)C.lds;
    int lane; asm volatile("v_mbcnt_lo_u32_b32 %0, -1, 0\n\tv_mbcnt_hi_u32_b32 %0, -1, %0" : "=v"(lane));
    asm volatile("" : "+s"(wid), "+s"(tag));
    C.lane = lane; C.wid = wid; C.tid = wid * 64 + lane; C.bid = blockIdx.x; C.tag = tag; C.sy = (unsigned*)(ws + WS_SYNC);
    return C;
}

constexpr int GU_K = 5, GU_KS = 8;
constexpr int QST = 49152;
struct QPre { unsigned voffX[4], voffW[2]; u32x4 rv, wgv, wuv; };
constexpr int MOE_WD = 147456 + 2048;
DI int moe_f0(int bid) { return ((bid & 7) * 28 + ((bid >> 3) < 28 ? (bid >> 3) : 27)) * MOE_COLS; }
DI void q_dma(const QPre& Q, const Params& P, const Ctx& C, int s, int j) {
    const int wid = C.wid, h = (C.bid & 7) * 4 + ((C.bid >> 3) & 3), bi = C.bid >> 5;
    const char* xbase = (const char*)P.ax + (size_t)(((wid >> 2) ? 15 - bi : bi) * 128 + (wid & 3) * 32) * 1024;
    const char* wbase = (const char*)P.wq + h * 256;
    const unsigned b = C.lds0 + ((s + 2) % 3) * QST;
    if (j < 2) glds16(wbase + (size_t)s * 64 * 8192, Q.voffW[j], b + 32768 + (2 * wid + j) * 1024);
    else glds16(xbase + s * 128, Q.voffX[j - 2], b + (4 * wid + j - 2) * 1024);
}
DI void phase_kv(const Params& P, Ctx& C, QPre& Q, unsigned tagv) {
    char* ws = P.ws;
    const int lane = C.lane, wid = C.wid, bid = C.bid, h = (bid & 7) * 4 + ((bid >> 3) & 3), p = bid >> 5;
    const int tok = bid * 8 + wid;
    u32x4 bxv, wrv, gwv;
    ld_x4(bxv, (const char*)P.ax + (size_t)tok * DM * 2, lane * 16); ld_x4(wrv, P.wr, lane * 16); ld_x4(gwv, P.gw, lane * 16);
    auto pre = [&]() {
        unsigned t, xb_[4], wr_[4], gw_[4];
        asm volatile("s_waitcnt vmcnt(18)\n\tv_readfirstlane_b32 %0, %13\n\t"
                     "v_mov_b32 %1, %14\n\tv_mov_b32 %2, %15\n\tv_mov_b32 %3, %16\n\tv_mov_b32 %4, %17\n\t"
                     "v_mov_b32 %5, %18\n\tv_mov_b32 %6, %19\n\tv_mov_b32 %7, %20\n\tv_mov_b32 %8, %21\n\t"
                     "v_mov_b32 %9, %22\n\tv_mov_b32 %10, %23\n\tv_mov_b32 %11, %24\n\tv_mov_b32 %12, %25"
                     : "=&s"(t), "=&v"(xb_[0]), "=&v"(xb_[1]), "=&v"(xb_[2]), "=&v"(xb_[3]), "=&v"(wr_[0]), "=&v"(wr_[1]), "=&v"(wr_[2]), "=&v"(wr_[3]),
                       "=&v"(gw_[0]), "=&v"(gw_[1]), "=&v"(gw_[2]), "=&v"(gw_[3])
                     : "v"(tagv), "v"(bxv[0]), "v"(bxv[1]), "v"(bxv[2]), "v"(bxv[3]), "v"(wrv[0]), "v"(wrv[1]), "v"(wrv[2]), "v"(wrv[3]),
                       "v"(gwv[0]), "v"(gwv[1]), "v"(gwv[2]), "v"(gwv[3]));
        C.tag = t + 1u;
        float sr = 0.f, sg = 0.f;
#pragma unroll
        for (int j = 0; j < 4; ++j) {
            const float x0 = lo16(xb_[j]), x1 = hi16(xb_[j]);
            sr += x0 * lo16(wr_[j]); sr += x1 * hi16(wr_[j]); sg += x0 * lo16(gw_[j]); sg += x1 * hi16(gw_[j]);
        }
        sr = wave_sum(sr, lane); sg = wave_sum(sg, lane);
        if (lane == 0) { store_f32_wt((float*)(ws + WS_R) + tok, bfround(sr)); store_f32_wt((float*)(ws + WS_G) + tok, sigmoidf_(sg)); }
    };
    auto mid = [&]() { if (C.tid == 0) flag_store(C.sy + SY_R + bid, C.tag); };
    const char* cbase = (const char*)P.ckv + (size_t)p * 256 * 1024;
    const char* kbase = (const char*)P.wuk + h * 256;
    const char* vbase = (const char*)P.wuv + h * 256;
    const unsigned outoff = (unsigned)((h * 64 + 8 * p) * 8 + 2 * (wid & 3)) * 1024;
    const unsigned kvbytes = (unsigned)((size_t)SEQ * NHD * 2);
    unsigned rp[4];
    auto tail = [&](auto KI) {
        constexpr int k = 4 + KI / 32, ks = KI % 32;
        if constexpr (k == 4 && ks == 20) {
            const int ln = lane_now();
#pragma unroll
            for (int j = 0; j < 4; ++j) { const int r = 8 * j + (ln >> 3); Q.voffX[j] = r * 1024 + (((ln & 7) ^ ((r >> 1) & 7)) << 4); }
#pragma unroll
            for (int j = 0; j < 2; ++j) { const int row = 4 * (2 * wid + j) + (ln >> 4); Q.voffW[j] = row * 8192 + (((ln & 15) ^ ((row & 3) << 2)) << 4); }
            if (wid == 0) {
#pragma unroll
                for (int j = 0; j < 4; ++j) asm volatile("global_load_dword %0, %1, %2 sc1" : "=v"(rp[j]) : "v"(ln * 4), "s"(C.sy + SY_R + 64 * j));
            }
        }
        if constexpr (k == GU_K && ks == GU_KS) ld_x4(Q.wgv, (const char*)P.wg + moe_f0(bid) * 2, (wid * 64 + lane_now()) * (FF * 2));
        if constexpr (k == GU_K && ks == GU_KS + 1) ld_x4(Q.wuv, (const char*)P.wu + moe_f0(bid) * 2, (wid * 64 + lane_now()) * (FF * 2));
        if constexpr (k == 6 && ks == 8) glds16((const char*)P.wd + (size_t)(moe_f0(bid) + wid) * 1024, lane_now() * 16, C.lds0 + MOE_WD + wid * 1024);
        if constexpr (k == 5 && ks >= 28) q_dma(Q, P, C, 0, ks - 28);
        if constexpr (k == 6 && ks < 2) q_dma(Q, P, C, 0, 4 + ks);
        if constexpr (k == 6 && ks == 20) {
            if (wid == 0) {
                asm volatile("s_waitcnt vmcnt(13)" : "+v"(rp[0]), "+v"(rp[1]), "+v"(rp[2]), "+v"(rp[3]));
                if (__builtin_expect(!__all(rp[0] == C.tag && rp[1] == C.tag && rp[2] == C.tag && rp[3] == C.tag), 0)) wait_flags<4>(C.sy + SY_R, 256, C.tag, lane_now());
            }
        }
        if constexpr (k == 6 && ks >= 28) q_dma(Q, P, C, 1, ks - 28);
        if constexpr (k == 7 && ks < 2) q_dma(Q, P, C, 1, 4 + ks);
        if constexpr (k == 7 && ks == 5) asm volatile("global_load_dwordx4 %0, %1, %2 sc1" : "=v"(Q.rv) : "v"((wid * 64 + lane_now()) * 16), "s"(P.ws + WS_R));
    };
    if (wid < 4) kv_body<false>(cbase, kbase, vbase, make_rsrc(ws + WS_KF, kvbytes), outoff, C.lds, C.lds0, wid, lane, pre, mid, tail);
    else         kv_body<true>(cbase, kbase, vbase, make_rsrc(ws + WS_VF, kvbytes), outoff, C.lds, C.lds0, wid, lane, pre, mid, tail);
}

DI unsigned umax_(unsigned a, unsigned b) { return a > b ? a : b; }
DI unsigned umin_(unsigned a, unsigned b) { return a < b ? a : b; }

constexpr int K2_QB = 131072;
constexpr float QSCALE = 0.08838834764831845f * 1.4426950408889634f;
constexpr float THR = 8.0f;
constexpr int RING0 = 3;

#define SETPRIO(x) __builtin_amdgcn_s_setprio(x)
struct SMState { float mhat, l; f32x16 negm; };
#define PIN(x) asm volatile("" : "+v"(x))
DI float max3(float a, float b, float c) { return fmaxf(fmaxf(a, b), c); }

#define EXP2(x) __builtin_amdgcn_exp2f(x)
#define VOFF(i) (((i) >> 3) * 8192 + (2 * ((i) & 3) + (((i) >> 2) & 1)) * 1024)
#define KOFF(i) (((i) & 1) * 8192 + ((i) >> 1) * 1024)
DI float fadd_s(float a, float b) { float r; asm("v_add_f32_e32 %0, %1, %2" : "=v"(r) : "v"(a), "v"(b)); return r; }
DI float max3_s(float a, float b, float c) { float r; asm("v_max3_f32 %0, %1, %2, %3" : "=v"(r) : "v"(a), "v"(b), "v"(c)); return r; }
DI float max2_s(float a, float b) { float r; asm("v_max_f32_e32 %0, %1, %2" : "=v"(r) : "v"(a), "v"(b)); return r; }
template <int OFF> DI void glds_step(const void* sbase, unsigned voff, unsigned lds_addr) {
    if (OFF == 0) asm volatile("s_mov_b32 m0, %2\n\ts_nop 0\n\tglobal_load_lds_dwordx4 %0, %1 sc1" :: "v"(voff), "s"(sbase), "s"(lds_addr));
    else asm volatile("global_load_lds_dwordx4 %0, %1 offset:%c2 sc1" :: "v"(voff), "s"(sbase), "i"(OFF));
}
DI f32x16 mfma_c(bf16x8 a, bf16x8 b, const f32x16& c) {
    f32x16 d; asm volatile("v_mfma_f32_32x32x16_bf16 %0, %1, %2, %3" : "=&v"(d) : "v"(a), "v"(b), "v"(c)); return d;
}
struct DmaJob { const char* src0; const char* src1; unsigned dst0, dst1; bool has0, has1; };

constexpr int LA = 4;
template <bool QK, bool FIN, class Mid>
DI void attn_A(lptr kp, const lptr& vpn, const bf16x8 (&qf)[8], const f32x16& negm, const bf16x8 (&kpre)[LA], bf16x8 (&vpre)[LA], f32x16& s0, f32x16& s1,
               const f32x16& p0, const f32x16& p1, bf16x8 (&pf)[4], float& l, const DmaJob& dj, unsigned vlin, Mid&& mid) {
    bf16x8 fr[16];
    unsigned pw[16];
    float sa = 0.f, sb = 0.f;
#pragma unroll
    for (int i = 0; i < LA; ++i) fr[i] = kpre[i];
#pragma unroll
    for (int i = 0; i < 16; ++i) {
        if (QK && i + LA < 16) fr[i + LA] = ldsr128(kp + KOFF(i + LA));
        if (i + LA >= 16) vpre[i + LA - 16] = ldsr128(vpn + VOFF(i + LA - 16));
        if (QK) {
            if (i == 0) s0 = mfma_c(fr[0], qf[0], negm);
            else if (i == 1) s1 = mfma_c(fr[1], qf[0], negm);
            else if (i & 1) s1 = MFMA(fr[i], qf[i >> 1], s1);
            else s0 = MFMA(fr[i], qf[i >> 1], s0);
        }
        if (FIN) {
            const float e0 = i < 8 ? p0[2 * i] : p1[2 * i - 16], e1 = i < 8 ? p0[2 * i + 1] : p1[2 * i - 15];
            if (i == 0) { sa = e0; sb = e1; } else { sa = fadd_s(sa, e0); sb = fadd_s(sb, e1); PIN(sa); PIN(sb); }
            pw[i] = cvtpk(e0, e1);
            PIN(pw[i]);
        }
        if (i == 3) mid();
        if (QK) {
            if (i == 1 && dj.has0) { glds_step<0>(dj.src0, vlin, dj.dst0); glds_step<1024>(dj.src0, vlin, dj.dst0); }
            if (i == 5 && dj.has0) { glds_step<2048>(dj.src0, vlin, dj.dst0); glds_step<3072>(dj.src0, vlin, dj.dst0); }
            if (i == 9 && dj.has1) { glds_step<0>(dj.src1, vlin, dj.dst1); glds_step<1024>(dj.src1, vlin, dj.dst1); }
            if (i == 13 && dj.has1) { glds_step<2048>(dj.src1, vlin, dj.dst1); glds_step<3072>(dj.src1, vlin, dj.dst1); }
        }
        SBAR();
    }
    if (FIN) {
        l += sa + sb;
#pragma unroll
        for (int i = 0; i < 4; ++i) { u32x4 w = {pw[4 * i], pw[4 * i + 1], pw[4 * i + 2], pw[4 * i + 3]}; pf[i] = __builtin_bit_cast(bf16x8, w); }
    }
}
template <bool PV, bool SM, class Mid>
DI void attn_B(lptr vp, const lptr& kpn, f32x16 (&o)[4], const bf16x8 (&pf)[4], const bf16x8 (&vpre)[LA], bf16x8 (&kpre)[LA],
               f32x16& s0, f32x16& s1, SMState& st, bool first, bool mask, int qrel  , bool& pend, float& fpend, Mid&& mid) {
    bf16x8 fr[16];
    float ma = 0.f, mb = 0.f, mc = 0.f;
#pragma unroll
    for (int i = 0; i < LA; ++i) fr[i] = vpre[i];
    if (SM && __builtin_expect(mask, 0)) {
        const float NEG = -__builtin_inff();
#pragma unroll
        for (int r = 0; r < 16; ++r) { const int c = (r & 3) + 8 * (r >> 3) + 16 * ((r >> 2) & 1);
            if (c > qrel) s0[r] = NEG;
            if (c + 32 > qrel) s1[r] = NEG; }
    }
    SETPRIO(1);
    static_for<16>([&](auto I) {
        constexpr int i = I;
        if (PV && i + LA < 16) fr[i + LA] = ldsr128(vp + VOFF(i + LA));
        if (i + LA >= 16) kpre[i + LA - 16] = ldsr128(kpn + KOFF(i + LA - 16));
        if (PV) o[i & 3] = MFMA(fr[i], pf[i >> 2], o[i & 3]);
        if (SM) {
            if (i == 0) { ma = max3(s0[0], s0[1], s0[2]); mb = max3(s1[0], s1[1], s1[2]); mc = max3(s0[3], s0[4], s1[3]); PIN(ma); PIN(mb); PIN(mc); }
            if (i == 1) { ma = max3_s(ma, s0[5], s0[6]); mb = max3_s(mb, s1[4], s1[5]); mc = max3_s(mc, s0[7], s1[6]); PIN(ma); PIN(mb); PIN(mc); }
            if (i == 2) { ma = max3_s(ma, s0[8], s0[9]); mb = max3_s(mb, s1[7], s1[8]); mc = max3_s(mc, s0[10], s1[9]); PIN(ma); PIN(mb); PIN(mc); }
            if (i == 3) { ma = max3_s(ma, s0[11], s0[12]); mb = max3_s(mb, s1[10], s1[11]); mc = max3_s(mc, s0[13], s1[12]); PIN(ma); PIN(mb); PIN(mc); }
            if (i == 4) { ma = max3_s(ma, s0[14], s0[15]); mb = max3_s(mb, s1[13], s1[14]); mc = max2_s(mc, s1[15]); PIN(ma); PIN(mb); PIN(mc); }
            if (i == 5) {
                float rm = max3_s(ma, mb, mc);
                auto rr = __builtin_amdgcn_permlane32_swap(__float_as_uint(rm), __float_as_uint(rm), false, false);
                rm = max2_s(__uint_as_float(rr[0]), __uint_as_float(rr[1]));
                if (__builtin_expect(first || __any(rm > THR), 0)) {
                    const float dl = first ? rm : fmaxf(rm, 0.f), f = first ? 1.0f : __builtin_amdgcn_exp2f(-dl);
                    st.mhat += dl; st.l *= f;
#pragma unroll
                    for (int r = 0; r < 16; ++r) { s0[r] -= dl; s1[r] -= dl; st.negm[r] = -st.mhat; }
                    pend = !first; fpend = f;
                }
            }
            if (i >= 6) {
                constexpr int e0 = i < 14 ? 3 * (i - 6) : 24 + 4 * (i - 14), e1 = i < 14 ? e0 + 3 : e0 + 4;
#pragma unroll
                for (int e = e0; e < e1; ++e) { if (e < 16) { s0[e] = EXP2(s0[e]); } else { s1[e - 16] = EXP2(s1[e - 16]); } }
                PIN(s0); PIN(s1);
            }
        }
        if (i == 7) mid();
        SBAR();
    });
    SETPRIO(0);
}

struct Routed { int sel0, sel1; float sig0, sig1; };
DI void phase_attn(const Params& P, const Ctx& C, QPre& Q, Routed& R) {
    char* ws = P.ws; char* lds_ = C.lds_;
    const u16 *wo = P.wo, *bx = P.ax;
    const lptr lds = C.lds;
    const unsigned lds0 = C.lds0;
    const int tid = C.tid, lane = C.lane, l31 = lane & 31, hi = lane >> 5, wid = C.wid;
    const int bid = C.bid, h = (bid & 7) * 4 + ((bid >> 3) & 3), bi = bid >> 5;
    const int grp = wid >> 2, rg = wid & 3;
    const int blkA = bi, blkB = 15 - bi;
    LAS unsigned* sk = (LAS unsigned*)((LAS char*)lds_ + 147456);
    LAS float* hs = (LAS float*)((LAS char*)lds_ + 147456 + 128);
    LAS float* red = (LAS float*)((LAS char*)lds_ + 147456 + 256);
    const int cg = moe_f0(bid) / MOE_COLS;
    unsigned k1 = 0, k2 = 0, xs0, xs1;
    u32x4 skv[4];
    float mv[32], ma0, ma1, hsv[16];
    unsigned wdv[8];
    auto top2 = [&](unsigned b1, unsigned b2) { k2 = umax_(umin_(k1, b1), umax_(k2, b2)); k1 = umax_(k1, b1); };
    auto ins = [&](unsigned key) { k2 = umax_(k2, umin_(k1, key)); k1 = umax_(k1, key); };
    auto moe_gap = [&](auto GI) {
        constexpr int G = GI;
        if constexpr (G == 13 || G == 14) {
            if constexpr (G == 13) asm volatile("" : "+v"(Q.rv));
#pragma unroll
            for (int j = 2 * (G - 13); j < 2 * (G - 13) + 2; ++j) {
                const unsigned u = Q.rv[j] >> 16;
                const unsigned o = (u & 0x8000u) ? (~u & 0xFFFFu) : (u | 0x8000u);
                ins((o << 16) | (unsigned)(0xFFFF - (tid * 4 + j)));
            }
            PIN(k1); PIN(k2);
        }
        if constexpr (G == 15) { top2(dppu<0x128>(k1), dppu<0x128>(k2)); PIN(k1); PIN(k2); }
        if constexpr (G == 16) { top2(dppu<0x124>(k1), dppu<0x124>(k2)); PIN(k1); PIN(k2); }
        if constexpr (G == 17) { top2(dppu<0x122>(k1), dppu<0x122>(k2)); PIN(k1); PIN(k2); }
        if constexpr (G == 18) { top2(dppu<0x121>(k1), dppu<0x121>(k2)); PIN(k1); PIN(k2); }
        if constexpr (G == 19) { auto a = __builtin_amdgcn_permlane16_swap(k1, k1, false, false); auto b = __builtin_amdgcn_permlane16_swap(k2, k2, false, false);
            k1 = a[0]; k2 = b[0]; top2(a[1], b[1]); PIN(k1); PIN(k2); }
        if constexpr (G == 20) { auto a = __builtin_amdgcn_permlane32_swap(k1, k1, false, false); auto b = __builtin_amdgcn_permlane32_swap(k2, k2, false, false);
            k1 = a[0]; k2 = b[0]; top2(a[1], b[1]);
            if (lane == 0) { sk[wid * 2] = k1; sk[wid * 2 + 1] = k2; } }
        if constexpr (G == 29) {
#pragma unroll
            for (int j = 0; j < 4; ++j) skv[j] = *(const LAS u32x4*)(sk + 4 * j);
            k1 = 0; k2 = 0;
        }
        if constexpr (G == 30 || G == 31) {
#pragma unroll
            for (int j = 8 * (G - 30); j < 8 * (G - 30) + 8; j += 2) top2(skv[j >> 2][j & 3], skv[j >> 2][(j & 3) + 1]);
            PIN(k1); PIN(k2);
        }
        if constexpr (G == 32) {
            k1 = __builtin_amdgcn_readfirstlane(k1); k2 = __builtin_amdgcn_readfirstlane(k2);
            R.sel0 = 0xFFFF - (int)(k1 & 0xFFFFu); R.sel1 = 0xFFFF - (int)(k2 & 0xFFFFu);
            ld_u16_nop(xs0, (const char*)bx + R.sel0 * 1024, tid * 2);
            ld_u16_nop(xs1, (const char*)bx + R.sel1 * 1024, tid * 2);
        }
        if constexpr (G == 33) {
            const unsigned o0 = k1 >> 16, o1 = k2 >> 16;
            const float r0 = __uint_as_float(((o0 & 0x8000u) ? (o0 & 0x7FFFu) : (~o0 & 0xFFFFu)) << 16);
            const float r1 = __uint_as_float(((o1 & 0x8000u) ? (o1 & 0x7FFFu) : (~o1 & 0xFFFFu)) << 16);
            R.sig0 = __uint_as_float(__builtin_amdgcn_readfirstlane(__float_as_uint(sigmoidf_(r0))));
            R.sig1 = __uint_as_float(__builtin_amdgcn_readfirstlane(__float_as_uint(sigmoidf_(r1))));
        }
        if constexpr (G >= 61 && G <= 64) {
            if constexpr (G == 61) { asm volatile("" : "+v"(xs0), "+v"(xs1), "+v"(Q.wgv), "+v"(Q.wuv)); ma0 = lo16(xs0); ma1 = lo16(xs1); }
            constexpr int c = 2 * (G - 61);
            const float g0 = lo16(Q.wgv[c >> 1]), g1 = hi16(Q.wgv[c >> 1]), u0 = lo16(Q.wuv[c >> 1]), u1 = hi16(Q.wuv[c >> 1]);
            mv[c] = ma0 * g0; mv[c + 1] = ma0 * g1; mv[8 + c] = ma0 * u0; mv[9 + c] = ma0 * u1;
            mv[16 + c] = ma1 * g0; mv[17 + c] = ma1 * g1; mv[24 + c] = ma1 * u0; mv[25 + c] = ma1 * u1;
            PIN(mv[c]); PIN(mv[c + 1]); PIN(mv[8 + c]); PIN(mv[9 + c]); PIN(mv[16 + c]); PIN(mv[17 + c]); PIN(mv[24 + c]); PIN(mv[25 + c]);
        }
        if constexpr (G >= 65 && G <= 68) {
#pragma unroll
            for (int j = 4 * (G - 65); j < 4 * (G - 65) + 4; ++j) { auto r = __builtin_amdgcn_permlane32_swap(__float_as_uint(mv[j]), __float_as_uint(mv[j + 16]), false, false);
                mv[j] = __uint_as_float(r[0]) + __uint_as_float(r[1]); PIN(mv[j]); }
        }
        if constexpr (G == 69 || G == 70) {
#pragma unroll
            for (int j = 4 * (G - 69); j < 4 * (G - 69) + 4; ++j) { auto r = __builtin_amdgcn_permlane16_swap(__float_as_uint(mv[j]), __float_as_uint(mv[j + 8]), false, false);
                mv[j] = __uint_as_float(r[0]) + __uint_as_float(r[1]); PIN(mv[j]); }
        }
        if constexpr (G == 71) { const bool up_ = lane & 8;
#pragma unroll
            for (int j = 0; j < 4; ++j) { const float send = up_ ? mv[j] : mv[j + 4], keep = up_ ? mv[j + 4] : mv[j]; mv[j] = keep + dppf<0x128>(send); PIN(mv[j]); } }
        if constexpr (G == 72) { const bool up_ = lane & 4;
#pragma unroll
            for (int j = 0; j < 2; ++j) { const float send = up_ ? mv[j] : mv[j + 2], keep = up_ ? mv[j + 2] : mv[j]; mv[j] = keep + dppf<0x1B>(dppf<0x141>(send)); PIN(mv[j]); } }
        if constexpr (G == 73) { const bool up_ = lane & 2;
            const float send = up_ ? mv[0] : mv[1], keep = up_ ? mv[1] : mv[0]; mv[0] = keep + dppf<0x4E>(send);
            mv[0] += dppf<0xB1>(mv[0]);
            if (!(lane & 1)) red[wid * 32 + (lane >> 1)] = mv[0];
        }
        if constexpr (G == 77) {
            if (tid < 16) {
                const int c = tid & 7, t = tid >> 3;
                float g = 0.f, u = 0.f;
#pragma unroll
                for (int w = 0; w < 8; ++w) { g += red[w * 32 + 16 * t + c]; u += red[w * 32 + 16 * t + 8 + c]; }
                g = bfround(g); u = bfround(u);
                hs[t * 8 + c] = bfround(bfround(g * sigmoidf_(g)) * u);
            }
        }
        if constexpr (G == 93) {
#pragma unroll
            for (int j = 0; j < 8; ++j) wdv[j] = *(const LAS u16*)((LAS char*)lds_ + MOE_WD + j * 1024 + tid * 2);
#pragma unroll
            for (int j = 0; j < 4; ++j) { const f32x4 t = *(const LAS f32x4*)(hs + 4 * j); hsv[4 * j] = t[0]; hsv[4 * j + 1] = t[1]; hsv[4 * j + 2] = t[2]; hsv[4 * j + 3] = t[3]; }
        }
        if constexpr (G == 95) {
            float y0 = 0.f, y1 = 0.f;
#pragma unroll
            for (int j = 0; j < 8; ++j) { const float w = lo16(wdv[j]); y0 += hsv[j] * w; y1 += hsv[8 + j] * w; }
            float* yp = (float*)(ws + WS_YP) + (size_t)cg * 1024;
            store_f32_wt(yp + tid, y0); store_f32_wt(yp + 512 + tid, y1);
        }
    };

    const char* Kh = ws + WS_KF + (size_t)h * 64 * 8192;
    const unsigned vlin = lane * 16;
    bf16x8 qf[8];
    {
        lptr xA[4];
#pragma unroll
        for (int ks = 0; ks < 4; ++ks) xA[ks] = lds + (32 * wid + l31) * 128 + (((2 * ks + hi) ^ ((l31 >> 1) & 7)) << 4);
        const int tq = (lane & 15) >> 2, tp = lane & 3, tblk = (lane >> 4) & 1;
        lptr wA[4];
#pragma unroll
        for (int c = 0; c < 4; ++c) wA[c] = lds + 32768 + (8 * hi + tq) * 256 + ((c ^ tq) << 6) + (tp >> 1) * 32 + tblk * 16 + (tp & 1) * 8;
        auto dmaS = [&](int s, int j) { q_dma(Q, P, C, s, j); };
        auto rdW = [&](int s, int i) { const int o = ((s + 2) % 3) * QST + (i >> 2) * 4096; return cat(ldstr(wA[i & 3] + o), ldstr(wA[i & 3] + o + 1024)); };
        auto rdX = [&](int s, int ks) { return ldsr128(xA[ks] + ((s + 2) % 3) * QST); };
        f32x16 acc[4] = {};
        bf16x8 wfr[8], xfr[2];
        unsigned kvf = 0;
        WAIT_BAR(14);
        xfr[0] = rdX(0, 0);
#pragma unroll
        for (int i = 0; i < 4; ++i) wfr[i] = rdW(0, i);
        static_for<128>([&](auto SI) {
            {
                constexpr int s = SI / 16, i = SI % 16;
                if (i == 12 && s < 7) {
                    if (s == 0) { WAIT_BAR(8); } else if (s == 1) { WAIT_BAR(6); } else if (s == 2) { WAIT_BAR(8); } else if (s < 6) { WAIT_BAR(6); } else { WAIT_BAR(0); }
                }
                const int nx = i + 4;
                if (nx < 16) { if ((nx & 3) == 0) xfr[(nx >> 2) & 1] = rdX(s, nx >> 2); wfr[nx & 7] = rdW(s, nx); }
                else if (s < 7) { if (nx == 16) xfr[0] = rdX(s + 1, 0); wfr[nx & 7] = rdW(s + 1, nx - 16); }
                acc[i & 3] = MFMA(wfr[i & 7], xfr[(i >> 2) & 1], acc[i & 3]);
                if (i == 12 || i == 14) { if (s + 3 < 8) dmaS(s + 3, (i - 12) / 2); }
                else if (s == 0) { if (i < 6) dmaS(2, i); }
                else if (i < 12 && i % 3 == 0) { if (s + 2 < 8) dmaS(s + 2, 2 + i / 3); }
                if (s == 6 && grp == 0) {
                    if (i == 12) glds16x4_dev(Kh + rg * 4096, vlin, lds0 + RING0 * 16384 + rg * 4096);
                    if (i == 14) glds16x4_dev(Kh + 16384 + rg * 4096, vlin, lds0 + (RING0 + 2) * 16384 + rg * 4096);
                }
                moe_gap(SI);
                if (s == 1 && i == 13 && tid == 0) flag_store(C.sy + SY_KV + h * 8 + bi, C.tag);
                if (s == 4 && i == 3 && wid == 0)
                    asm volatile("global_load_dword %0, %1, %2 sc1" : "=v"(kvf) : "v"((lane & 7) * 4), "s"(C.sy + SY_KV + h * 8));
                if (s == 5 && i == 13 && wid == 0) {
                    asm volatile("" : "+v"(kvf));
                    if (__builtin_expect(!__all(kvf == C.tag), 0)) wait_flags<1>(C.sy + SY_KV + h * 8, 8, C.tag, lane);
                }
                if (s == 6 && i == 13 && tid == 0) flag_store(C.sy + SY_Y + bid, C.tag);
                SBAR();
            }
        });
#pragma unroll
        for (int c = 0; c < 4; ++c) { qf[2 * c] = pack8(acc[c], 0, QSCALE); qf[2 * c + 1] = pack8(acc[c], 1, QSCALE); }
    }
    const int n1 = 2 * bi + 2;
    auto tile_of = [&](int n, int g) { return n < n1 ? n : n1 + 2 * (n - n1) + g; };
    auto jstar = [&](int tn) { return tn <= 2 * n1 ? tn : tn <= 2 * n1 + 2 ? tn + 1 : 2 * tn - 2 * n1 - 2; };
    auto jg1 = [&](int tn) { return tn == 2 * n1 + 1 ? tn : 2 * tn - 2 * n1 - 1; };
    int tb_d0, tb_d1, tb_kv, tb_rel;
    auto make_job = [&](int tn, int& d0, int& d1) {
        const int n = tn >> 1;
        int a_off, b_off; bool a_ok, b_ok;
        const int kb = (int)WS_KF + h * 64 * 8192 + rg * 4096, vb = (int)WS_VF + h * 64 * 8192 + rg * 4096;
        if (tn & 1) { a_ok = n >= 1 && n <= 17; a_off = vb + tile_of(n - 1, 0) * 16384; b_ok = n >= n1 && n <= 16; b_off = kb + tile_of(n, 1) * 16384; }
        else        { a_ok = n <= 16;           a_off = kb + tile_of(n, 0) * 16384;     b_ok = n - 2 >= n1 && n <= 18; b_off = vb + tile_of(n - 2, 1) * 16384; }
        const int da = a_off | ((jstar(tn) + RING0) & 7) | 8, db = b_off | ((jg1(tn) + RING0) & 7) | 8;
        d0 = a_ok ? da : b_ok ? db : 0; d1 = (a_ok && b_ok) ? db : 0;
    };
    {
        const int n = lane < 19 ? lane : 18;
        make_job(2 * n + grp + 3, tb_d0, tb_d1);
        const int ks = ((n < n1 || grp == 0 ? jstar(2 * n) : jg1(2 * n + 1)) + RING0) & 7;
        const int vs = ((n < n1 || grp == 0 ? jstar(2 * n + 3) : jg1(2 * n + 4)) + RING0) & 7;
        tb_kv = ks | vs << 8;
        tb_rel = 128 * ((grp || n >= n1) ? blkB : blkA) + 32 * rg - 64 * tile_of(n, grp);
    }
    auto job_from = [&](int d0, int d1) -> DmaJob {
        DmaJob d;
        d.has0 = d0 & 8; d.has1 = d1 & 8;
        d.src0 = ws + (unsigned)(d0 & ~4095); d.src1 = ws + (unsigned)(d1 & ~4095);
        d.dst0 = lds0 + (d0 & 7) * 16384 + rg * 4096; d.dst1 = lds0 + (d1 & 7) * 16384 + rg * 4096;
        return d;
    };
    auto job = [&](int n) { return job_from(__builtin_amdgcn_readlane(tb_d0, n), __builtin_amdgcn_readlane(tb_d1, n)); };
    if (grp == 1) {
#pragma unroll
        for (int c = 0; c < 8; ++c) *(LAS bf16x8*)((LAS char*)lds_ + K2_QB + rg * 8192 + c * 1024 + lane * 16) = qf[c];
    }
    f32x16 o[4] = {};
    SMState st; st.mhat = 0.f; st.l = 0.f; st.negm = f32x16{};
    const lptr kp0 = lds + lane * 16;
    char* ctx = ws + WS_CTX;
    const rsrc_t ctxr = make_rsrc(ctx, (unsigned)((size_t)SEQ * NHD * 2));
    auto store_ctx = [&](int blk, int d, const f32x16& v, float inv) {
        const unsigned off = (unsigned)(((4 * blk + rg) * 32 + h) * 8 + 2 * d) * 1024 + lane * 16;
        store_wt(ctxr, off, pack8(v, 0, inv));
        store_wt(ctxr, off + 1024, pack8(v, 1, inv));
    };
    auto half_sum = [&](float v) { auto rr = __builtin_amdgcn_permlane32_swap(__float_as_uint(v), __float_as_uint(v), false, false);
                                   return __uint_as_float(rr[0]) + __uint_as_float(rr[1]); };
    auto kslot = [&](int n) { return kp0 + (__builtin_amdgcn_readlane(tb_kv, n) & 7) * 16384; };
    auto vslot = [&](int n) { return kp0 + ((__builtin_amdgcn_readlane(tb_kv, n) >> 8) & 7) * 16384; };
    WAIT_BAR(0);
    bool first = true, pend = false;
    float invA = 0.f, fpend = 1.f;
    f32x16 sA0, sA1, sB0, sB1; bf16x8 pf[4], vpre[LA], kpre[LA];
#pragma unroll
    for (int i = 0; i < LA; ++i) kpre[i] = ldsr128(kslot(0) + KOFF(i));
    if (grp == 1) { WAIT_BAR(0); }
    const int nsw = grp == 0 ? n1 - 1 : 100;
    auto headB = [&](int n) {
        if (__builtin_expect(n == nsw + 1, 0)) { invA = 1.0f / half_sum(st.l); st.mhat = 0.f; st.l = 0.f; first = true; }
    };
    auto tailB = [&](int n) {
        first = false;
        if (__builtin_expect(pend || (unsigned)(n - nsw) < 3u, 0)) {
            if (n == nsw + 2 && tid == 0) flag_store(C.sy + SY_CTX + blkA * 32 + h, C.tag);
            if (pend) {
#pragma unroll
                for (int d = 0; d < 4; ++d)
#pragma unroll
                    for (int r = 0; r < 16; ++r) o[d][r] *= fpend;
                pend = false;
            }
            if (n == nsw + 1) {
#pragma unroll
                for (int d = 0; d < 4; ++d) store_ctx(blkA, d, o[d], invA);
#pragma unroll
                for (int d = 0; d < 4; ++d) o[d] = f32x16{};
            }
            if (n == nsw) {
#pragma unroll
                for (int c = 0; c < 8; ++c) qf[c] = ldsr128(lds + K2_QB + rg * 8192 + c * 1024 + lane * 16);
                st.negm = f32x16{};
            }
        }
    };
#define BARRIER_L() asm volatile("s_waitcnt lgkmcnt(0)\n\ts_barrier" ::: "memory")
    lptr kcur = kslot(0), vcur = kp0;
    DmaJob dj = job(0);
    int rel = 0;
    auto pinjob = [&]() { asm volatile("" : "+s"(dj.src0), "+s"(dj.src1), "+s"(dj.dst0), "+s"(dj.dst1)); };
    auto midA = [&](int n) { vcur = vslot(n > 0 ? n - 1 : 0); rel = __builtin_amdgcn_readlane(tb_rel, n); asm volatile("" : "+v"(vcur), "+s"(rel)); };
    auto midB = [&](int n) { kcur = kslot(n + 1); dj = job(n + 1); asm volatile("" : "+v"(kcur)); pinjob(); };
#define RELARGS first, rel < 63, rel + l31 - 4 * hi, pend, fpend
    attn_A<true, false>(kcur, vcur, qf, st.negm, kpre, vpre, sA0, sA1, sA0, sA1, pf, st.l, dj, vlin, [&]() { midA(0); });
    BARRIER_L();
    attn_B<false, true>(vcur, kcur, o, pf, vpre, kpre, sA0, sA1, st, RELARGS, [&]() { midB(0); });
    tailB(0);
    WAIT_BAR(0);
#define STEP(n, C0, C1, P0, P1) do { \
        attn_A<true, true>(kcur, vcur, qf, st.negm, kpre, vpre, C0, C1, P0, P1, pf, st.l, dj, vlin, [&]() { midA(n); }); \
        BARRIER_L(); \
        headB(n); \
        attn_B<true, true>(vcur, kcur, o, pf, vpre, kpre, C0, C1, st, RELARGS, [&]() { midB(n); }); \
        tailB(n); \
        if ((n) < 16) { WAIT_BAR(0); } else { BARRIER_L(); }     \
    } while (0)
    unsigned pfd;
    for (int n = 1; n < 17; n += 2) {
        STEP(n, sB0, sB1, sA0, sA1);
        STEP(n + 1, sA0, sA1, sB0, sB1);
    }
#undef STEP
    asm volatile("global_load_dword %0, %1, %2" : "=v"(pfd) : "v"(vlin * 8),
                 "s"((const char*)wo + (size_t)(wid < 2 ? bid * 2 + wid : (bid >> 3) * 4 + ((wid - 2) & 3)) * 8192));
    attn_A<false, true>(kcur, vcur, qf, st.negm, kpre, vpre, sB0, sB1, sA0, sA1, pf, st.l, dj, vlin, [&]() { midA(17); });
    BARRIER_L();
    attn_B<true, false>(vcur, kcur, o, pf, vpre, kpre, sB0, sB1, st, false, false, 0, pend, fpend, [&]() {});
    BARRIER_L();
    if (grp == 0) { BARRIER_L(); }
    unsigned cfl = 0;
    const int ort = (bid & 7) * 4 + ((bid >> 3) & 3);
    if (wid == 0) asm volatile("global_load_dword %0, %1, %2 sc1" : "=v"(cfl) : "v"((lane & 31) * 4), "s"(C.sy + SY_CTX + (ort >> 1) * 32));
    BARRIER_L();
    {
        const float lsum = half_sum(st.l);
        LAS char* xch = (LAS char*)lds_;
        LAS char* mine = xch + wid * 8192;
        auto put = [&](int dd, const f32x16& v) {
#pragma unroll
            for (int q4 = 0; q4 < 4; ++q4)
                *(LAS f32x4*)(mine + (dd * 4 + q4) * 1024 + lane * 16) = (f32x4){v[4 * q4], v[4 * q4 + 1], v[4 * q4 + 2], v[4 * q4 + 3]};
        };
        if (grp) { put(0, o[0]); put(1, o[1]); } else { put(0, o[2]); put(1, o[3]); }
        LAS float* ml = (LAS float*)(xch + 65536);
        ml[wid * 128 + lane] = st.mhat; ml[wid * 128 + 64 + lane] = lsum;
        BARRIER_L();
        const int ow = wid ^ 4;
        const float m2 = ml[ow * 128 + lane], l2 = ml[ow * 128 + 64 + lane];
        const float mm = fmaxf(st.mhat, m2);
        const float f1 = __builtin_amdgcn_exp2f(st.mhat - mm), f2 = __builtin_amdgcn_exp2f(m2 - mm);
        const float inv = 1.0f / (lsum * f1 + l2 * f2);
        const float c1 = f1 * inv, c2 = f2 * inv;
        const LAS char* theirs = xch + ow * 8192;
        auto fin = [&](int dd, f32x16 v) {
#pragma unroll
            for (int q4 = 0; q4 < 4; ++q4) {
                const f32x4 t = *(const LAS f32x4*)(theirs + (dd * 4 + q4) * 1024 + lane * 16);
#pragma unroll
                for (int e = 0; e < 4; ++e) v[4 * q4 + e] = v[4 * q4 + e] * c1 + t[e] * c2;
            }
            store_ctx(blkB, 2 * grp + dd, v, 1.0f);
        };
        if (grp) { fin(0, o[2]); fin(1, o[3]); } else { fin(0, o[0]); fin(1, o[1]); }
    }
    asm volatile("s_waitcnt vmcnt(0)" : "+v"(pfd), "+v"(cfl));
    if (wid == 0) {
        const unsigned bal = (unsigned)__ballot(cfl == C.tag);
        unsigned m0 = 0;
#pragma unroll
        for (int g = 0; g < 8; ++g) m0 |= (((bal >> (4 * g)) & 15u) == 15u ? 1u : 0u) << g;
        if (__builtin_popcount(m0) < 2) m0 = 0;
        if (lane == 0) *(LAS unsigned*)((LAS char*)lds_ + 163840 - 16) = m0;
    }
    BARRIER_L();
    if (tid == 0) { flag_store(C.sy + SY_CTX + blkB * 32 + h, C.tag); flag_store(C.sy + SY_CTX + blkA * 32 + h, C.tag); }
}

constexpr int K3_SLOT = 32768, K3_NS = 5;
DI void phase_oproj(const Params& P, const Ctx& C, const Routed& R) {
    char* ws = P.ws; char* lds_ = C.lds_;
    const lptr lds = C.lds;
    const unsigned lds0 = C.lds0;
    const int lane = C.lane, l31 = lane & 31, hi = lane >> 5, wid = C.wid;
    const int bid = C.bid, rt = (bid & 7) * 4 + ((bid >> 3) & 3), ct = bid >> 5;
    unsigned m0 = __builtin_amdgcn_readfirstlane(*(const LAS unsigned*)((LAS char*)lds_ + 163840 - 16));
    if (!m0) {
        if (wid == 0) {
            const unsigned* cf_ = C.sy + SY_CTX + (rt >> 1) * 32;
            const unsigned long long t0 = __builtin_amdgcn_s_memrealtime();
            unsigned m = 0;
            for (;;) {
                const unsigned bal = (unsigned)__ballot(flag_load(cf_ + (lane & 31)) == C.tag);
                m = 0;
#pragma unroll
                for (int g = 0; g < 8; ++g) m |= (((bal >> (4 * g)) & 15u) == 15u ? 1u : 0u) << g;
                if (__builtin_popcount(m) >= 2) break;
                if (__builtin_amdgcn_s_memrealtime() - t0 > 5000000ull) { m = 0xFFu; break; }
                __builtin_amdgcn_s_sleep(2);
            }
            if (lane == 0) *(LAS unsigned*)((LAS char*)lds_ + 163840 - 32) = m;
        }
        asm volatile("s_waitcnt lgkmcnt(0)\n\ts_barrier" ::: "memory");
        m0 = __builtin_amdgcn_readfirstlane(*(const LAS unsigned*)((LAS char*)lds_ + 163840 - 32));
        asm volatile("s_waitcnt lgkmcnt(0)\n\ts_barrier" ::: "memory");
    }
    const int nAg = __builtin_popcount(m0);
    unsigned ord = 0;
    { int u = 0;
#pragma unroll
      for (int g = 0; g < 8; ++g) if ((m0 >> g) & 1) { ord |= (unsigned)g << (4 * u); ++u; }
#pragma unroll
      for (int g = 0; g < 8; ++g) if (!((m0 >> g) & 1)) { ord |= (unsigned)g << (4 * u); ++u; } }
    if (wid >= 4) {
        const int lw = wid - 4;
        const char* csrc = ws + WS_CTX + (size_t)(2 * rt + (lw >> 1)) * 32 * 8192 + (lw & 1) * 4096;
        const char* wsrc = (const char*)P.wo + ct * 128;
        unsigned voffW[4];
#pragma unroll
        for (int j = 0; j < 4; ++j) { const int row = (lw * 4 + j) * 8 + (lane >> 3);
            voffW[j] = row * 1024 + (((lane & 7) ^ (((row >> 1) & 1) << 2)) << 4); }
        const unsigned vlin = lane * 16;
        auto head = [&](int t) { return (int)(4 * ((ord >> (4 * (t >> 2))) & 7) + (t & 3)); };
        auto stageW = [&](int t, int slot) {
            const unsigned b = lds0 + slot * K3_SLOT + lw * 4096;
#pragma unroll
            for (int j = 0; j < 4; ++j) glds16(wsrc + (size_t)head(t) * 131072, voffW[j], b + 16384 + j * 1024);
        };
        auto stageC = [&](int t, int slot) { glds16x4_dev(csrc + (size_t)head(t) * 8192, vlin, lds0 + slot * K3_SLOT + lw * 4096); };
#pragma unroll
        for (int t = 0; t < 4; ++t) { stageC(t, t); stageW(t, t); }
        int slot = 4;
        for (int hh = 0; hh < 28; ++hh) {
            WAIT_BAR(24);
            stageC(hh + 4, slot); stageW(hh + 4, slot);
            slot = slot == K3_NS - 1 ? 0 : slot + 1;
        }
        WAIT_BAR(24); WAIT_BAR(16); WAIT_BAR(8); WAIT_BAR(0);
        return;
    }
    const int qg2 = wid >> 1, cb = wid & 1;
    const int i0 = R.sel0, i1 = R.sel1;
    const bool has0 = (i0 >> 6) == rt, has1 = (i1 >> 6) == rt;
    const int q = rt * 64 + qg2 * 32 + l31;
    const unsigned* cflags = C.sy + SY_CTX + (rt >> 1) * 32;
    const float g = __uint_as_float(flag_load((const unsigned*)(ws + WS_G) + q));
    s16x4 xb[4];
#pragma unroll
    for (int q4 = 0; q4 < 4; ++q4) xb[q4] = *(const s16x4*)(P.ax + (size_t)q * DM + ct * 64 + cb * 32 + 8 * q4 + 4 * hi);
    const int tq = (lane & 15) >> 2, tp = lane & 3, tblk = (lane >> 4) & 1;
    const lptr wA = lds + 16384 + (4 * hi + tq) * 128 + ((cb ^ ((tq >> 1) & 1)) << 6) + tblk * 32 + tp * 8;
    const lptr cA = lds + qg2 * 8192 + lane * 16;
    f32x16 accs[8] = {};
    int bo = 0;
    auto run4 = [&](f32x16& acc) {
#pragma unroll 1
        for (int hh = 0; hh < 4; ++hh) {
            asm volatile("s_waitcnt lgkmcnt(0)\n\ts_barrier" ::: "memory");
            bf16x8 cf[8], wf[8];
#pragma unroll
            for (int f = 0; f < 4; ++f) { wf[f] = cat(ldstr(wA + bo + f * 2048), ldstr(wA + bo + f * 2048 + 1024)); cf[f] = ldsr128(cA + bo + f * 1024); }
            SBAR();
#pragma unroll
            for (int f = 0; f < 8; ++f) {
                acc = MFMA(wf[f], cf[f], acc);
                if (f + 4 < 8) { wf[f + 4] = cat(ldstr(wA + bo + (f + 4) * 2048), ldstr(wA + bo + (f + 4) * 2048 + 1024)); cf[f + 4] = ldsr128(cA + bo + (f + 4) * 1024); }
                SBAR();
            }
            bo = bo == (K3_NS - 1) * K3_SLOT ? 0 : bo + K3_SLOT;
        }
    };
    auto slots = [&](int u0, int u1) {
        for (int u = u0; u < u1; ++u) {
            switch ((ord >> (4 * u)) & 7) {
                case 0: run4(accs[0]); break; case 1: run4(accs[1]); break; case 2: run4(accs[2]); break; case 3: run4(accs[3]); break;
                case 4: run4(accs[4]); break; case 5: run4(accs[5]); break; case 6: run4(accs[6]); break; default: run4(accs[7]); break;
            }
            if (wid == 0 && u == nAg - 2 && nAg < 8) wait_flags<1>(cflags, 32, C.tag, lane);
        }
    };
    unsigned yf[4]; f32x4 yv4[4];
    if (wid == 1) {
#pragma unroll
        for (int j = 0; j < 4; ++j) yf[j] = flag_load(C.sy + SY_Y + lane + 64 * j);
    }
    slots(0, 1);
    if (wid == 1) {
        if (!__all(yf[0] == C.tag && yf[1] == C.tag && yf[2] == C.tag && yf[3] == C.tag)) wait_flags<4>(C.sy + SY_Y, 256, C.tag, lane);
        const rsrc_t ypr = make_rsrc(ws + WS_YP, MOE_WGS * 4096);
#pragma unroll
        for (int j = 0; j < 4; ++j)
            yv4[j] = __builtin_bit_cast(f32x4, __builtin_amdgcn_raw_buffer_load_b128(ypr, (lane + 64 * j) * 4096 + bid * 16, 0, 16));
    }
    slots(1, 3);
    if (wid == 1) {
        f32x4 y = (yv4[0] + yv4[1]) + (yv4[2] + yv4[3]);
#pragma unroll
        for (int e = 0; e < 4; ++e) y[e] = wave_sum(y[e], lane);
        if (lane < 4) store_f32_wt((float*)(ws + WS_Y) + bid * 4 + lane, lane == 0 ? y[0] : lane == 1 ? y[1] : lane == 2 ? y[2] : y[3]);
    }
    slots(3, 6);
    if (wid == 1) {
        asm volatile("s_waitcnt vmcnt(0)" ::: "memory");
        if (lane == 0) flag_store(C.sy + SY_Y2 + bid, C.tag);
    }
    slots(6, 8);
    f32x16 acc = accs[0];
#pragma unroll
    for (int gg = 1; gg < 8; ++gg) acc += accs[gg];
    const bool r0 = (q == i0), r1 = (q == i1);
    f32x4 yrow[4] = {};
    if (has0 || has1) {
        wait_flags<1>(C.sy + SY_Y2 + ct * 16, 16, C.tag, lane);
        wait_flags<1>(C.sy + SY_Y2 + 128 + ct * 16, 16, C.tag, lane);
        const rsrc_t yr = make_rsrc(ws + WS_Y, 4096);
#pragma unroll
        for (int q4 = 0; q4 < 4; ++q4)
            yrow[q4] = __builtin_bit_cast(f32x4, __builtin_amdgcn_raw_buffer_load_b128(yr, ((r1 ? 512 : 0) + ct * 64 + cb * 32 + 8 * q4 + 4 * hi) * 4, 0, 16));
    }
    {
        const float ysc = r0 ? R.sig0 : r1 ? R.sig1 : 0.f;
        u32x2 w2[4];
#pragma unroll
        for (int q4 = 0; q4 < 4; ++q4) {
            float res[4];
#pragma unroll
            for (int e = 0; e < 4; ++e) {
                float b = bf2f((u16)xb[q4][e]);
                if (r0 || r1) b += yrow[q4][e] * ysc;
                res[e] = g * acc[4 * q4 + e] + (1.0f - g) * b;
            }
            w2[q4] = (u32x2){cvtpk(res[0], res[1]), cvtpk(res[2], res[3])};
        }
        const rsrc_t outr = make_rsrc(P.out, SEQ * DM * 2);
#pragma unroll
        for (int k = 0; k < 4; k += 2) {
            auto rx = __builtin_amdgcn_permlane32_swap(w2[k][0], w2[k + 1][0], false, false);
            auto ry = __builtin_amdgcn_permlane32_swap(w2[k][1], w2[k + 1][1], false, false);
            const u32x4 v = {rx[0], ry[0], rx[1], ry[1]};
            __builtin_amdgcn_raw_buffer_store_b128(v, outr, (q * DM + ct * 64 + cb * 32 + 8 * k + 8 * hi) * 2, 0, 16);
        }
    }
}

__global__ __launch_bounds__(512, 2)
void mega_kernel(Params Parg) {
    extern __shared__ __attribute__((aligned(16))) char lds_[];
    const int wid = __builtin_amdgcn_readfirstlane(threadIdx.x >> 6);
    unsigned tag;
    Routed R;
    QPre Q;
    {
        const Params P = load_params();
        unsigned tagv;
        asm volatile("global_load_dword %0, %1, %2 sc1" : "=v"(tagv) : "v"(0), "s"((const unsigned*)(P.ws + WS_SYNC) + SY_EPOCH));
        Ctx C = make_ctx(lds_, P.ws, wid, 0u);
        phase_kv(P, C, Q, tagv);
        tag = __builtin_amdgcn_readfirstlane(C.tag);
    }
    { const Params P = load_params(); phase_attn(P, make_ctx(lds_, P.ws, wid, tag), Q, R); }
    {
        const Params P = load_params();
        const Ctx C = make_ctx(lds_, P.ws, wid, tag);
        phase_oproj(P, C, R);
        if (C.tid == 0) __hip_atomic_fetch_add(C.sy + SY_EPOCH, 1u, __ATOMIC_RELAXED, __HIP_MEMORY_SCOPE_AGENT);
    }
}

extern "C" void kernel_launch(void* const* d_in, const int* in_sizes, int n_in, void* d_out, int out_size, void* d_ws, size_t ws_size,
                              hipStream_t stream) {
    const u16* ax = (const u16*)d_in[0]; const u16* ckv = (const u16*)d_in[1]; const u16* wq = (const u16*)d_in[2];
    const u16* wuk = (const u16*)d_in[3]; const u16* wuv = (const u16*)d_in[4]; const u16* wo = (const u16*)d_in[5];
      const u16* wr = (const u16*)d_in[7]; const u16* wg = (const u16*)d_in[8];
    const u16* wu = (const u16*)d_in[9]; const u16* wd = (const u16*)d_in[10]; const u16* gw = (const u16*)d_in[11];
    char* ws = (char*)d_ws;
    (void)hipFuncSetAttribute((const void*)mega_kernel, hipFuncAttributeMaxDynamicSharedMemorySize, 163840);
    Params P{ax, ckv, wq, wuk, wuv, wo, wr, wg, wu, wd, gw, ws, (u16*)d_out};
    mega_kernel<<<256, 512, 163840, stream>>>(P);
}
```

```cpp
#include <hip/hip_runtime.h>
#include <hip/hip_bf16.h>
#include <stdint.h>
#include <utility>

typedef unsigned short u16;
typedef short bf16x8 __attribute__((ext_vector_type(8)));
typedef short s16x4 __attribute__((ext_vector_type(4)));
typedef short v4i16_t __attribute__((ext_vector_type(4)));
typedef float f32x16 __attribute__((ext_vector_type(16)));
typedef float f32x4 __attribute__((ext_vector_type(4)));
typedef unsigned u32x4 __attribute__((ext_vector_type(4)));
typedef unsigned u32x2 __attribute__((ext_vector_type(2)));
#define LAS __attribute__((address_space(3)))
#define DI __device__ __forceinline__
#define MFMA(a, b, c) __builtin_amdgcn_mfma_f32_32x32x16_bf16((a), (b), (c), 0, 0, 0)
#define MFMA16(a, b, c) __builtin_amdgcn_mfma_f32_16x16x32_bf16((a), (b), (c), 0, 0, 0)
#define SBAR() __builtin_amdgcn_sched_barrier(0)
#define PIN(x) asm volatile("" : "+v"(x))

constexpr int SEQ = 2048, DM = 512, NH = 32, HD = 128, NHD = NH * HD;
constexpr int FF = 1792;
constexpr int MOE_WGS = 224, MOE_COLS = 8;

constexpr size_t WS_KF = 0;
constexpr size_t WS_VF = WS_KF + (size_t)SEQ * NHD * 2;
constexpr size_t WS_CTX = WS_VF + (size_t)SEQ * NHD * 2;
constexpr size_t WS_R = WS_CTX + (size_t)SEQ * NHD * 2;
constexpr size_t WS_G = WS_R + SEQ * 4;
constexpr size_t WS_Y = WS_G + SEQ * 4;
constexpr size_t WS_YP = WS_Y + 4096;
constexpr size_t WS_SYNC = WS_YP + 224 * 4096;
constexpr size_t WS_X = WS_SYNC + 16384;
constexpr int SY_EPOCH = 0, SY_KV = 256, SY_R = 512, SY_Y = 768, SY_Y2 = 1024, SY_CTX = 1280, SY_X = 1792;

template <int... Is, class F> DI void static_for_(std::integer_sequence<int, Is...>, F&& f) { (f(std::integral_constant<int, Is>{}), ...); }
template <int N, class F> DI void static_for(F&& f) { static_for_(std::make_integer_sequence<int, N>{}, f); }
DI unsigned cvtpk(float lo, float hi) {
    typedef float f32x2 __attribute__((ext_vector_type(2)));
    typedef __bf16 bf16x2 __attribute__((ext_vector_type(2)));
    f32x2 v = {lo, hi}; bf16x2 b = __builtin_convertvector(v, bf16x2); return __builtin_bit_cast(unsigned, b);
}
DI float bf2f(u16 v) { return __uint_as_float((unsigned)v << 16); }
DI float bfround(float x) { return __uint_as_float(cvtpk(x, x) << 16); }
DI bf16x8 pack8(const f32x16& x, int s, float sc) {
    u32x4 w = {cvtpk(x[8 * s] * sc, x[8 * s + 1] * sc), cvtpk(x[8 * s + 2] * sc, x[8 * s + 3] * sc),
               cvtpk(x[8 * s + 4] * sc, x[8 * s + 5] * sc), cvtpk(x[8 * s + 6] * sc, x[8 * s + 7] * sc)};
    return __builtin_bit_cast(bf16x8, w);
}
DI void glds16(const void* sbase, unsigned voff, unsigned lds_addr) {
    lds_addr = __builtin_amdgcn_readfirstlane(lds_addr);
    asm volatile("s_mov_b32 m0, %2\n\ts_nop 0\n\tglobal_load_lds_dwordx4 %0, %1" :: "v"(voff), "s"(sbase), "s"(lds_addr) : "memory");
}
DI unsigned flag_load(const unsigned* p) { return __hip_atomic_load(p, __ATOMIC_RELAXED, __HIP_MEMORY_SCOPE_AGENT); }
DI void flag_store(unsigned* p, unsigned v) { __hip_atomic_store(p, v, __ATOMIC_RELAXED, __HIP_MEMORY_SCOPE_AGENT); }
DI void store_f32_wt(float* p, float v) { __hip_atomic_store((unsigned*)p, __float_as_uint(v), __ATOMIC_RELAXED, __HIP_MEMORY_SCOPE_AGENT); }
template <int NW> DI void wait_flags(const unsigned* f, int n, unsigned tag, int lane) {
    const unsigned long long t0 = __builtin_amdgcn_s_memrealtime();
    for (;;) {
        bool ok = true;
#pragma unroll
        for (int j = 0; j < NW; ++j) { const int i = lane + 64 * j; ok &= (i < n ? flag_load(f + i) : tag) == tag; }
        if (__all(ok)) break;
        if (__builtin_amdgcn_s_memrealtime() - t0 > 5000000ull) break;
        __builtin_amdgcn_s_sleep(2);
    }
}
DI void glds16x4_dev(const void* sbase, unsigned voff, unsigned lds_addr) {
    lds_addr = __builtin_amdgcn_readfirstlane(lds_addr);
    asm volatile("s_mov_b32 m0, %2\n\ts_nop 0\n\tglobal_load_lds_dwordx4 %0, %1 sc1\n\tglobal_load_lds_dwordx4 %0, %1 offset:1024 sc1\n\t"
                 "global_load_lds_dwordx4 %0, %1 offset:2048 sc1\n\tglobal_load_lds_dwordx4 %0, %1 offset:3072 sc1"
                 :: "v"(voff), "s"(sbase), "s"(lds_addr) : "memory");
}
#define WAIT_BAR(N) asm volatile("s_waitcnt vmcnt(" #N ") lgkmcnt(0)\n\ts_barrier" ::: "memory")
DI int lane_now() { int l; asm volatile("v_mbcnt_lo_u32_b32 %0, -1, 0\n\tv_mbcnt_hi_u32_b32 %0, -1, %0" : "=v"(l)); return l; }
typedef LAS const char* lptr;
DI bf16x8 ldsr128(lptr p) { return *(const LAS bf16x8*)p; }
DI s16x4 ldstr(lptr p) { return __builtin_bit_cast(s16x4, __builtin_amdgcn_ds_read_tr16_b64_v4i16((LAS v4i16_t*)p)); }
DI bf16x8 cat(s16x4 a, s16x4 b) { return __builtin_shufflevector(a, b, 0, 1, 2, 3, 4, 5, 6, 7); }
DI unsigned shx(unsigned v, int m, int lane) { return (unsigned)__builtin_amdgcn_ds_bpermute((lane ^ m) << 2, (int)v); }
DI float shx(float v, int m, int lane) { return __uint_as_float(shx(__float_as_uint(v), m, lane)); }
template <int CTRL> DI unsigned dppu(unsigned v) { return (unsigned)__builtin_amdgcn_update_dpp(0, (int)v, CTRL, 0xF, 0xF, true); }
template <int CTRL> DI float dppf(float v) { return __uint_as_float(dppu<CTRL>(__float_as_uint(v))); }
DI float wave_sum(float v, int lane) {
#pragma unroll
    for (int o = 32; o >= 1; o >>= 1) v += shx(v, o, lane);
    return v;
}
DI float sigmoidf_(float x) { return 1.0f / (1.0f + __expf(-x)); }

constexpr int K1_STAGE = 49152;
typedef __amdgpu_buffer_rsrc_t rsrc_t;
DI rsrc_t make_rsrc(const void* p, unsigned bytes) { return __builtin_amdgcn_make_buffer_rsrc((void*)p, (short)0, (int)bytes, 0x00020000); }
DI void store_wt(rsrc_t r, unsigned off, bf16x8 v) {
    __builtin_amdgcn_raw_buffer_store_b128(__builtin_bit_cast(u32x4, v), r, (int)off, 0, 16);
}

template <bool ISV, class Pre, class Mid, class Tail>
DI void kv_body(const char* cbase, const char* kbase, const char* vbase, rsrc_t outr, unsigned outoff, lptr lds, unsigned lds0, int wid, int lane,
                Pre&& pre, Mid&& mid, Tail&& tail) {
    const int r16 = lane & 15, q4 = lane >> 4;
    unsigned voffW[2], voffC[2], voffG[2];
#pragma unroll
    for (int j = 0; j < 2; ++j) {
        const int n = 2 * wid + j;
        { const int row = 4 * n + (lane >> 4); voffW[j] = row * 8192 + (((lane & 15) ^ ((row & 3) << 2) ^ (((row >> 3) & 1) << 1)) << 4); }
        { const int row = 8 * n + (lane >> 3); voffC[j] = row * 1024 + (((lane & 7) ^ ((row >> 1) & 7)) << 4); }
        voffG[j] = (lane ^ ((wid + 8 * j) & 15)) << 4;
    }
    auto stage = [&](int s) {
        const unsigned b = lds0 + (s % 3) * K1_STAGE + 2 * wid * 1024;
#pragma unroll
        for (int j = 0; j < 2; ++j) glds16(kbase + (size_t)s * 64 * 8192, voffW[j], b + j * 1024);
#pragma unroll
        for (int j = 0; j < 2; ++j) glds16(vbase + (size_t)s * 64 * 8192, voffW[j], b + 16384 + j * 1024);
#pragma unroll
        for (int j = 0; j < 2; ++j) glds16(cbase + s * 128, voffC[j], b + 32768 + j * 1024);
    };
    auto gbuf = [&](int k) { return k == 4 ? 98304 : k == 5 ? 131072 : k == 6 ? 0 : 49152; };
    const int tq = (lane & 15) >> 2, tp = lane & 3;
    lptr wA[2], cA[2];
#pragma unroll
    for (int b = 0; b < 2; ++b) {
        wA[b] = lds + (ISV ? 16384 : 0) + (8 * q4 + tq) * 256 + (((wid & 3) ^ tq) << 6) + ((b ^ (q4 & 1)) << 5) + tp * 8;
        cA[b] = lds + 32768 + r16 * 128 + (((4 * b + q4) ^ (r16 >> 1)) << 4);
    }
    auto mm = [&](const bf16x8& w, const bf16x8& c, const f32x4& acc) { return ISV ? MFMA16(c, w, acc) : MFMA16(w, c, acc); };
    const unsigned olane = outoff + (q4 >> 1) * 1024 + (ISV ? (r16 + 32 * (q4 & 1)) * 16 : (16 * (r16 >> 3) + (r16 & 7) + 32 * (q4 & 1)) * 16);
    auto out_tile = [&](int kg, const f32x4& t00, const f32x4& t01, const f32x4& t10, const f32x4& t11) {
        auto pk = [](const f32x4& x, const f32x4& y) { u32x4 w = {cvtpk(x[0], x[1]), cvtpk(x[2], x[3]), cvtpk(y[0], y[1]), cvtpk(y[2], y[3])}; return __builtin_bit_cast(bf16x8, w); };
        if (ISV) { store_wt(outr, olane + kg * 8192, pk(t00, t10)); store_wt(outr, olane + kg * 8192 + 256, pk(t01, t11)); }
        else     { store_wt(outr, olane + kg * 8192, pk(t00, t01)); store_wt(outr, olane + kg * 8192 + 128, pk(t10, t11)); }
    };

    bf16x8 wf[16][2];
    f32x4 acc[8][2] = {};
    lptr gA[4];
    stage(0); stage(1); stage(2);
    auto rdW = [&](int s, int sl) { const int o = (s % 3) * K1_STAGE + sl * 8192;
#pragma unroll
        for (int b = 0; b < 2; ++b) wf[2 * s + sl][b] = cat(ldstr(wA[b] + o), ldstr(wA[b] + o + 1024)); };
    auto rdC = [&](int s, int i) { return ldsr128(cA[i >> 3] + (s % 3) * K1_STAGE + (i & 7) * 2048); };
    auto rdG = [&](int k, int ks) { return ldsr128(gA[(ks >> 1) & 3] + gbuf(k) + (ks & 1) * 16384 + (ks >> 3) * 256); };
    auto dmaS = [&](int s, int j) {
        const unsigned b = lds0 + (s % 3) * K1_STAGE + 2 * wid * 1024;
        if (j < 2) glds16(kbase + (size_t)s * 64 * 8192, voffW[j], b + j * 1024);
        else if (j < 4) glds16(vbase + (size_t)s * 64 * 8192, voffW[j - 2], b + 16384 + (j - 2) * 1024);
        else glds16(cbase + s * 128, voffC[j - 4], b + 32768 + (j - 4) * 1024);
    };
    auto dmaG = [&](int k, int j) { glds16(cbase + (size_t)(32 * k + wid + 8 * j) * 1024, voffG[j & 1], lds0 + gbuf(k) + (wid + 8 * j) * 1024); };
    bf16x8 cf[8];
    WAIT_BAR(12);
    rdW(0, 0);
#pragma unroll
    for (int i = 0; i < 4; ++i) cf[i] = rdC(0, i);
    static_for<128>([&](auto SI) {
        {
            constexpr int s = SI / 16, i = SI % 16;
            if (i == 12) {
                if (s < 6) { WAIT_BAR(6); } else if (s == 6) { WAIT_BAR(8); } else { WAIT_BAR(8); }
            }
            if (s == 7 && i == 11) {
                const int ln = lane_now();
#pragma unroll
                for (int j = 0; j < 4; ++j) gA[j] = lds + (ln & 15) * 1024 + (((4 * j + (ln >> 4)) ^ (ln & 15)) << 4);
            }
            if constexpr (s == 0 && i < 12) pre(SI);
            if (s == 1 && i == 13) mid();
            constexpr int nx = i + 4;
            if constexpr (nx < 16) { if (nx == 8) rdW(s, 1); cf[nx & 7] = rdC(s, nx); }
            else if constexpr (s < 7) { if (nx == 16) rdW(s + 1, 0); cf[nx & 7] = rdC(s + 1, nx - 16); }
            else cf[nx & 7] = rdG(4, nx - 16);
            acc[i & 7][0] = mm(wf[2 * s + (i >> 3)][0], cf[i & 7], acc[i & 7][0]);
            acc[i & 7][1] = mm(wf[2 * s + (i >> 3)][1], cf[i & 7], acc[i & 7][1]);
            if (s + 3 < 8) { if (i == 12 || i == 14) dmaS(s + 3, (i - 12) / 2); }
            else if (i >= 12) {
                const int j = i - 12;
                if (s == 5) dmaG(4, j);
                else if (s == 6) dmaG(6, j);
                else dmaG(7, j);
            }
            if (i < 12 && i % 3 == 0) { if (s >= 1 && s + 2 < 8) dmaS(s + 2, 2 + i / 3); }
            if (s == 6 && i >= 2 && i < 10 && !(i & 1)) dmaG(5, (i - 2) / 2);
            if (s == 7 && i == 15) out_tile(0, acc[0][0], acc[0][1], acc[1][0], acc[1][1]);
            SBAR();
        }
    });
    f32x4 a2[2][2][2];
    static_for<128>([&](auto KI) {
        {
            constexpr int k = 4 + KI / 32, ks = KI % 32;
            if (ks == 28 && k < 7) {
                if (k == 4) { WAIT_BAR(16); } else if (k == 5) { WAIT_BAR(16); } else { WAIT_BAR(21); }
            }
            constexpr int nx = ks + 4;
            if constexpr (nx < 32) cf[nx & 7] = rdG(k, nx); else if constexpr (k < 7) cf[nx & 7] = rdG(k + 1, nx - 32);
#pragma unroll
            for (int b = 0; b < 2; ++b) a2[k & 1][ks & 1][b] = mm(wf[ks >> 1][b], cf[ks & 7], ks < 2 ? f32x4{} : a2[k & 1][ks & 1][b]);
            if constexpr (k == 4) { if constexpr (ks < 3) out_tile(ks + 1, acc[2 * ks + 2][0], acc[2 * ks + 2][1], acc[2 * ks + 3][0], acc[2 * ks + 3][1]); }
            else if constexpr (ks == 3) out_tile(k - 1, a2[(k - 1) & 1][0][0], a2[(k - 1) & 1][0][1], a2[(k - 1) & 1][1][0], a2[(k - 1) & 1][1][1]);
            tail(KI);
            SBAR();
        }
    });
    out_tile(7, a2[1][0][0], a2[1][0][1], a2[1][1][0], a2[1][1][1]);
}

DI void ld_x4(u32x4& d, const void* sbase, unsigned voff) { asm volatile("global_load_dwordx4 %0, %1, %2" : "=v"(d) : "v"(voff), "s"(sbase)); }
DI void ld_u16_nop(unsigned& d, const void* sbase, unsigned voff) { asm volatile("s_nop 4\n\tglobal_load_ushort %0, %1, %2" : "=v"(d) : "v"(voff), "s"(sbase)); }
DI float lo16(unsigned v) { return __uint_as_float(v << 16); }
DI float hi16(unsigned v) { return __uint_as_float(v & 0xFFFF0000u); }
struct Params {
    const u16 *ax, *ckv, *wq, *wuk, *wuv, *wo, *wr, *wg, *wu, *wd, *gw;
    char* ws; u16* out;
};
struct Ctx { lptr lds; char* lds_; unsigned lds0; int tid, lane, wid, bid; unsigned tag; unsigned* sy; };
typedef __attribute__((address_space(4))) const Params* kparams_t;
DI Params load_params() {
    kparams_t pp = (kparams_t)__builtin_amdgcn_kernarg_segment_ptr();
    asm volatile("" : "+s"(pp));
    Params P;
    P.ax = pp->ax; P.ckv = pp->ckv; P.wq = pp->wq; P.wuk = pp->wuk; P.wuv = pp->wuv; P.wo = pp->wo; P.wr = pp->wr; P.wg = pp->wg; P.wu = pp->wu;
    P.wd = pp->wd; P.gw = pp->gw; P.ws = pp->ws; P.out = pp->out;
    return P;
}
DI Ctx make_ctx(char* lds_, char* ws, int wid, unsigned tag) {
    Ctx C;
    C.lds = (lptr)lds_; C.lds_ = lds_; C.lds0 = (unsigned)(uintptr_t)C.lds;
    int lane; asm volatile("v_mbcnt_lo_u32_b32 %0, -1, 0\n\tv_mbcnt_hi_u32_b32 %0, -1, %0" : "=v"(lane));
    asm volatile("" : "+s"(wid), "+s"(tag));
    C.lane = lane; C.wid = wid; C.tid = wid * 64 + lane; C.bid = blockIdx.x; C.tag = tag; C.sy = (unsigned*)(ws + WS_SYNC);
    return C;
}

constexpr int GU_K = 5, GU_KS = 8;
constexpr int QST = 49152;
struct QPre { unsigned voffX[4], voffW[2]; u32x4 rv, wgv, wuv; };
constexpr int MOE_WD = 147456 + 2048;
DI int moe_f0(int bid) { return ((bid & 7) * 28 + ((bid >> 3) < 28 ? (bid >> 3) : 27)) * MOE_COLS; }
DI void q_dma(const QPre& Q, const Params& P, const Ctx& C, int s, int j) {
    const int wid = C.wid, h = (C.bid & 7) * 4 + ((C.bid >> 3) & 3), bi = C.bid >> 5;
    const char* xbase = (const char*)P.ax + (size_t)(((wid >> 2) ? 15 - bi : bi) * 128 + (wid & 3) * 32) * 1024;
    const char* wbase = (const char*)P.wq + h * 256;
    const unsigned b = C.lds0 + ((s + 2) % 3) * QST;
    if (j < 2) glds16(wbase + (size_t)s * 64 * 8192, Q.voffW[j], b + 32768 + (2 * wid + j) * 1024);
    else glds16(xbase + s * 128, Q.voffX[j - 2], b + (4 * wid + j - 2) * 1024);
}
DI void phase_kv(const Params& P, Ctx& C, QPre& Q, unsigned tagv) {
    char* ws = P.ws;
    const int lane = C.lane, wid = C.wid, bid = C.bid, h = (bid & 7) * 4 + ((bid >> 3) & 3), p = bid >> 5;
    const int tok = bid * 8 + wid;
    u32x4 bxv, wrv, gwv;
    ld_x4(bxv, (const char*)P.ax + (size_t)tok * DM * 2, lane * 16); ld_x4(wrv, P.wr, lane * 16); ld_x4(gwv, P.gw, lane * 16);
    unsigned xb_[4], wr_[4], gw_[4];
    float sr = 0.f, sg = 0.f;
    auto pre = [&](auto GI) {
        constexpr int G = GI;
        if constexpr (G == 0) {
            unsigned t;
            asm volatile("v_readfirstlane_b32 %0, %13\n\t"
                         "v_mov_b32 %1, %14\n\tv_mov_b32 %2, %15\n\tv_mov_b32 %3, %16\n\tv_mov_b32 %4, %17\n\t"
                         "v_mov_b32 %5, %18\n\tv_mov_b32 %6, %19\n\tv_mov_b32 %7, %20\n\tv_mov_b32 %8, %21\n\t"
                         "v_mov_b32 %9, %22\n\tv_mov_b32 %10, %23\n\tv_mov_b32 %11, %24\n\tv_mov_b32 %12, %25"
                         : "=&s"(t), "=&v"(xb_[0]), "=&v"(xb_[1]), "=&v"(xb_[2]), "=&v"(xb_[3]), "=&v"(wr_[0]), "=&v"(wr_[1]), "=&v"(wr_[2]), "=&v"(wr_[3]),
                           "=&v"(gw_[0]), "=&v"(gw_[1]), "=&v"(gw_[2]), "=&v"(gw_[3])
                         : "v"(tagv), "v"(bxv[0]), "v"(bxv[1]), "v"(bxv[2]), "v"(bxv[3]), "v"(wrv[0]), "v"(wrv[1]), "v"(wrv[2]), "v"(wrv[3]),
                           "v"(gwv[0]), "v"(gwv[1]), "v"(gwv[2]), "v"(gwv[3]));
            C.tag = t + 1u;
        }
        if constexpr (G >= 1 && G <= 4) {
            constexpr int j = G - 1;
            const float x0 = lo16(xb_[j]), x1 = hi16(xb_[j]);
            sr += x0 * lo16(wr_[j]); sr += x1 * hi16(wr_[j]); sg += x0 * lo16(gw_[j]); sg += x1 * hi16(gw_[j]);
            PIN(sr); PIN(sg);
        }
        if constexpr (G == 5) { sr += dppf<0x128>(sr); sg += dppf<0x128>(sg); sr += dppf<0x124>(sr); sg += dppf<0x124>(sg); PIN(sr); PIN(sg); }
        if constexpr (G == 6) { sr += dppf<0x122>(sr); sg += dppf<0x122>(sg); sr += dppf<0x121>(sr); sg += dppf<0x121>(sg); PIN(sr); PIN(sg); }
        if constexpr (G == 7) {
            auto a = __builtin_amdgcn_permlane16_swap(__float_as_uint(sr), __float_as_uint(sr), false, false); sr = __uint_as_float(a[0]) + __uint_as_float(a[1]);
            auto b = __builtin_amdgcn_permlane16_swap(__float_as_uint(sg), __float_as_uint(sg), false, false); sg = __uint_as_float(b[0]) + __uint_as_float(b[1]);
            PIN(sr); PIN(sg);
        }
        if constexpr (G == 8) {
            auto a = __builtin_amdgcn_permlane32_swap(__float_as_uint(sr), __float_as_uint(sr), false, false); sr = __uint_as_float(a[0]) + __uint_as_float(a[1]);
            auto b = __builtin_amdgcn_permlane32_swap(__float_as_uint(sg), __float_as_uint(sg), false, false); sg = __uint_as_float(b[0]) + __uint_as_float(b[1]);
            PIN(sr); PIN(sg);
        }
        if constexpr (G == 10) { if (lane == 0) { store_f32_wt((float*)(P.ws + WS_R) + tok, bfround(sr)); store_f32_wt((float*)(P.ws + WS_G) + tok, sigmoidf_(sg)); } }
    };
    auto mid = [&]() { if (C.tid == 0) flag_store(C.sy + SY_R + bid, C.tag); };
    const char* cbase = (const char*)P.ckv + (size_t)p * 256 * 1024;
    const char* kbase = (const char*)P.wuk + h * 256;
    const char* vbase = (const char*)P.wuv + h * 256;
    const unsigned outoff = (unsigned)((h * 64 + 8 * p) * 8 + 2 * (wid & 3)) * 1024;
    const unsigned kvbytes = (unsigned)((size_t)SEQ * NHD * 2);
    unsigned rp[4];
    auto tail = [&](auto KI) {
        constexpr int k = 4 + KI / 32, ks = KI % 32;
        if constexpr (k == 4 && ks == 20) {
            const int ln = lane_now();
#pragma unroll
            for (int j = 0; j < 4; ++j) { const int r = 8 * j + (ln >> 3); Q.voffX[j] = r * 1024 + (((ln & 7) ^ ((r >> 1) & 7)) << 4); }
#pragma unroll
            for (int j = 0; j < 2; ++j) { const int row = 4 * (2 * wid + j) + (ln >> 4); Q.voffW[j] = row * 8192 + (((ln & 15) ^ ((row & 3) << 2)) << 4); }
            if (wid == 0) {
#pragma unroll
                for (int j = 0; j < 4; ++j) asm volatile("global_load_dword %0, %1, %2 sc1" : "=v"(rp[j]) : "v"(ln * 4), "s"(C.sy + SY_R + 64 * j));
            }
        }
        if constexpr (k == GU_K && ks == GU_KS) ld_x4(Q.wgv, (const char*)P.wg + moe_f0(bid) * 2, (wid * 64 + lane_now()) * (FF * 2));
        if constexpr (k == GU_K && ks == GU_KS + 1) ld_x4(Q.wuv, (const char*)P.wu + moe_f0(bid) * 2, (wid * 64 + lane_now()) * (FF * 2));
        if constexpr (k == 6 && ks == 8) glds16((const char*)P.wd + (size_t)(moe_f0(bid) + wid) * 1024, lane_now() * 16, C.lds0 + MOE_WD + wid * 1024);
        if constexpr (k == 5 && ks >= 28) q_dma(Q, P, C, 0, ks - 28);
        if constexpr (k == 6 && ks < 2) q_dma(Q, P, C, 0, 4 + ks);
        if constexpr (k == 6 && ks == 20) {
            if (wid == 0) {
                asm volatile("s_waitcnt vmcnt(13)" : "+v"(rp[0]), "+v"(rp[1]), "+v"(rp[2]), "+v"(rp[3]));
                if (__builtin_expect(!__all(rp[0] == C.tag && rp[1] == C.tag && rp[2] == C.tag && rp[3] == C.tag), 0)) wait_flags<4>(C.sy + SY_R, 256, C.tag, lane_now());
            }
        }
        if constexpr (k == 6 && ks >= 28) q_dma(Q, P, C, 1, ks - 28);
        if constexpr (k == 7 && ks < 2) q_dma(Q, P, C, 1, 4 + ks);
        if constexpr (k == 7 && ks == 5) asm volatile("global_load_dwordx4 %0, %1, %2 sc1" : "=v"(Q.rv) : "v"((wid * 64 + lane_now()) * 16), "s"(P.ws + WS_R));
    };
    if (wid < 4) kv_body<false>(cbase, kbase, vbase, make_rsrc(ws + WS_KF, kvbytes), outoff, C.lds, C.lds0, wid, lane, pre, mid, tail);
    else         kv_body<true>(cbase, kbase, vbase, make_rsrc(ws + WS_VF, kvbytes), outoff, C.lds, C.lds0, wid, lane, pre, mid, tail);
}

DI unsigned umax_(unsigned a, unsigned b) { return a > b ? a : b; }
DI unsigned umin_(unsigned a, unsigned b) { return a < b ? a : b; }

constexpr int K2_QB = 131072;
constexpr float QSCALE = 0.08838834764831845f * 1.4426950408889634f;
constexpr float THR = 8.0f;
constexpr int RING0 = 3;

#define SETPRIO(x) __builtin_amdgcn_s_setprio(x)
struct SMState { float mhat, l; f32x16 negm; };
DI float max3(float a, float b, float c) { return fmaxf(fmaxf(a, b), c); }

#define EXP2(x) __builtin_amdgcn_exp2f(x)
#define VOFF(i) (((i) >> 3) * 8192 + (2 * ((i) & 3) + (((i) >> 2) & 1)) * 1024)
#define KOFF(i) (((i) & 1) * 8192 + ((i) >> 1) * 1024)
DI float fadd_s(float a, float b) { float r; asm("v_add_f32_e32 %0, %1, %2" : "=v"(r) : "v"(a), "v"(b)); return r; }
DI float max3_s(float a, float b, float c) { float r; asm("v_max3_f32 %0, %1, %2, %3" : "=v"(r) : "v"(a), "v"(b), "v"(c)); return r; }
DI float max2_s(float a, float b) { float r; asm("v_max_f32_e32 %0, %1, %2" : "=v"(r) : "v"(a), "v"(b)); return r; }
template <int OFF> DI void glds_step(const void* sbase, unsigned voff, unsigned lds_addr) {
    if (OFF == 0) asm volatile("s_mov_b32 m0, %2\n\ts_nop 0\n\tglobal_load_lds_dwordx4 %0, %1 sc1" :: "v"(voff), "s"(sbase), "s"(lds_addr));
    else asm volatile("global_load_lds_dwordx4 %0, %1 offset:%c2 sc1" :: "v"(voff), "s"(sbase), "i"(OFF));
}
DI f32x16 mfma_c(bf16x8 a, bf16x8 b, const f32x16& c) {
    f32x16 d; asm volatile("v_mfma_f32_32x32x16_bf16 %0, %1, %2, %3" : "=&v"(d) : "v"(a), "v"(b), "v"(c)); return d;
}
struct DmaJob { const char* src0; const char* src1; unsigned dst0, dst1; bool has0, has1; };

constexpr int LA = 4;
template <bool QK, bool FIN, class Mid>
DI void attn_A(lptr kp, const lptr& vpn, const bf16x8 (&qf)[8], const f32x16& negm, const bf16x8 (&kpre)[LA], bf16x8 (&vpre)[LA], f32x16& s0, f32x16& s1,
               const f32x16& p0, const f32x16& p1, bf16x8 (&pf)[4], float& l, const DmaJob& dj, unsigned vlin, Mid&& mid) {
    bf16x8 fr[16];
    unsigned pw[16];
    float sa = 0.f, sb = 0.f;
#pragma unroll
    for (int i = 0; i < LA; ++i) fr[i] = kpre[i];
#pragma unroll
    for (int i = 0; i < 16; ++i) {
        if (QK && i + LA < 16) fr[i + LA] = ldsr128(kp + KOFF(i + LA));
        if (i + LA >= 16) vpre[i + LA - 16] = ldsr128(vpn + VOFF(i + LA - 16));
        if (QK) {
            if (i == 0) s0 = mfma_c(fr[0], qf[0], negm);
            else if (i == 1) s1 = mfma_c(fr[1], qf[0], negm);
            else if (i & 1) s1 = MFMA(fr[i], qf[i >> 1], s1);
            else s0 = MFMA(fr[i], qf[i >> 1], s0);
        }
        if (FIN) {
            const float e0 = i < 8 ? p0[2 * i] : p1[2 * i - 16], e1 = i < 8 ? p0[2 * i + 1] : p1[2 * i - 15];
            if (i == 0) { sa = e0; sb = e1; } else { sa = fadd_s(sa, e0); sb = fadd_s(sb, e1); PIN(sa); PIN(sb); }
            pw[i] = cvtpk(e0, e1);
            PIN(pw[i]);
        }
        if (i == 3) mid();
        if (QK) {
            if (i == 1 && dj.has0) { glds_step<0>(dj.src0, vlin, dj.dst0); glds_step<1024>(dj.src0, vlin, dj.dst0); }
            if (i == 5 && dj.has0) { glds_step<2048>(dj.src0, vlin, dj.dst0); glds_step<3072>(dj.src0, vlin, dj.dst0); }
            if (i == 9 && dj.has1) { glds_step<0>(dj.src1, vlin, dj.dst1); glds_step<1024>(dj.src1, vlin, dj.dst1); }
            if (i == 13 && dj.has1) { glds_step<2048>(dj.src1, vlin, dj.dst1); glds_step<3072>(dj.src1, vlin, dj.dst1); }
        }
        SBAR();
    }
    if (FIN) {
        l += sa + sb;
#pragma unroll
        for (int i = 0; i < 4; ++i) { u32x4 w = {pw[4 * i], pw[4 * i + 1], pw[4 * i + 2], pw[4 * i + 3]}; pf[i] = __builtin_bit_cast(bf16x8, w); }
    }
}
template <bool PV, bool SM, class Mid>
DI void attn_B(lptr vp, const lptr& kpn, f32x16 (&o)[4], const bf16x8 (&pf)[4], const bf16x8 (&vpre)[LA], bf16x8 (&kpre)[LA],
               f32x16& s0, f32x16& s1, SMState& st, bool first, bool mask, int qrel  , bool& pend, float& fpend, Mid&& mid) {
    bf16x8 fr[16];
    float ma = 0.f, mb = 0.f, mc = 0.f;
#pragma unroll
    for (int i = 0; i < LA; ++i) fr[i] = vpre[i];
    if (SM && __builtin_expect(mask, 0)) {
        const float NEG = -__builtin_inff();
#pragma unroll
        for (int r = 0; r < 16; ++r) { const int c = (r & 3) + 8 * (r >> 3) + 16 * ((r >> 2) & 1);
            if (c > qrel) s0[r] = NEG;
            if (c + 32 > qrel) s1[r] = NEG; }
    }
    SETPRIO(1);
    static_for<16>([&](auto I) {
        constexpr int i = I;
        if (PV && i + LA < 16) fr[i + LA] = ldsr128(vp + VOFF(i + LA));
        if (i + LA >= 16) kpre[i + LA - 16] = ldsr128(kpn + KOFF(i + LA - 16));
        if (PV) o[i & 3] = MFMA(fr[i], pf[i >> 2], o[i & 3]);
        if (SM) {
            if (i == 0) { ma = max3(s0[0], s0[1], s0[2]); mb = max3(s1[0], s1[1], s1[2]); mc = max3(s0[3], s0[4], s1[3]); PIN(ma); PIN(mb); PIN(mc); }
            if (i == 1) { ma = max3_s(ma, s0[5], s0[6]); mb = max3_s(mb, s1[4], s1[5]); mc = max3_s(mc, s0[7], s1[6]); PIN(ma); PIN(mb); PIN(mc); }
            if (i == 2) { ma = max3_s(ma, s0[8], s0[9]); mb = max3_s(mb, s1[7], s1[8]); mc = max3_s(mc, s0[10], s1[9]); PIN(ma); PIN(mb); PIN(mc); }
            if (i == 3) { ma = max3_s(ma, s0[11], s0[12]); mb = max3_s(mb, s1[10], s1[11]); mc = max3_s(mc, s0[13], s1[12]); PIN(ma); PIN(mb); PIN(mc); }
            if (i == 4) { ma = max3_s(ma, s0[14], s0[15]); mb = max3_s(mb, s1[13], s1[14]); mc = max2_s(mc, s1[15]); PIN(ma); PIN(mb); PIN(mc); }
            if (i == 5) {
                float rm = max3_s(ma, mb, mc);
                auto rr = __builtin_amdgcn_permlane32_swap(__float_as_uint(rm), __float_as_uint(rm), false, false);
                rm = max2_s(__uint_as_float(rr[0]), __uint_as_float(rr[1]));
                if (__builtin_expect(first || __any(rm > THR), 0)) {
                    const float dl = first ? rm : fmaxf(rm, 0.f), f = first ? 1.0f : __builtin_amdgcn_exp2f(-dl);
                    st.mhat += dl; st.l *= f;
#pragma unroll
                    for (int r = 0; r < 16; ++r) { s0[r] -= dl; s1[r] -= dl; st.negm[r] = -st.mhat; }
                    pend = !first; fpend = f;
                }
            }
            if (i >= 6) {
                constexpr int e0 = i < 14 ? 3 * (i - 6) : 24 + 4 * (i - 14), e1 = i < 14 ? e0 + 3 : e0 + 4;
#pragma unroll
                for (int e = e0; e < e1; ++e) { if (e < 16) { s0[e] = EXP2(s0[e]); } else { s1[e - 16] = EXP2(s1[e - 16]); } }
                PIN(s0); PIN(s1);
            }
        }
        if (i == 7) mid();
        SBAR();
    });
    SETPRIO(0);
}

struct Routed { int sel0, sel1; float sig0, sig1; };
DI void phase_attn(const Params& P, const Ctx& C, QPre& Q, Routed& R) {
    char* ws = P.ws; char* lds_ = C.lds_;
    const u16 *wo = P.wo, *bx = P.ax;
    const lptr lds = C.lds;
    const unsigned lds0 = C.lds0;
    const int tid = C.tid, lane = C.lane, l31 = lane & 31, hi = lane >> 5, wid = C.wid;
    const int bid = C.bid, h = (bid & 7) * 4 + ((bid >> 3) & 3), bi = bid >> 5;
    const int grp = wid >> 2, rg = wid & 3;
    const int blkA = bi, blkB = 15 - bi;
    LAS unsigned* sk = (LAS unsigned*)((LAS char*)lds_ + 147456);
    LAS float* hs = (LAS float*)((LAS char*)lds_ + 147456 + 128);
    LAS float* red = (LAS float*)((LAS char*)lds_ + 147456 + 256);
    const int cg = moe_f0(bid) / MOE_COLS;
    unsigned k1 = 0, k2 = 0, xs0, xs1;
    u32x4 skv[4];
    float mv[32], ma0, ma1, hsv[16];
    unsigned wdv[8];
    auto top2 = [&](unsigned b1, unsigned b2) { k2 = umax_(umin_(k1, b1), umax_(k2, b2)); k1 = umax_(k1, b1); };
    auto ins = [&](unsigned key) { k2 = umax_(k2, umin_(k1, key)); k1 = umax_(k1, key); };
    auto moe_gap = [&](auto GI) {
        constexpr int G = GI;
        if constexpr (G == 13 || G == 14) {
            if constexpr (G == 13) asm volatile("" : "+v"(Q.rv));
#pragma unroll
            for (int j = 2 * (G - 13); j < 2 * (G - 13) + 2; ++j) {
                const unsigned u = Q.rv[j] >> 16;
                const unsigned o = (u & 0x8000u) ? (~u & 0xFFFFu) : (u | 0x8000u);
                ins((o << 16) | (unsigned)(0xFFFF - (tid * 4 + j)));
            }
            PIN(k1); PIN(k2);
        }
        if constexpr (G == 15) { top2(dppu<0x128>(k1), dppu<0x128>(k2)); PIN(k1); PIN(k2); }
        if constexpr (G == 16) { top2(dppu<0x124>(k1), dppu<0x124>(k2)); PIN(k1); PIN(k2); }
        if constexpr (G == 17) { top2(dppu<0x122>(k1), dppu<0x122>(k2)); PIN(k1); PIN(k2); }
        if constexpr (G == 18) { top2(dppu<0x121>(k1), dppu<0x121>(k2)); PIN(k1); PIN(k2); }
        if constexpr (G == 19) { auto a = __builtin_amdgcn_permlane16_swap(k1, k1, false, false); auto b = __builtin_amdgcn_permlane16_swap(k2, k2, false, false);
            k1 = a[0]; k2 = b[0]; top2(a[1], b[1]); PIN(k1); PIN(k2); }
        if constexpr (G == 20) { auto a = __builtin_amdgcn_permlane32_swap(k1, k1, false, false); auto b = __builtin_amdgcn_permlane32_swap(k2, k2, false, false);
            k1 = a[0]; k2 = b[0]; top2(a[1], b[1]);
            if (lane == 0) { sk[wid * 2] = k1; sk[wid * 2 + 1] = k2; } }
        if constexpr (G == 29) {
#pragma unroll
            for (int j = 0; j < 4; ++j) skv[j] = *(const LAS u32x4*)(sk + 4 * j);
            k1 = 0; k2 = 0;
        }
        if constexpr (G == 30 || G == 31) {
#pragma unroll
            for (int j = 8 * (G - 30); j < 8 * (G - 30) + 8; j += 2) top2(skv[j >> 2][j & 3], skv[j >> 2][(j & 3) + 1]);
            PIN(k1); PIN(k2);
        }
        if constexpr (G == 32) {
            k1 = __builtin_amdgcn_readfirstlane(k1); k2 = __builtin_amdgcn_readfirstlane(k2);
            R.sel0 = 0xFFFF - (int)(k1 & 0xFFFFu); R.sel1 = 0xFFFF - (int)(k2 & 0xFFFFu);
            ld_u16_nop(xs0, (const char*)bx + R.sel0 * 1024, tid * 2);
            ld_u16_nop(xs1, (const char*)bx + R.sel1 * 1024, tid * 2);
        }
        if constexpr (G == 33) {
            const unsigned o0 = k1 >> 16, o1 = k2 >> 16;
            const float r0 = __uint_as_float(((o0 & 0x8000u) ? (o0 & 0x7FFFu) : (~o0 & 0xFFFFu)) << 16);
            const float r1 = __uint_as_float(((o1 & 0x8000u) ? (o1 & 0x7FFFu) : (~o1 & 0xFFFFu)) << 16);
            R.sig0 = __uint_as_float(__builtin_amdgcn_readfirstlane(__float_as_uint(sigmoidf_(r0))));
            R.sig1 = __uint_as_float(__builtin_amdgcn_readfirstlane(__float_as_uint(sigmoidf_(r1))));
        }
        if constexpr (G >= 61 && G <= 64) {
            if constexpr (G == 61) { asm volatile("" : "+v"(xs0), "+v"(xs1), "+v"(Q.wgv), "+v"(Q.wuv)); ma0 = lo16(xs0); ma1 = lo16(xs1); }
            constexpr int c = 2 * (G - 61);
            const float g0 = lo16(Q.wgv[c >> 1]), g1 = hi16(Q.wgv[c >> 1]), u0 = lo16(Q.wuv[c >> 1]), u1 = hi16(Q.wuv[c >> 1]);
            mv[c] = ma0 * g0; mv[c + 1] = ma0 * g1; mv[8 + c] = ma0 * u0; mv[9 + c] = ma0 * u1;
            mv[16 + c] = ma1 * g0; mv[17 + c] = ma1 * g1; mv[24 + c] = ma1 * u0; mv[25 + c] = ma1 * u1;
            PIN(mv[c]); PIN(mv[c + 1]); PIN(mv[8 + c]); PIN(mv[9 + c]); PIN(mv[16 + c]); PIN(mv[17 + c]); PIN(mv[24 + c]); PIN(mv[25 + c]);
        }
        if constexpr (G >= 65 && G <= 68) {
#pragma unroll
            for (int j = 4 * (G - 65); j < 4 * (G - 65) + 4; ++j) { auto r = __builtin_amdgcn_permlane32_swap(__float_as_uint(mv[j]), __float_as_uint(mv[j + 16]), false, false);
                mv[j] = __uint_as_float(r[0]) + __uint_as_float(r[1]); PIN(mv[j]); }
        }
        if constexpr (G == 69 || G == 70) {
#pragma unroll
            for (int j = 4 * (G - 69); j < 4 * (G - 69) + 4; ++j) { auto r = __builtin_amdgcn_permlane16_swap(__float_as_uint(mv[j]), __float_as_uint(mv[j + 8]), false, false);
                mv[j] = __uint_as_float(r[0]) + __uint_as_float(r[1]); PIN(mv[j]); }
        }
        if constexpr (G == 71) { const bool up_ = lane & 8;
#pragma unroll
            for (int j = 0; j < 4; ++j) { const float send = up_ ? mv[j] : mv[j + 4], keep = up_ ? mv[j + 4] : mv[j]; mv[j] = keep + dppf<0x128>(send); PIN(mv[j]); } }
        if constexpr (G == 72) { const bool up_ = lane & 4;
#pragma unroll
            for (int j = 0; j < 2; ++j) { const float send = up_ ? mv[j] : mv[j + 2], keep = up_ ? mv[j + 2] : mv[j]; mv[j] = keep + dppf<0x1B>(dppf<0x141>(send)); PIN(mv[j]); } }
        if constexpr (G == 73) { const bool up_ = lane & 2;
            const float send = up_ ? mv[0] : mv[1], keep = up_ ? mv[1] : mv[0]; mv[0] = keep + dppf<0x4E>(send);
            mv[0] += dppf<0xB1>(mv[0]);
            if (!(lane & 1)) red[wid * 32 + (lane >> 1)] = mv[0];
        }
        if constexpr (G == 77) {
            if (tid < 16) {
                const int c = tid & 7, t = tid >> 3;
                float g = 0.f, u = 0.f;
#pragma unroll
                for (int w = 0; w < 8; ++w) { g += red[w * 32 + 16 * t + c]; u += red[w * 32 + 16 * t + 8 + c]; }
                g = bfround(g); u = bfround(u);
                hs[t * 8 + c] = bfround(bfround(g * sigmoidf_(g)) * u);
            }
        }
        if constexpr (G == 93) {
#pragma unroll
            for (int j = 0; j < 8; ++j) wdv[j] = *(const LAS u16*)((LAS char*)lds_ + MOE_WD + j * 1024 + tid * 2);
#pragma unroll
            for (int j = 0; j < 4; ++j) { const f32x4 t = *(const LAS f32x4*)(hs + 4 * j); hsv[4 * j] = t[0]; hsv[4 * j + 1] = t[1]; hsv[4 * j + 2] = t[2]; hsv[4 * j + 3] = t[3]; }
        }
        if constexpr (G == 95) {
            float y0 = 0.f, y1 = 0.f;
#pragma unroll
            for (int j = 0; j < 8; ++j) { const float w = lo16(wdv[j]); y0 += hsv[j] * w; y1 += hsv[8 + j] * w; }
            float* yp = (float*)(ws + WS_YP) + (size_t)cg * 1024;
            store_f32_wt(yp + tid, y0); store_f32_wt(yp + 512 + tid, y1);
        }
    };

    const char* Kh = ws + WS_KF + (size_t)h * 64 * 8192;
    const unsigned vlin = lane * 16;
    bf16x8 qf[8];
    {
        lptr xA[4];
#pragma unroll
        for (int ks = 0; ks < 4; ++ks) xA[ks] = lds + (32 * wid + l31) * 128 + (((2 * ks + hi) ^ ((l31 >> 1) & 7)) << 4);
        const int tq = (lane & 15) >> 2, tp = lane & 3, tblk = (lane >> 4) & 1;
        lptr wA[4];
#pragma unroll
        for (int c = 0; c < 4; ++c) wA[c] = lds + 32768 + (8 * hi + tq) * 256 + ((c ^ tq) << 6) + (tp >> 1) * 32 + tblk * 16 + (tp & 1) * 8;
        auto dmaS = [&](int s, int j) { q_dma(Q, P, C, s, j); };
        auto rdW = [&](int s, int i) { const int o = ((s + 2) % 3) * QST + (i >> 2) * 4096; return cat(ldstr(wA[i & 3] + o), ldstr(wA[i & 3] + o + 1024)); };
        auto rdX = [&](int s, int ks) { return ldsr128(xA[ks] + ((s + 2) % 3) * QST); };
        f32x16 acc[4] = {};
        bf16x8 wfr[8], xfr[2];
        unsigned kvf = 0;
        WAIT_BAR(14);
        xfr[0] = rdX(0, 0);
#pragma unroll
        for (int i = 0; i < 4; ++i) wfr[i] = rdW(0, i);
        static_for<128>([&](auto SI) {
            {
                constexpr int s = SI / 16, i = SI % 16;
                if (i == 12 && s < 7) {
                    if (s == 0) { WAIT_BAR(8); } else if (s == 1) { WAIT_BAR(6); } else if (s == 2) { WAIT_BAR(8); } else if (s < 6) { WAIT_BAR(6); } else { WAIT_BAR(0); }
                }
                const int nx = i + 4;
                if (nx < 16) { if ((nx & 3) == 0) xfr[(nx >> 2) & 1] = rdX(s, nx >> 2); wfr[nx & 7] = rdW(s, nx); }
                else if (s < 7) { if (nx == 16) xfr[0] = rdX(s + 1, 0); wfr[nx & 7] = rdW(s + 1, nx - 16); }
                acc[i & 3] = MFMA(wfr[i & 7], xfr[(i >> 2) & 1], acc[i & 3]);
                if (i == 12 || i == 14) { if (s + 3 < 8) dmaS(s + 3, (i - 12) / 2); }
                else if (s == 0) { if (i < 6) dmaS(2, i); }
                else if (i < 12 && i % 3 == 0) { if (s + 2 < 8) dmaS(s + 2, 2 + i / 3); }
                if (s == 6 && grp == 0) {
                    if (i == 12) glds16x4_dev(Kh + rg * 4096, vlin, lds0 + RING0 * 16384 + rg * 4096);
                    if (i == 14) glds16x4_dev(Kh + 16384 + rg * 4096, vlin, lds0 + (RING0 + 2) * 16384 + rg * 4096);
                }
                moe_gap(SI);
                if (s == 1 && i == 13 && tid == 0) flag_store(C.sy + SY_KV + h * 8 + bi, C.tag);
                if (s == 4 && i == 3 && wid == 0)
                    asm volatile("global_load_dword %0, %1, %2 sc1" : "=v"(kvf) : "v"((lane & 7) * 4), "s"(C.sy + SY_KV + h * 8));
                if (s == 5 && i == 13 && wid == 0) {
                    asm volatile("" : "+v"(kvf));
                    if (__builtin_expect(!__all(kvf == C.tag), 0)) wait_flags<1>(C.sy + SY_KV + h * 8, 8, C.tag, lane);
                }
                if (s == 6 && i == 13 && tid == 0) flag_store(C.sy + SY_Y + bid, C.tag);
                SBAR();
            }
        });
#pragma unroll
        for (int c = 0; c < 4; ++c) { qf[2 * c] = pack8(acc[c], 0, QSCALE); qf[2 * c + 1] = pack8(acc[c], 1, QSCALE); }
    }
    const int n1 = 2 * bi + 2;
    auto tile_of = [&](int n, int g) { return n < n1 ? n : n1 + 2 * (n - n1) + g; };
    auto jstar = [&](int tn) { return tn <= 2 * n1 ? tn : tn <= 2 * n1 + 2 ? tn + 1 : 2 * tn - 2 * n1 - 2; };
    auto jg1 = [&](int tn) { return tn == 2 * n1 + 1 ? tn : 2 * tn - 2 * n1 - 1; };
    int tb_d0, tb_d1, tb_kv, tb_rel;
    auto make_job = [&](int tn, int& d0, int& d1) {
        const int n = tn >> 1;
        int a_off, b_off; bool a_ok, b_ok;
        const int kb = (int)WS_KF + h * 64 * 8192 + rg * 4096, vb = (int)WS_VF + h * 64 * 8192 + rg * 4096;
        if (tn & 1) { a_ok = n >= 1 && n <= 17; a_off = vb + tile_of(n - 1, 0) * 16384; b_ok = n >= n1 && n <= 16; b_off = kb + tile_of(n, 1) * 16384; }
        else        { a_ok = n <= 16;           a_off = kb + tile_of(n, 0) * 16384;     b_ok = n - 2 >= n1 && n <= 18; b_off = vb + tile_of(n - 2, 1) * 16384; }
        const int da = a_off | ((jstar(tn) + RING0) & 7) | 8, db = b_off | ((jg1(tn) + RING0) & 7) | 8;
        d0 = a_ok ? da : b_ok ? db : 0; d1 = (a_ok && b_ok) ? db : 0;
    };
    {
        const int n = lane < 19 ? lane : 18;
        make_job(2 * n + grp + 3, tb_d0, tb_d1);
        const int ks = ((n < n1 || grp == 0 ? jstar(2 * n) : jg1(2 * n + 1)) + RING0) & 7;
        const int vs = ((n < n1 || grp == 0 ? jstar(2 * n + 3) : jg1(2 * n + 4)) + RING0) & 7;
        tb_kv = ks | vs << 8;
        tb_rel = 128 * ((grp || n >= n1) ? blkB : blkA) + 32 * rg - 64 * tile_of(n, grp);
    }
    auto job_from = [&](int d0, int d1) -> DmaJob {
        DmaJob d;
        d.has0 = d0 & 8; d.has1 = d1 & 8;
        d.src0 = ws + (unsigned)(d0 & ~4095); d.src1 = ws + (unsigned)(d1 & ~4095);
        d.dst0 = lds0 + (d0 & 7) * 16384 + rg * 4096; d.dst1 = lds0 + (d1 & 7) * 16384 + rg * 4096;
        return d;
    };
    auto job = [&](int n) { return job_from(__builtin_amdgcn_readlane(tb_d0, n), __builtin_amdgcn_readlane(tb_d1, n)); };
    if (grp == 1) {
#pragma unroll
        for (int c = 0; c < 8; ++c) *(LAS bf16x8*)((LAS char*)lds_ + K2_QB + rg * 8192 + c * 1024 + lane * 16) = qf[c];
    }
    f32x16 o[4] = {};
    SMState st; st.mhat = 0.f; st.l = 0.f; st.negm = f32x16{};
    const lptr kp0 = lds + lane * 16;
    char* ctx = ws + WS_CTX;
    const rsrc_t ctxr = make_rsrc(ctx, (unsigned)((size_t)SEQ * NHD * 2));
    auto store_ctx = [&](int blk, int d, const f32x16& v, float inv) {
        const unsigned off = (unsigned)(((4 * blk + rg) * 32 + h) * 8 + 2 * d) * 1024 + lane * 16;
        store_wt(ctxr, off, pack8(v, 0, inv));
        store_wt(ctxr, off + 1024, pack8(v, 1, inv));
    };
    auto half_sum = [&](float v) { auto rr = __builtin_amdgcn_permlane32_swap(__float_as_uint(v), __float_as_uint(v), false, false);
                                   return __uint_as_float(rr[0]) + __uint_as_float(rr[1]); };
    auto kslot = [&](int n) { return kp0 + (__builtin_amdgcn_readlane(tb_kv, n) & 7) * 16384; };
    auto vslot = [&](int n) { return kp0 + ((__builtin_amdgcn_readlane(tb_kv, n) >> 8) & 7) * 16384; };
    WAIT_BAR(0);
    bool first = true, pend = false;
    float invA = 0.f, fpend = 1.f;
    f32x16 sA0, sA1, sB0, sB1; bf16x8 pf[4], vpre[LA], kpre[LA];
#pragma unroll
    for (int i = 0; i < LA; ++i) kpre[i] = ldsr128(kslot(0) + KOFF(i));
    if (grp == 1) { WAIT_BAR(0); }
    const int nsw = grp == 0 ? n1 - 1 : 100;
    auto headB = [&](int n) {
        if (__builtin_expect(n == nsw + 1, 0)) { invA = 1.0f / half_sum(st.l); st.mhat = 0.f; st.l = 0.f; first = true; }
    };
    auto tailB = [&](int n) {
        first = false;
        if (__builtin_expect(pend || (unsigned)(n - nsw) < 3u, 0)) {
            if (n == nsw + 2 && tid == 0) flag_store(C.sy + SY_CTX + blkA * 32 + h, C.tag);
            if (pend) {
#pragma unroll
                for (int d = 0; d < 4; ++d)
#pragma unroll
                    for (int r = 0; r < 16; ++r) o[d][r] *= fpend;
                pend = false;
            }
            if (n == nsw + 1) {
#pragma unroll
                for (int d = 0; d < 4; ++d) store_ctx(blkA, d, o[d], invA);
#pragma unroll
                for (int d = 0; d < 4; ++d) o[d] = f32x16{};
            }
            if (n == nsw) {
#pragma unroll
                for (int c = 0; c < 8; ++c) qf[c] = ldsr128(lds + K2_QB + rg * 8192 + c * 1024 + lane * 16);
                st.negm = f32x16{};
            }
        }
    };
#define BARRIER_L() asm volatile("s_waitcnt lgkmcnt(0)\n\ts_barrier" ::: "memory")
    lptr kcur = kslot(0), vcur = kp0;
    DmaJob dj = job(0);
    int rel = 0;
    auto pinjob = [&]() { asm volatile("" : "+s"(dj.src0), "+s"(dj.src1), "+s"(dj.dst0), "+s"(dj.dst1)); };
    auto midA = [&](int n) { vcur = vslot(n > 0 ? n - 1 : 0); rel = __builtin_amdgcn_readlane(tb_rel, n); asm volatile("" : "+v"(vcur), "+s"(rel)); };
    auto midB = [&](int n) { kcur = kslot(n + 1); dj = job(n + 1); asm volatile("" : "+v"(kcur)); pinjob(); };
#define RELARGS first, rel < 63, rel + l31 - 4 * hi, pend, fpend
    attn_A<true, false>(kcur, vcur, qf, st.negm, kpre, vpre, sA0, sA1, sA0, sA1, pf, st.l, dj, vlin, [&]() { midA(0); });
    BARRIER_L();
    attn_B<false, true>(vcur, kcur, o, pf, vpre, kpre, sA0, sA1, st, RELARGS, [&]() { midB(0); });
    tailB(0);
    WAIT_BAR(0);
#define STEP(n, C0, C1, P0, P1) do { \
        attn_A<true, true>(kcur, vcur, qf, st.negm, kpre, vpre, C0, C1, P0, P1, pf, st.l, dj, vlin, [&]() { midA(n); }); \
        BARRIER_L(); \
        headB(n); \
        attn_B<true, true>(vcur, kcur, o, pf, vpre, kpre, C0, C1, st, RELARGS, [&]() { midB(n); }); \
        tailB(n); \
        if ((n) < 16) { WAIT_BAR(0); } else { BARRIER_L(); }     \
    } while (0)
    unsigned pfd;
    for (int n = 1; n < 17; n += 2) {
        STEP(n, sB0, sB1, sA0, sA1);
        STEP(n + 1, sA0, sA1, sB0, sB1);
    }
#undef STEP
    asm volatile("global_load_dword %0, %1, %2" : "=v"(pfd) : "v"(vlin * 8),
                 "s"((const char*)wo + (size_t)(wid < 2 ? bid * 2 + wid : ((bid & 7) >> 1) * 128 + ((bid >> 3) & 15) * 4 + ((wid - 2) & 3)) * 8192));
    attn_A<false, true>(kcur, vcur, qf, st.negm, kpre, vpre, sB0, sB1, sA0, sA1, pf, st.l, dj, vlin, [&]() { midA(17); });
    BARRIER_L();
    attn_B<true, false>(vcur, kcur, o, pf, vpre, kpre, sB0, sB1, st, false, false, 0, pend, fpend, [&]() {});
    BARRIER_L();
    if (grp == 0) { BARRIER_L(); }
    BARRIER_L();
    {
        const float lsum = half_sum(st.l);
        LAS char* xch = (LAS char*)lds_;
        LAS char* mine = xch + wid * 8192;
        auto put = [&](int dd, const f32x16& v) {
#pragma unroll
            for (int q4 = 0; q4 < 4; ++q4)
                *(LAS f32x4*)(mine + (dd * 4 + q4) * 1024 + lane * 16) = (f32x4){v[4 * q4], v[4 * q4 + 1], v[4 * q4 + 2], v[4 * q4 + 3]};
        };
        if (grp) { put(0, o[0]); put(1, o[1]); } else { put(0, o[2]); put(1, o[3]); }
        LAS float* ml = (LAS float*)(xch + 65536);
        ml[wid * 128 + lane] = st.mhat; ml[wid * 128 + 64 + lane] = lsum;
        BARRIER_L();
        const int ow = wid ^ 4;
        const float m2 = ml[ow * 128 + lane], l2 = ml[ow * 128 + 64 + lane];
        const float mm = fmaxf(st.mhat, m2);
        const float f1 = __builtin_amdgcn_exp2f(st.mhat - mm), f2 = __builtin_amdgcn_exp2f(m2 - mm);
        const float inv = 1.0f / (lsum * f1 + l2 * f2);
        const float c1 = f1 * inv, c2 = f2 * inv;
        const LAS char* theirs = xch + ow * 8192;
        auto fin = [&](int dd, f32x16 v) {
#pragma unroll
            for (int q4 = 0; q4 < 4; ++q4) {
                const f32x4 t = *(const LAS f32x4*)(theirs + (dd * 4 + q4) * 1024 + lane * 16);
#pragma unroll
                for (int e = 0; e < 4; ++e) v[4 * q4 + e] = v[4 * q4 + e] * c1 + t[e] * c2;
            }
            store_ctx(blkB, 2 * grp + dd, v, 1.0f);
        };
        if (grp) { fin(0, o[2]); fin(1, o[3]); } else { fin(0, o[0]); fin(1, o[1]); }
    }
    asm volatile("s_waitcnt vmcnt(0)" : "+v"(pfd));
    BARRIER_L();
    if (tid == 0) { flag_store(C.sy + SY_CTX + blkB * 32 + h, C.tag); flag_store(C.sy + SY_CTX + blkA * 32 + h, C.tag); }
}

constexpr int K3_SLOT = 32768;
DI unsigned take(unsigned src) { unsigned d; asm volatile("v_mov_b32 %0, %1" : "=v"(d) : "v"(src)); return d; }
DI void phase_oproj(const Params& P, const Ctx& C, const Routed& R) {
    char* ws = P.ws;
    const lptr lds = C.lds;
    const unsigned lds0 = C.lds0;
    const int lane = C.lane, l31 = lane & 31, hi = lane >> 5, wid = C.wid;
    const int bid = C.bid, kq = (bid & 7) >> 1, ct = (bid >> 3) & 3, blk = 2 * (bid >> 5) + (bid & 1);
    const int qp = wid >> 2, cb = wid & 3;
    const char* csrc = ws + WS_CTX + (size_t)((4 * blk + (wid >> 1)) * 32 + 8 * kq) * 8192 + (wid & 1) * 2048;
    const char* wsrc = (const char*)P.wo + (size_t)kq * 1024 * 1024 + ct * 256;
    unsigned voffW[2];
#pragma unroll
    for (int j = 0; j < 2; ++j) voffW[j] = (8 * wid + 4 * j + (lane >> 4)) * 1024 + (((lane & 15) ^ ((lane >> 4) << 2)) << 4);
    const unsigned vlin = lane * 16;
    auto dma = [&](int t, int j) {
        const unsigned b = lds0 + (t % 5) * K3_SLOT;
        if (j < 2) { const char* src = csrc + t * 4096 + j * 1024;
            asm volatile("s_mov_b32 m0, %2\n\ts_nop 0\n\tglobal_load_lds_dwordx4 %0, %1 sc1" :: "v"(vlin), "s"(src),
                         "s"(__builtin_amdgcn_readfirstlane(b + (wid >> 1) * 4096 + (wid & 1) * 2048 + j * 1024)) : "memory"); }
        else glds16(wsrc + (size_t)t * 65536, voffW[j - 2], b + 16384 + (8 * wid + 4 * (j - 2)) * 256);
    };
    if (wid == 0) wait_flags<1>(C.sy + SY_CTX + blk * 32 + 8 * kq, 8, C.tag, lane);
    asm volatile("s_barrier" ::: "memory");
#pragma unroll
    for (int t = 0; t < 5; ++t)
#pragma unroll
        for (int j = 0; j < 4; ++j) dma(t, j);
    const int tq = (lane & 15) >> 2, tp = lane & 3, tblk = (lane >> 4) & 1;
    const lptr wA = lds + 16384 + (4 * hi + tq) * 256 + ((cb ^ tq) << 6) + tblk * 32 + tp * 8;
    const lptr cA = lds + 2 * qp * 4096 + lane * 16;
    bf16x8 wf[4], cf[4][2];
    auto rd = [&](int G) { const int o = ((G >> 2) % 5) * K3_SLOT, f = G & 3;
        wf[f] = cat(ldstr(wA + o + f * 4096), ldstr(wA + o + f * 4096 + 2048));
        cf[f][0] = ldsr128(cA + o + f * 1024); cf[f][1] = ldsr128(cA + o + 4096 + f * 1024); };
    f32x16 acc[2] = {};
    unsigned yf[4]; u32x4 yv4[4];
    WAIT_BAR(16);
    rd(0); rd(1);
    static_for<64>([&](auto GI) {
        constexpr int G = GI, t = G >> 2, f = G & 3;
        if constexpr (f == 2 && t < 15) {
            if constexpr (t <= 11) { WAIT_BAR(12); } else if constexpr (t == 12) { WAIT_BAR(8); } else if constexpr (t == 13) { WAIT_BAR(4); } else { WAIT_BAR(0); }
        }
        if constexpr (G + 2 < 64) rd(G + 2);
        acc[0] = MFMA(wf[f], cf[f][0], acc[0]);
        acc[1] = MFMA(wf[f], cf[f][1], acc[1]);
        if constexpr (f >= 2) { if constexpr (t + 5 < 16) dma(t + 5, f - 2); } else if constexpr (t >= 1 && t + 4 < 16) dma(t + 4, f + 2);
        if (wid == 1) {
            if constexpr (G == 0) {
#pragma unroll
                for (int j = 0; j < 4; ++j) asm volatile("global_load_dword %0, %1, %2 sc1" : "=v"(yf[j]) : "v"(lane * 4), "s"(C.sy + SY_Y + 64 * j));
            }
            if constexpr (G == 15) {
                const bool ok = take(yf[0]) == C.tag && take(yf[1]) == C.tag && take(yf[2]) == C.tag && take(yf[3]) == C.tag;
                if (__builtin_expect(!__all(ok), 0)) wait_flags<4>(C.sy + SY_Y, 256, C.tag, lane);
#pragma unroll
                for (int j = 0; j < 4; ++j) {
                    const int pp = lane + 64 * j < MOE_WGS ? lane + 64 * j : MOE_WGS - 1;
                    asm volatile("global_load_dwordx4 %0, %1, %2 sc1" : "=v"(yv4[j]) : "v"(pp * 4096), "s"(ws + WS_YP + bid * 16));
                }
            }
            if constexpr (G == 31) {
                f32x4 y = {0.f, 0.f, 0.f, 0.f};
#pragma unroll
                for (int j = 0; j < 4; ++j)
#pragma unroll
                    for (int e = 0; e < 4; ++e) { const float v = __uint_as_float(take(yv4[j][e])); y[e] += lane + 64 * j < MOE_WGS ? v : 0.f; }
#pragma unroll
                for (int e = 0; e < 4; ++e) y[e] = wave_sum(y[e], lane);
                if (lane < 4) store_f32_wt((float*)(ws + WS_Y) + bid * 4 + lane, lane == 0 ? y[0] : lane == 1 ? y[1] : lane == 2 ? y[2] : y[3]);
            }
            if constexpr (G == 47) { if (lane == 0) flag_store(C.sy + SY_Y2 + bid, C.tag); }
        }
        SBAR();
    });
    const int tile = blk * 4 + ct;
    const rsrc_t xr = make_rsrc(ws + WS_X, 64u * 16u * 16384u);
#pragma unroll
    for (int a = 0; a < 2; ++a) {
        const int qg = 2 * qp + a;
        if (qg != kq) {
            const unsigned off = (unsigned)(((tile * 4 + qg) * 4 + kq) * 4 + cb) * 4096 + lane * 16;
#pragma unroll
            for (int q4 = 0; q4 < 4; ++q4) {
                const f32x4 v = {acc[a][4 * q4], acc[a][4 * q4 + 1], acc[a][4 * q4 + 2], acc[a][4 * q4 + 3]};
                __builtin_amdgcn_raw_buffer_store_b128(__builtin_bit_cast(u32x4, v), xr, off + q4 * 1024, 0, 16);
            }
        }
    }
    WAIT_BAR(0);
    if (C.tid == 0) flag_store(C.sy + SY_X + tile * 4 + kq, C.tag);
    if (qp != (kq >> 1)) return;
    const f32x16 own = (kq & 1) ? acc[1] : acc[0];
    const int q = (4 * blk + kq) * 32 + l31;
    const float g = __uint_as_float(flag_load((const unsigned*)(ws + WS_G) + q));
    s16x4 xb[4];
#pragma unroll
    for (int q4 = 0; q4 < 4; ++q4) xb[q4] = *(const s16x4*)(P.ax + (size_t)q * DM + ct * 128 + cb * 32 + 8 * q4 + 4 * hi);
    const int i0 = R.sel0, i1 = R.sel1;
    const bool r0 = (q == i0), r1 = (q == i1);
    f32x4 yrow[4] = {};
    if ((i0 >> 5) == 4 * blk + kq || (i1 >> 5) == 4 * blk + kq) {
        wait_flags<1>(C.sy + SY_Y2 + ct * 32, 32, C.tag, lane);
        wait_flags<1>(C.sy + SY_Y2 + 128 + ct * 32, 32, C.tag, lane);
        const rsrc_t yr = make_rsrc(ws + WS_Y, 4096);
#pragma unroll
        for (int q4 = 0; q4 < 4; ++q4)
            yrow[q4] = __builtin_bit_cast(f32x4, __builtin_amdgcn_raw_buffer_load_b128(yr, ((r1 ? 512 : 0) + ct * 128 + cb * 32 + 8 * q4 + 4 * hi) * 4, 0, 16));
    }
    wait_flags<1>(C.sy + SY_X + tile * 4, 4, C.tag, lane);
    f32x4 part[4][4];
#pragma unroll
    for (int k = 0; k < 4; ++k)
#pragma unroll
        for (int q4 = 0; q4 < 4; ++q4)
            part[k][q4] = __builtin_bit_cast(f32x4, __builtin_amdgcn_raw_buffer_load_b128(xr, (k == kq ? 0x7FFFFFF0u : (unsigned)(((tile * 4 + kq) * 4 + k) * 4 + cb) * 4096 + lane * 16 + q4 * 1024), 0, 16));
    {
        const float ysc = r0 ? R.sig0 : r1 ? R.sig1 : 0.f;
        u32x2 w2[4];
#pragma unroll
        for (int q4 = 0; q4 < 4; ++q4) {
            float res[4];
#pragma unroll
            for (int e = 0; e < 4; ++e) {
                float a = 0.f;
#pragma unroll
                for (int k = 0; k < 4; ++k) a += k == kq ? own[4 * q4 + e] : part[k][q4][e];
                float b = bf2f((u16)xb[q4][e]);
                if (r0 || r1) b += yrow[q4][e] * ysc;
                res[e] = g * a + (1.0f - g) * b;
            }
            w2[q4] = (u32x2){cvtpk(res[0], res[1]), cvtpk(res[2], res[3])};
        }
        const rsrc_t outr = make_rsrc(P.out, SEQ * DM * 2);
#pragma unroll
        for (int k = 0; k < 4; k += 2) {
            auto rx = __builtin_amdgcn_permlane32_swap(w2[k][0], w2[k + 1][0], false, false);
            auto ry = __builtin_amdgcn_permlane32_swap(w2[k][1], w2[k + 1][1], false, false);
            const u32x4 v = {rx[0], ry[0], rx[1], ry[1]};
            __builtin_amdgcn_raw_buffer_store_b128(v, outr, (q * DM + ct * 128 + cb * 32 + 8 * k + 8 * hi) * 2, 0, 16);
        }
    }
}

__global__ __launch_bounds__(512, 2)
void mega_kernel(Params Parg) {
    extern __shared__ __attribute__((aligned(16))) char lds_[];
    const int wid = __builtin_amdgcn_readfirstlane(threadIdx.x >> 6);
    unsigned tag;
    Routed R;
    QPre Q;
    {
        const Params P = load_params();
        unsigned tagv;
        asm volatile("global_load_dword %0, %1, %2 sc1" : "=v"(tagv) : "v"(0), "s"((const unsigned*)(P.ws + WS_SYNC) + SY_EPOCH));
        Ctx C = make_ctx(lds_, P.ws, wid, 0u);
        phase_kv(P, C, Q, tagv);
        tag = __builtin_amdgcn_readfirstlane(C.tag);
    }
    { const Params P = load_params(); phase_attn(P, make_ctx(lds_, P.ws, wid, tag), Q, R); }
    {
        const Params P = load_params();
        const Ctx C = make_ctx(lds_, P.ws, wid, tag);
        phase_oproj(P, C, R);
        if (C.tid == 0) __hip_atomic_fetch_add(C.sy + SY_EPOCH, 1u, __ATOMIC_RELAXED, __HIP_MEMORY_SCOPE_AGENT);
    }
}

extern "C" void kernel_launch(void* const* d_in, const int* in_sizes, int n_in, void* d_out, int out_size, void* d_ws, size_t ws_size,
                              hipStream_t stream) {
    const u16* ax = (const u16*)d_in[0]; const u16* ckv = (const u16*)d_in[1]; const u16* wq = (const u16*)d_in[2];
    const u16* wuk = (const u16*)d_in[3]; const u16* wuv = (const u16*)d_in[4]; const u16* wo = (const u16*)d_in[5];
      const u16* wr = (const u16*)d_in[7]; const u16* wg = (const u16*)d_in[8];
    const u16* wu = (const u16*)d_in[9]; const u16* wd = (const u16*)d_in[10]; const u16* gw = (const u16*)d_in[11];
    char* ws = (char*)d_ws;
    (void)hipFuncSetAttribute((const void*)mega_kernel, hipFuncAttributeMaxDynamicSharedMemorySize, 163840);
    Params P{ax, ckv, wq, wuk, wuv, wo, wr, wg, wu, wd, gw, ws, (u16*)d_out};
    mega_kernel<<<256, 512, 163840, stream>>>(P);
}
```

```cpp
#include <hip/hip_runtime.h>
#include <hip/hip_bf16.h>
#include <stdint.h>
#include <utility>

typedef unsigned short u16;
typedef short bf16x8 __attribute__((ext_vector_type(8)));
typedef short s16x4 __attribute__((ext_vector_type(4)));
typedef short v4i16_t __attribute__((ext_vector_type(4)));
typedef float f32x16 __attribute__((ext_vector_type(16)));
typedef float f32x4 __attribute__((ext_vector_type(4)));
typedef unsigned u32x4 __attribute__((ext_vector_type(4)));
typedef unsigned u32x2 __attribute__((ext_vector_type(2)));
#define LAS __attribute__((address_space(3)))
#define DI __device__ __forceinline__
#define MFMA(a, b, c) __builtin_amdgcn_mfma_f32_32x32x16_bf16((a), (b), (c), 0, 0, 0)
#define MFMA16(a, b, c) __builtin_amdgcn_mfma_f32_16x16x32_bf16((a), (b), (c), 0, 0, 0)
#define SBAR() __builtin_amdgcn_sched_barrier(0)
#define PIN(x) asm volatile("" : "+v"(x))

constexpr int SEQ = 2048, DM = 512, NH = 32, HD = 128, NHD = NH * HD;
constexpr int FF = 1792;
constexpr int MOE_WGS = 224, MOE_COLS = 8;

constexpr size_t WS_KF = 0;
constexpr size_t WS_VF = WS_KF + (size_t)SEQ * NHD * 2;
constexpr size_t WS_CTX = WS_VF + (size_t)SEQ * NHD * 2;
constexpr size_t WS_R = WS_CTX + (size_t)SEQ * NHD * 2;
constexpr size_t WS_G = WS_R + SEQ * 4;
constexpr size_t WS_Y = WS_G + SEQ * 4;
constexpr size_t WS_YP = WS_Y + 4096;
constexpr size_t WS_SYNC = WS_YP + 224 * 4096;
constexpr size_t WS_X = WS_SYNC + 16384;
constexpr int SY_EPOCH = 0, SY_KV = 256, SY_R = 512, SY_Y = 768, SY_Y2 = 1024, SY_CTX = 1280, SY_X = 1792;

template <int... Is, class F> DI void static_for_(std::integer_sequence<int, Is...>, F&& f) { (f(std::integral_constant<int, Is>{}), ...); }
template <int N, class F> DI void static_for(F&& f) { static_for_(std::make_integer_sequence<int, N>{}, f); }
DI unsigned cvtpk(float lo, float hi) {
    typedef float f32x2 __attribute__((ext_vector_type(2)));
    typedef __bf16 bf16x2 __attribute__((ext_vector_type(2)));
    f32x2 v = {lo, hi}; bf16x2 b = __builtin_convertvector(v, bf16x2); return __builtin_bit_cast(unsigned, b);
}
DI float bf2f(u16 v) { return __uint_as_float((unsigned)v << 16); }
DI float bfround(float x) { return __uint_as_float(cvtpk(x, x) << 16); }
DI bf16x8 pack8(const f32x16& x, int s, float sc) {
    u32x4 w = {cvtpk(x[8 * s] * sc, x[8 * s + 1] * sc), cvtpk(x[8 * s + 2] * sc, x[8 * s + 3] * sc),
               cvtpk(x[8 * s + 4] * sc, x[8 * s + 5] * sc), cvtpk(x[8 * s + 6] * sc, x[8 * s + 7] * sc)};
    return __builtin_bit_cast(bf16x8, w);
}
DI void glds16(const void* sbase, unsigned voff, unsigned lds_addr) {
    lds_addr = __builtin_amdgcn_readfirstlane(lds_addr);
    asm volatile("s_mov_b32 m0, %2\n\ts_nop 0\n\tglobal_load_lds_dwordx4 %0, %1" :: "v"(voff), "s"(sbase), "s"(lds_addr) : "memory");
}
DI unsigned flag_load(const unsigned* p) { return __hip_atomic_load(p, __ATOMIC_RELAXED, __HIP_MEMORY_SCOPE_AGENT); }
DI void flag_store(unsigned* p, unsigned v) { __hip_atomic_store(p, v, __ATOMIC_RELAXED, __HIP_MEMORY_SCOPE_AGENT); }
DI void store_f32_wt(float* p, float v) { __hip_atomic_store((unsigned*)p, __float_as_uint(v), __ATOMIC_RELAXED, __HIP_MEMORY_SCOPE_AGENT); }
template <int NW> DI void wait_flags(const unsigned* f, int n, unsigned tag, int lane) {
    const unsigned long long t0 = __builtin_amdgcn_s_memrealtime();
    for (;;) {
        bool ok = true;
#pragma unroll
        for (int j = 0; j < NW; ++j) { const int i = lane + 64 * j; ok &= (i < n ? flag_load(f + i) : tag) == tag; }
        if (__all(ok)) break;
        if (__builtin_amdgcn_s_memrealtime() - t0 > 5000000ull) break;
        __builtin_amdgcn_s_sleep(2);
    }
}
DI void glds16x4_dev(const void* sbase, unsigned voff, unsigned lds_addr) {
    lds_addr = __builtin_amdgcn_readfirstlane(lds_addr);
    asm volatile("s_mov_b32 m0, %2\n\ts_nop 0\n\tglobal_load_lds_dwordx4 %0, %1 sc1\n\tglobal_load_lds_dwordx4 %0, %1 offset:1024 sc1\n\t"
                 "global_load_lds_dwordx4 %0, %1 offset:2048 sc1\n\tglobal_load_lds_dwordx4 %0, %1 offset:3072 sc1"
                 :: "v"(voff), "s"(sbase), "s"(lds_addr) : "memory");
}
#define WAIT_BAR(N) asm volatile("s_waitcnt vmcnt(" #N ") lgkmcnt(0)\n\ts_barrier" ::: "memory")
DI int lane_now() { int l; asm volatile("v_mbcnt_lo_u32_b32 %0, -1, 0\n\tv_mbcnt_hi_u32_b32 %0, -1, %0" : "=v"(l)); return l; }
typedef LAS const char* lptr;
DI bf16x8 ldsr128(lptr p) { return *(const LAS bf16x8*)p; }
DI s16x4 ldstr(lptr p) { return __builtin_bit_cast(s16x4, __builtin_amdgcn_ds_read_tr16_b64_v4i16((LAS v4i16_t*)p)); }
DI bf16x8 cat(s16x4 a, s16x4 b) { return __builtin_shufflevector(a, b, 0, 1, 2, 3, 4, 5, 6, 7); }
DI unsigned shx(unsigned v, int m, int lane) { return (unsigned)__builtin_amdgcn_ds_bpermute((lane ^ m) << 2, (int)v); }
DI float shx(float v, int m, int lane) { return __uint_as_float(shx(__float_as_uint(v), m, lane)); }
template <int CTRL> DI unsigned dppu(unsigned v) { return (unsigned)__builtin_amdgcn_update_dpp(0, (int)v, CTRL, 0xF, 0xF, true); }
template <int CTRL> DI float dppf(float v) { return __uint_as_float(dppu<CTRL>(__float_as_uint(v))); }
DI float wave_sum(float v, int lane) {
#pragma unroll
    for (int o = 32; o >= 1; o >>= 1) v += shx(v, o, lane);
    return v;
}
DI float sigmoidf_(float x) { return 1.0f / (1.0f + __expf(-x)); }

constexpr int K1_STAGE = 49152;
typedef __amdgpu_buffer_rsrc_t rsrc_t;
DI rsrc_t make_rsrc(const void* p, unsigned bytes) { return __builtin_amdgcn_make_buffer_rsrc((void*)p, (short)0, (int)bytes, 0x00020000); }
DI void store_wt(rsrc_t r, unsigned off, bf16x8 v) {
    __builtin_amdgcn_raw_buffer_store_b128(__builtin_bit_cast(u32x4, v), r, (int)off, 0, 16);
}

template <bool ISV, class Pre, class Mid, class Tail>
DI void kv_body(const char* cbase, const char* kbase, const char* vbase, rsrc_t outr, unsigned outoff, lptr lds, unsigned lds0, int wid, int lane,
                Pre&& pre, Mid&& mid, Tail&& tail) {
    const int r16 = lane & 15, q4 = lane >> 4;
    unsigned voffW[2], voffC[2], voffG[2];
#pragma unroll
    for (int j = 0; j < 2; ++j) {
        const int n = 2 * wid + j;
        { const int row = 4 * n + (lane >> 4); voffW[j] = row * 8192 + (((lane & 15) ^ ((row & 3) << 2) ^ (((row >> 3) & 1) << 1)) << 4); }
        { const int row = 8 * n + (lane >> 3); voffC[j] = row * 1024 + (((lane & 7) ^ ((row >> 1) & 7)) << 4); }
        voffG[j] = (lane ^ ((wid + 8 * j) & 15)) << 4;
    }
    auto stage = [&](int s) {
        const unsigned b = lds0 + (s % 3) * K1_STAGE + 2 * wid * 1024;
#pragma unroll
        for (int j = 0; j < 2; ++j) glds16(kbase + (size_t)s * 64 * 8192, voffW[j], b + j * 1024);
#pragma unroll
        for (int j = 0; j < 2; ++j) glds16(vbase + (size_t)s * 64 * 8192, voffW[j], b + 16384 + j * 1024);
#pragma unroll
        for (int j = 0; j < 2; ++j) glds16(cbase + s * 128, voffC[j], b + 32768 + j * 1024);
    };
    auto gbuf = [&](int k) { return k == 4 ? 98304 : k == 5 ? 131072 : k == 6 ? 0 : 49152; };
    const int tq = (lane & 15) >> 2, tp = lane & 3;
    lptr wA[2], cA[2];
#pragma unroll
    for (int b = 0; b < 2; ++b) {
        wA[b] = lds + (ISV ? 16384 : 0) + (8 * q4 + tq) * 256 + (((wid & 3) ^ tq) << 6) + ((b ^ (q4 & 1)) << 5) + tp * 8;
        cA[b] = lds + 32768 + r16 * 128 + (((4 * b + q4) ^ (r16 >> 1)) << 4);
    }
    auto mm = [&](const bf16x8& w, const bf16x8& c, const f32x4& acc) { return ISV ? MFMA16(c, w, acc) : MFMA16(w, c, acc); };
    const unsigned olane = outoff + (q4 >> 1) * 1024 + (ISV ? (r16 + 32 * (q4 & 1)) * 16 : (16 * (r16 >> 3) + (r16 & 7) + 32 * (q4 & 1)) * 16);
    auto out_tile = [&](int kg, const f32x4& t00, const f32x4& t01, const f32x4& t10, const f32x4& t11) {
        auto pk = [](const f32x4& x, const f32x4& y) { u32x4 w = {cvtpk(x[0], x[1]), cvtpk(x[2], x[3]), cvtpk(y[0], y[1]), cvtpk(y[2], y[3])}; return __builtin_bit_cast(bf16x8, w); };
        if (ISV) { store_wt(outr, olane + kg * 8192, pk(t00, t10)); store_wt(outr, olane + kg * 8192 + 256, pk(t01, t11)); }
        else     { store_wt(outr, olane + kg * 8192, pk(t00, t01)); store_wt(outr, olane + kg * 8192 + 128, pk(t10, t11)); }
    };

    bf16x8 wf[16][2];
    f32x4 acc[8][2] = {};
    lptr gA[4];
    stage(0); stage(1); stage(2);
    auto rdW = [&](int s, int sl) { const int o = (s % 3) * K1_STAGE + sl * 8192;
#pragma unroll
        for (int b = 0; b < 2; ++b) wf[2 * s + sl][b] = cat(ldstr(wA[b] + o), ldstr(wA[b] + o + 1024)); };
    auto rdC = [&](int s, int i) { return ldsr128(cA[i >> 3] + (s % 3) * K1_STAGE + (i & 7) * 2048); };
    auto rdG = [&](int k, int ks) { return ldsr128(gA[(ks >> 1) & 3] + gbuf(k) + (ks & 1) * 16384 + (ks >> 3) * 256); };
    auto dmaS = [&](int s, int j) {
        const unsigned b = lds0 + (s % 3) * K1_STAGE + 2 * wid * 1024;
        if (j < 2) glds16(kbase + (size_t)s * 64 * 8192, voffW[j], b + j * 1024);
        else if (j < 4) glds16(vbase + (size_t)s * 64 * 8192, voffW[j - 2], b + 16384 + (j - 2) * 1024);
        else glds16(cbase + s * 128, voffC[j - 4], b + 32768 + (j - 4) * 1024);
    };
    auto dmaG = [&](int k, int j) { glds16(cbase + (size_t)(32 * k + wid + 8 * j) * 1024, voffG[j & 1], lds0 + gbuf(k) + (wid + 8 * j) * 1024); };
    bf16x8 cf[8];
    WAIT_BAR(12);
    rdW(0, 0);
#pragma unroll
    for (int i = 0; i < 4; ++i) cf[i] = rdC(0, i);
    static_for<128>([&](auto SI) {
        {
            constexpr int s = SI / 16, i = SI % 16;
            if (i == 12) {
                if (s < 6) { WAIT_BAR(6); } else if (s == 6) { WAIT_BAR(8); } else { WAIT_BAR(8); }
            }
            if (s == 7 && i == 11) {
                const int ln = lane_now();
#pragma unroll
                for (int j = 0; j < 4; ++j) gA[j] = lds + (ln & 15) * 1024 + (((4 * j + (ln >> 4)) ^ (ln & 15)) << 4);
            }
            if constexpr (s == 0 && i < 12) pre(SI);
            if (s == 1 && i == 13) mid();
            constexpr int nx = i + 4;
            if constexpr (nx < 16) { if (nx == 8) rdW(s, 1); cf[nx & 7] = rdC(s, nx); }
            else if constexpr (s < 7) { if (nx == 16) rdW(s + 1, 0); cf[nx & 7] = rdC(s + 1, nx - 16); }
            else cf[nx & 7] = rdG(4, nx - 16);
            acc[i & 7][0] = mm(wf[2 * s + (i >> 3)][0], cf[i & 7], acc[i & 7][0]);
            acc[i & 7][1] = mm(wf[2 * s + (i >> 3)][1], cf[i & 7], acc[i & 7][1]);
            if (s + 3 < 8) { if (i == 12 || i == 14) dmaS(s + 3, (i - 12) / 2); }
            else if (i >= 12) {
                const int j = i - 12;
                if (s == 5) dmaG(4, j);
                else if (s == 6) dmaG(6, j);
                else dmaG(7, j);
            }
            if (i < 12 && i % 3 == 0) { if (s >= 1 && s + 2 < 8) dmaS(s + 2, 2 + i / 3); }
            if (s == 6 && i >= 2 && i < 10 && !(i & 1)) dmaG(5, (i - 2) / 2);
            if (s == 7 && i == 15) out_tile(0, acc[0][0], acc[0][1], acc[1][0], acc[1][1]);
            SBAR();
        }
    });
    f32x4 a2[2][2][2];
    static_for<128>([&](auto KI) {
        {
            constexpr int k = 4 + KI / 32, ks = KI % 32;
            if (ks == 28 && k < 7) {
                if (k == 4) { WAIT_BAR(16); } else if (k == 5) { WAIT_BAR(16); } else { WAIT_BAR(21); }
            }
            constexpr int nx = ks + 4;
            if constexpr (nx < 32) cf[nx & 7] = rdG(k, nx); else if constexpr (k < 7) cf[nx & 7] = rdG(k + 1, nx - 32);
#pragma unroll
            for (int b = 0; b < 2; ++b) a2[k & 1][ks & 1][b] = mm(wf[ks >> 1][b], cf[ks & 7], ks < 2 ? f32x4{} : a2[k & 1][ks & 1][b]);
            if constexpr (k == 4) { if constexpr (ks < 3) out_tile(ks + 1, acc[2 * ks + 2][0], acc[2 * ks + 2][1], acc[2 * ks + 3][0], acc[2 * ks + 3][1]); }
            else if constexpr (ks == 3) out_tile(k - 1, a2[(k - 1) & 1][0][0], a2[(k - 1) & 1][0][1], a2[(k - 1) & 1][1][0], a2[(k - 1) & 1][1][1]);
            tail(KI);
            SBAR();
        }
    });
    out_tile(7, a2[1][0][0], a2[1][0][1], a2[1][1][0], a2[1][1][1]);
}

DI void ld_x4(u32x4& d, const void* sbase, unsigned voff) { asm volatile("global_load_dwordx4 %0, %1, %2" : "=v"(d) : "v"(voff), "s"(sbase)); }
DI void ld_u16_nop(unsigned& d, const void* sbase, unsigned voff) { asm volatile("s_nop 4\n\tglobal_load_ushort %0, %1, %2" : "=v"(d) : "v"(voff), "s"(sbase)); }
DI float lo16(unsigned v) { return __uint_as_float(v << 16); }
DI float hi16(unsigned v) { return __uint_as_float(v & 0xFFFF0000u); }
struct Params {
    const u16 *ax, *ckv, *wq, *wuk, *wuv, *wo, *wr, *wg, *wu, *wd, *gw;
    char* ws; u16* out;
};
struct Ctx { lptr lds; char* lds_; unsigned lds0; int tid, lane, wid, bid; unsigned tag; unsigned* sy; };
typedef __attribute__((address_space(4))) const Params* kparams_t;
DI Params load_params() {
    kparams_t pp = (kparams_t)__builtin_amdgcn_kernarg_segment_ptr();
    asm volatile("" : "+s"(pp));
    Params P;
    P.ax = pp->ax; P.ckv = pp->ckv; P.wq = pp->wq; P.wuk = pp->wuk; P.wuv = pp->wuv; P.wo = pp->wo; P.wr = pp->wr; P.wg = pp->wg; P.wu = pp->wu;
    P.wd = pp->wd; P.gw = pp->gw; P.ws = pp->ws; P.out = pp->out;
    return P;
}
DI Ctx make_ctx(char* lds_, char* ws, int wid, unsigned tag) {
    Ctx C;
    C.lds = (lptr)lds_; C.lds_ = lds_; C.lds0 = (unsigned)(uintptr_t)C.lds;
    int lane; asm volatile("v_mbcnt_lo_u32_b32 %0, -1, 0\n\tv_mbcnt_hi_u32_b32 %0, -1, %0" : "=v"(lane));
    asm volatile("" : "+s"(wid), "+s"(tag));
    C.lane = lane; C.wid = wid; C.tid = wid * 64 + lane; C.bid = blockIdx.x; C.tag = tag; C.sy = (unsigned*)(ws + WS_SYNC);
    return C;
}

constexpr int GU_K = 5, GU_KS = 8;
constexpr int QST = 49152;
struct QPre { unsigned voffX[4], voffW[2]; u32x4 rv, wgv, wuv; };
constexpr int MOE_WD = 147456 + 2048;
DI int moe_f0(int bid) { return ((bid & 7) * 28 + ((bid >> 3) < 28 ? (bid >> 3) : 27)) * MOE_COLS; }
DI void q_dma(const QPre& Q, const Params& P, const Ctx& C, int s, int j) {
    const int wid = C.wid, h = (C.bid & 7) * 4 + ((C.bid >> 3) & 3), bi = C.bid >> 5;
    const char* xbase = (const char*)P.ax + (size_t)(((wid >> 2) ? 15 - bi : bi) * 128 + (wid & 3) * 32) * 1024;
    const char* wbase = (const char*)P.wq + h * 256;
    const unsigned b = C.lds0 + ((s + 2) % 3) * QST;
    if (j < 2) glds16(wbase + (size_t)s * 64 * 8192, Q.voffW[j], b + 32768 + (2 * wid + j) * 1024);
    else glds16(xbase + s * 128, Q.voffX[j - 2], b + (4 * wid + j - 2) * 1024);
}
DI void phase_kv(const Params& P, Ctx& C, QPre& Q, unsigned tagv) {
    char* ws = P.ws;
    const int lane = C.lane, wid = C.wid, bid = C.bid, h = (bid & 7) * 4 + ((bid >> 3) & 3), p = bid >> 5;
    const int tok = bid * 8 + wid;
    u32x4 bxv, wrv, gwv;
    ld_x4(bxv, (const char*)P.ax + (size_t)tok * DM * 2, lane * 16); ld_x4(wrv, P.wr, lane * 16); ld_x4(gwv, P.gw, lane * 16);
    unsigned xb_[4], wr_[4], gw_[4];
    float sr = 0.f, sg = 0.f;
    auto pre = [&](auto GI) {
        constexpr int G = GI;
        if constexpr (G == 0) {
            unsigned t;
            asm volatile("v_readfirstlane_b32 %0, %13\n\t"
                         "v_mov_b32 %1, %14\n\tv_mov_b32 %2, %15\n\tv_mov_b32 %3, %16\n\tv_mov_b32 %4, %17\n\t"
                         "v_mov_b32 %5, %18\n\tv_mov_b32 %6, %19\n\tv_mov_b32 %7, %20\n\tv_mov_b32 %8, %21\n\t"
                         "v_mov_b32 %9, %22\n\tv_mov_b32 %10, %23\n\tv_mov_b32 %11, %24\n\tv_mov_b32 %12, %25"
                         : "=&s"(t), "=&v"(xb_[0]), "=&v"(xb_[1]), "=&v"(xb_[2]), "=&v"(xb_[3]), "=&v"(wr_[0]), "=&v"(wr_[1]), "=&v"(wr_[2]), "=&v"(wr_[3]),
                           "=&v"(gw_[0]), "=&v"(gw_[1]), "=&v"(gw_[2]), "=&v"(gw_[3])
                         : "v"(tagv), "v"(bxv[0]), "v"(bxv[1]), "v"(bxv[2]), "v"(bxv[3]), "v"(wrv[0]), "v"(wrv[1]), "v"(wrv[2]), "v"(wrv[3]),
                           "v"(gwv[0]), "v"(gwv[1]), "v"(gwv[2]), "v"(gwv[3]));
            C.tag = t + 1u;
        }
        if constexpr (G >= 1 && G <= 4) {
            constexpr int j = G - 1;
            const float x0 = lo16(xb_[j]), x1 = hi16(xb_[j]);
            sr += x0 * lo16(wr_[j]); sr += x1 * hi16(wr_[j]); sg += x0 * lo16(gw_[j]); sg += x1 * hi16(gw_[j]);
            PIN(sr); PIN(sg);
        }
        if constexpr (G == 5) { sr += dppf<0x128>(sr); sg += dppf<0x128>(sg); sr += dppf<0x124>(sr); sg += dppf<0x124>(sg); PIN(sr); PIN(sg); }
        if constexpr (G == 6) { sr += dppf<0x122>(sr); sg += dppf<0x122>(sg); sr += dppf<0x121>(sr); sg += dppf<0x121>(sg); PIN(sr); PIN(sg); }
        if constexpr (G == 7) {
            auto a = __builtin_amdgcn_permlane16_swap(__float_as_uint(sr), __float_as_uint(sr), false, false); sr = __uint_as_float(a[0]) + __uint_as_float(a[1]);
            auto b = __builtin_amdgcn_permlane16_swap(__float_as_uint(sg), __float_as_uint(sg), false, false); sg = __uint_as_float(b[0]) + __uint_as_float(b[1]);
            PIN(sr); PIN(sg);
        }
        if constexpr (G == 8) {
            auto a = __builtin_amdgcn_permlane32_swap(__float_as_uint(sr), __float_as_uint(sr), false, false); sr = __uint_as_float(a[0]) + __uint_as_float(a[1]);
            auto b = __builtin_amdgcn_permlane32_swap(__float_as_uint(sg), __float_as_uint(sg), false, false); sg = __uint_as_float(b[0]) + __uint_as_float(b[1]);
            PIN(sr); PIN(sg);
        }
        if constexpr (G == 10) { if (lane == 0) { store_f32_wt((float*)(P.ws + WS_R) + tok, bfround(sr)); store_f32_wt((float*)(P.ws + WS_G) + tok, sigmoidf_(sg)); } }
    };
    auto mid = [&]() { if (C.tid == 0) flag_store(C.sy + SY_R + bid, C.tag); };
    const char* cbase = (const char*)P.ckv + (size_t)p * 256 * 1024;
    const char* kbase = (const char*)P.wuk + h * 256;
    const char* vbase = (const char*)P.wuv + h * 256;
    const unsigned outoff = (unsigned)((h * 64 + 8 * p) * 8 + 2 * (wid & 3)) * 1024;
    const unsigned kvbytes = (unsigned)((size_t)SEQ * NHD * 2);
    unsigned rp[4];
    auto tail = [&](auto KI) {
        constexpr int k = 4 + KI / 32, ks = KI % 32;
        if constexpr (k == 4 && ks == 20) {
            const int ln = lane_now();
#pragma unroll
            for (int j = 0; j < 4; ++j) { const int r = 8 * j + (ln >> 3); Q.voffX[j] = r * 1024 + (((ln & 7) ^ ((r >> 1) & 7)) << 4); }
#pragma unroll
            for (int j = 0; j < 2; ++j) { const int row = 4 * (2 * wid + j) + (ln >> 4); Q.voffW[j] = row * 8192 + (((ln & 15) ^ ((row & 3) << 2)) << 4); }
            if (wid == 0) {
#pragma unroll
                for (int j = 0; j < 4; ++j) asm volatile("global_load_dword %0, %1, %2 sc1" : "=v"(rp[j]) : "v"(ln * 4), "s"(C.sy + SY_R + 64 * j));
            }
        }
        if constexpr (k == GU_K && ks == GU_KS) ld_x4(Q.wgv, (const char*)P.wg + moe_f0(bid) * 2, (wid * 64 + lane_now()) * (FF * 2));
        if constexpr (k == GU_K && ks == GU_KS + 1) ld_x4(Q.wuv, (const char*)P.wu + moe_f0(bid) * 2, (wid * 64 + lane_now()) * (FF * 2));
        if constexpr (k == 6 && ks == 8) glds16((const char*)P.wd + (size_t)(moe_f0(bid) + wid) * 1024, lane_now() * 16, C.lds0 + MOE_WD + wid * 1024);
        if constexpr (k == 5 && ks >= 28) q_dma(Q, P, C, 0, ks - 28);
        if constexpr (k == 6 && ks < 2) q_dma(Q, P, C, 0, 4 + ks);
        if constexpr (k == 6 && ks == 20) {
            if (wid == 0) {
                asm volatile("s_waitcnt vmcnt(13)" : "+v"(rp[0]), "+v"(rp[1]), "+v"(rp[2]), "+v"(rp[3]));
                if (__builtin_expect(!__all(rp[0] == C.tag && rp[1] == C.tag && rp[2] == C.tag && rp[3] == C.tag), 0)) wait_flags<4>(C.sy + SY_R, 256, C.tag, lane_now());
            }
        }
        if constexpr (k == 6 && ks >= 28) q_dma(Q, P, C, 1, ks - 28);
        if constexpr (k == 7 && ks < 2) q_dma(Q, P, C, 1, 4 + ks);
        if constexpr (k == 7 && ks == 5) asm volatile("global_load_dwordx4 %0, %1, %2 sc1" : "=v"(Q.rv) : "v"((wid * 64 + lane_now()) * 16), "s"(P.ws + WS_R));
    };
    if (wid < 4) kv_body<false>(cbase, kbase, vbase, make_rsrc(ws + WS_KF, kvbytes), outoff, C.lds, C.lds0, wid, lane, pre, mid, tail);
    else         kv_body<true>(cbase, kbase, vbase, make_rsrc(ws + WS_VF, kvbytes), outoff, C.lds, C.lds0, wid, lane, pre, mid, tail);
}

DI unsigned umax_(unsigned a, unsigned b) { return a > b ? a : b; }
DI unsigned umin_(unsigned a, unsigned b) { return a < b ? a : b; }

constexpr int K2_QB = 131072;
constexpr float QSCALE = 0.08838834764831845f * 1.4426950408889634f;
constexpr float THR = 8.0f;
constexpr int RING0 = 3;

#define SETPRIO(x) __builtin_amdgcn_s_setprio(x)
struct SMState { float mhat, l; f32x16 negm; };
DI float max3(float a, float b, float c) { return fmaxf(fmaxf(a, b), c); }

#define EXP2(x) __builtin_amdgcn_exp2f(x)
#define VOFF(i) (((i) >> 3) * 8192 + (2 * ((i) & 3) + (((i) >> 2) & 1)) * 1024)
#define KOFF(i) (((i) & 1) * 8192 + ((i) >> 1) * 1024)
DI float fadd_s(float a, float b) { float r; asm("v_add_f32_e32 %0, %1, %2" : "=v"(r) : "v"(a), "v"(b)); return r; }
DI float max3_s(float a, float b, float c) { float r; asm("v_max3_f32 %0, %1, %2, %3" : "=v"(r) : "v"(a), "v"(b), "v"(c)); return r; }
DI float max2_s(float a, float b) { float r; asm("v_max_f32_e32 %0, %1, %2" : "=v"(r) : "v"(a), "v"(b)); return r; }
template <int OFF> DI void glds_step(const void* sbase, unsigned voff, unsigned lds_addr) {
    if (OFF == 0) asm volatile("s_mov_b32 m0, %2\n\ts_nop 0\n\tglobal_load_lds_dwordx4 %0, %1 sc1" :: "v"(voff), "s"(sbase), "s"(lds_addr));
    else asm volatile("global_load_lds_dwordx4 %0, %1 offset:%c2 sc1" :: "v"(voff), "s"(sbase), "i"(OFF));
}
DI f32x16 mfma_c(bf16x8 a, bf16x8 b, const f32x16& c) {
    f32x16 d; asm volatile("v_mfma_f32_32x32x16_bf16 %0, %1, %2, %3" : "=&v"(d) : "v"(a), "v"(b), "v"(c)); return d;
}
struct DmaJob { const char* src0; const char* src1; unsigned dst0, dst1; bool has0, has1; };

constexpr int LA = 4;
template <bool QK, bool FIN, class Mid>
DI void attn_A(lptr kp, const lptr& vpn, const bf16x8 (&qf)[8], const f32x16& negm, const bf16x8 (&kpre)[LA], bf16x8 (&vpre)[LA], f32x16& s0, f32x16& s1,
               const f32x16& p0, const f32x16& p1, bf16x8 (&pf)[4], float& l, const DmaJob& dj, unsigned vlin, Mid&& mid) {
    bf16x8 fr[16];
    unsigned pw[16];
    float sa = 0.f, sb = 0.f;
#pragma unroll
    for (int i = 0; i < LA; ++i) fr[i] = kpre[i];
#pragma unroll
    for (int i = 0; i < 16; ++i) {
        if (QK && i + LA < 16) fr[i + LA] = ldsr128(kp + KOFF(i + LA));
        if (i + LA >= 16) vpre[i + LA - 16] = ldsr128(vpn + VOFF(i + LA - 16));
        if (QK) {
            if (i == 0) s0 = mfma_c(fr[0], qf[0], negm);
            else if (i == 1) s1 = mfma_c(fr[1], qf[0], negm);
            else if (i & 1) s1 = MFMA(fr[i], qf[i >> 1], s1);
            else s0 = MFMA(fr[i], qf[i >> 1], s0);
        }
        if (FIN) {
            const float e0 = i < 8 ? p0[2 * i] : p1[2 * i - 16], e1 = i < 8 ? p0[2 * i + 1] : p1[2 * i - 15];
            if (i == 0) { sa = e0; sb = e1; } else { sa = fadd_s(sa, e0); sb = fadd_s(sb, e1); PIN(sa); PIN(sb); }
            pw[i] = cvtpk(e0, e1);
            PIN(pw[i]);
        }
        if (i == 3) mid();
        if (QK) {
            if (i == 1 && dj.has0) { glds_step<0>(dj.src0, vlin, dj.dst0); glds_step<1024>(dj.src0, vlin, dj.dst0); }
            if (i == 5 && dj.has0) { glds_step<2048>(dj.src0, vlin, dj.dst0); glds_step<3072>(dj.src0, vlin, dj.dst0); }
            if (i == 9 && dj.has1) { glds_step<0>(dj.src1, vlin, dj.dst1); glds_step<1024>(dj.src1, vlin, dj.dst1); }
            if (i == 13 && dj.has1) { glds_step<2048>(dj.src1, vlin, dj.dst1); glds_step<3072>(dj.src1, vlin, dj.dst1); }
        }
        SBAR();
    }
    if (FIN) {
        l += sa + sb;
#pragma unroll
        for (int i = 0; i < 4; ++i) { u32x4 w = {pw[4 * i], pw[4 * i + 1], pw[4 * i + 2], pw[4 * i + 3]}; pf[i] = __builtin_bit_cast(bf16x8, w); }
    }
}
template <bool PV, bool SM, class Mid>
DI void attn_B(lptr vp, const lptr& kpn, f32x16 (&o)[4], const bf16x8 (&pf)[4], const bf16x8 (&vpre)[LA], bf16x8 (&kpre)[LA],
               f32x16& s0, f32x16& s1, SMState& st, bool first, bool mask, int qrel  , bool& pend, float& fpend, Mid&& mid) {
    bf16x8 fr[16];
    float ma = 0.f, mb = 0.f, mc = 0.f;
#pragma unroll
    for (int i = 0; i < LA; ++i) fr[i] = vpre[i];
    if (SM && __builtin_expect(mask, 0)) {
        const float NEG = -__builtin_inff();
#pragma unroll
        for (int r = 0; r < 16; ++r) { const int c = (r & 3) + 8 * (r >> 3) + 16 * ((r >> 2) & 1);
            if (c > qrel) s0[r] = NEG;
            if (c + 32 > qrel) s1[r] = NEG; }
    }
    SETPRIO(1);
    static_for<16>([&](auto I) {
        constexpr int i = I;
        if (PV && i + LA < 16) fr[i + LA] = ldsr128(vp + VOFF(i + LA));
        if (i + LA >= 16) kpre[i + LA - 16] = ldsr128(kpn + KOFF(i + LA - 16));
        if (PV) o[i & 3] = MFMA(fr[i], pf[i >> 2], o[i & 3]);
        if (SM) {
            if (i == 0) { ma = max3(s0[0], s0[1], s0[2]); mb = max3(s1[0], s1[1], s1[2]); mc = max3(s0[3], s0[4], s1[3]); PIN(ma); PIN(mb); PIN(mc); }
            if (i == 1) { ma = max3_s(ma, s0[5], s0[6]); mb = max3_s(mb, s1[4], s1[5]); mc = max3_s(mc, s0[7], s1[6]); PIN(ma); PIN(mb); PIN(mc); }
            if (i == 2) { ma = max3_s(ma, s0[8], s0[9]); mb = max3_s(mb, s1[7], s1[8]); mc = max3_s(mc, s0[10], s1[9]); PIN(ma); PIN(mb); PIN(mc); }
            if (i == 3) { ma = max3_s(ma, s0[11], s0[12]); mb = max3_s(mb, s1[10], s1[11]); mc = max3_s(mc, s0[13], s1[12]); PIN(ma); PIN(mb); PIN(mc); }
            if (i == 4) { ma = max3_s(ma, s0[14], s0[15]); mb = max3_s(mb, s1[13], s1[14]); mc = max2_s(mc, s1[15]); PIN(ma); PIN(mb); PIN(mc); }
            if (i == 5) {
                float rm = max3_s(ma, mb, mc);
                auto rr = __builtin_amdgcn_permlane32_swap(__float_as_uint(rm), __float_as_uint(rm), false, false);
                rm = max2_s(__uint_as_float(rr[0]), __uint_as_float(rr[1]));
                if (__builtin_expect(first || __any(rm > THR), 0)) {
                    const float dl = first ? rm : fmaxf(rm, 0.f), f = first ? 1.0f : __builtin_amdgcn_exp2f(-dl);
                    st.mhat += dl; st.l *= f;
#pragma unroll
                    for (int r = 0; r < 16; ++r) { s0[r] -= dl; s1[r] -= dl; st.negm[r] = -st.mhat; }
                    pend = !first; fpend = f;
                }
            }
            if (i >= 6) {
                constexpr int e0 = i < 14 ? 3 * (i - 6) : 24 + 4 * (i - 14), e1 = i < 14 ? e0 + 3 : e0 + 4;
#pragma unroll
                for (int e = e0; e < e1; ++e) { if (e < 16) { s0[e] = EXP2(s0[e]); } else { s1[e - 16] = EXP2(s1[e - 16]); } }
                PIN(s0); PIN(s1);
            }
        }
        if (i == 7) mid();
        SBAR();
    });
    SETPRIO(0);
}

struct Routed { int sel0, sel1; float sig0, sig1; };
DI void phase_attn(const Params& P, const Ctx& C, QPre& Q, Routed& R) {
    char* ws = P.ws; char* lds_ = C.lds_;
    const u16 *wo = P.wo, *bx = P.ax;
    const lptr lds = C.lds;
    const unsigned lds0 = C.lds0;
    const int tid = C.tid, lane = C.lane, l31 = lane & 31, hi = lane >> 5, wid = C.wid;
    const int bid = C.bid, h = (bid & 7) * 4 + ((bid >> 3) & 3), bi = bid >> 5;
    const int grp = wid >> 2, rg = wid & 3;
    const int blkA = bi, blkB = 15 - bi;
    LAS unsigned* sk = (LAS unsigned*)((LAS char*)lds_ + 147456);
    LAS float* hs = (LAS float*)((LAS char*)lds_ + 147456 + 128);
    LAS float* red = (LAS float*)((LAS char*)lds_ + 147456 + 256);
    const int cg = moe_f0(bid) / MOE_COLS;
    unsigned k1 = 0, k2 = 0, xs0, xs1;
    u32x4 skv[4];
    float mv[32], ma0, ma1, hsv[16];
    unsigned wdv[8];
    auto top2 = [&](unsigned b1, unsigned b2) { k2 = umax_(umin_(k1, b1), umax_(k2, b2)); k1 = umax_(k1, b1); };
    auto ins = [&](unsigned key) { k2 = umax_(k2, umin_(k1, key)); k1 = umax_(k1, key); };
    auto moe_gap = [&](auto GI) {
        constexpr int G = GI;
        if constexpr (G == 13 || G == 14) {
            if constexpr (G == 13) asm volatile("" : "+v"(Q.rv));
#pragma unroll
            for (int j = 2 * (G - 13); j < 2 * (G - 13) + 2; ++j) {
                const unsigned u = Q.rv[j] >> 16;
                const unsigned o = (u & 0x8000u) ? (~u & 0xFFFFu) : (u | 0x8000u);
                ins((o << 16) | (unsigned)(0xFFFF - (tid * 4 + j)));
            }
            PIN(k1); PIN(k2);
        }
        if constexpr (G == 15) { top2(dppu<0x128>(k1), dppu<0x128>(k2)); PIN(k1); PIN(k2); }
        if constexpr (G == 16) { top2(dppu<0x124>(k1), dppu<0x124>(k2)); PIN(k1); PIN(k2); }
        if constexpr (G == 17) { top2(dppu<0x122>(k1), dppu<0x122>(k2)); PIN(k1); PIN(k2); }
        if constexpr (G == 18) { top2(dppu<0x121>(k1), dppu<0x121>(k2)); PIN(k1); PIN(k2); }
        if constexpr (G == 19) { auto a = __builtin_amdgcn_permlane16_swap(k1, k1, false, false); auto b = __builtin_amdgcn_permlane16_swap(k2, k2, false, false);
            k1 = a[0]; k2 = b[0]; top2(a[1], b[1]); PIN(k1); PIN(k2); }
        if constexpr (G == 20) { auto a = __builtin_amdgcn_permlane32_swap(k1, k1, false, false); auto b = __builtin_amdgcn_permlane32_swap(k2, k2, false, false);
            k1 = a[0]; k2 = b[0]; top2(a[1], b[1]);
            if (lane == 0) { sk[wid * 2] = k1; sk[wid * 2 + 1] = k2; } }
        if constexpr (G == 29) {
#pragma unroll
            for (int j = 0; j < 4; ++j) skv[j] = *(const LAS u32x4*)(sk + 4 * j);
            k1 = 0; k2 = 0;
        }
        if constexpr (G == 30 || G == 31) {
#pragma unroll
            for (int j = 8 * (G - 30); j < 8 * (G - 30) + 8; j += 2) top2(skv[j >> 2][j & 3], skv[j >> 2][(j & 3) + 1]);
            PIN(k1); PIN(k2);
        }
        if constexpr (G == 32) {
            k1 = __builtin_amdgcn_readfirstlane(k1); k2 = __builtin_amdgcn_readfirstlane(k2);
            R.sel0 = 0xFFFF - (int)(k1 & 0xFFFFu); R.sel1 = 0xFFFF - (int)(k2 & 0xFFFFu);
            ld_u16_nop(xs0, (const char*)bx + R.sel0 * 1024, tid * 2);
            ld_u16_nop(xs1, (const char*)bx + R.sel1 * 1024, tid * 2);
        }
        if constexpr (G == 33) {
            const unsigned o0 = k1 >> 16, o1 = k2 >> 16;
            const float r0 = __uint_as_float(((o0 & 0x8000u) ? (o0 & 0x7FFFu) : (~o0 & 0xFFFFu)) << 16);
            const float r1 = __uint_as_float(((o1 & 0x8000u) ? (o1 & 0x7FFFu) : (~o1 & 0xFFFFu)) << 16);
            R.sig0 = __uint_as_float(__builtin_amdgcn_readfirstlane(__float_as_uint(sigmoidf_(r0))));
            R.sig1 = __uint_as_float(__builtin_amdgcn_readfirstlane(__float_as_uint(sigmoidf_(r1))));
        }
        if constexpr (G >= 61 && G <= 64) {
            if constexpr (G == 61) { asm volatile("" : "+v"(xs0), "+v"(xs1), "+v"(Q.wgv), "+v"(Q.wuv)); ma0 = lo16(xs0); ma1 = lo16(xs1); }
            constexpr int c = 2 * (G - 61);
            const float g0 = lo16(Q.wgv[c >> 1]), g1 = hi16(Q.wgv[c >> 1]), u0 = lo16(Q.wuv[c >> 1]), u1 = hi16(Q.wuv[c >> 1]);
            mv[c] = ma0 * g0; mv[c + 1] = ma0 * g1; mv[8 + c] = ma0 * u0; mv[9 + c] = ma0 * u1;
            mv[16 + c] = ma1 * g0; mv[17 + c] = ma1 * g1; mv[24 + c] = ma1 * u0; mv[25 + c] = ma1 * u1;
            PIN(mv[c]); PIN(mv[c + 1]); PIN(mv[8 + c]); PIN(mv[9 + c]); PIN(mv[16 + c]); PIN(mv[17 + c]); PIN(mv[24 + c]); PIN(mv[25 + c]);
        }
        if constexpr (G >= 65 && G <= 68) {
#pragma unroll
            for (int j = 4 * (G - 65); j < 4 * (G - 65) + 4; ++j) { auto r = __builtin_amdgcn_permlane32_swap(__float_as_uint(mv[j]), __float_as_uint(mv[j + 16]), false, false);
                mv[j] = __uint_as_float(r[0]) + __uint_as_float(r[1]); PIN(mv[j]); }
        }
        if constexpr (G == 69 || G == 70) {
#pragma unroll
            for (int j = 4 * (G - 69); j < 4 * (G - 69) + 4; ++j) { auto r = __builtin_amdgcn_permlane16_swap(__float_as_uint(mv[j]), __float_as_uint(mv[j + 8]), false, false);
                mv[j] = __uint_as_float(r[0]) + __uint_as_float(r[1]); PIN(mv[j]); }
        }
        if constexpr (G == 71) { const bool up_ = lane & 8;
#pragma unroll
            for (int j = 0; j < 4; ++j) { const float send = up_ ? mv[j] : mv[j + 4], keep = up_ ? mv[j + 4] : mv[j]; mv[j] = keep + dppf<0x128>(send); PIN(mv[j]); } }
        if constexpr (G == 72) { const bool up_ = lane & 4;
#pragma unroll
            for (int j = 0; j < 2; ++j) { const float send = up_ ? mv[j] : mv[j + 2], keep = up_ ? mv[j + 2] : mv[j]; mv[j] = keep + dppf<0x1B>(dppf<0x141>(send)); PIN(mv[j]); } }
        if constexpr (G == 73) { const bool up_ = lane & 2;
            const float send = up_ ? mv[0] : mv[1], keep = up_ ? mv[1] : mv[0]; mv[0] = keep + dppf<0x4E>(send);
            mv[0] += dppf<0xB1>(mv[0]);
            if (!(lane & 1)) red[wid * 32 + (lane >> 1)] = mv[0];
        }
        if constexpr (G == 77) {
            if (tid < 16) {
                const int c = tid & 7, t = tid >> 3;
                float g = 0.f, u = 0.f;
#pragma unroll
                for (int w = 0; w < 8; ++w) { g += red[w * 32 + 16 * t + c]; u += red[w * 32 + 16 * t + 8 + c]; }
                g = bfround(g); u = bfround(u);
                hs[t * 8 + c] = bfround(bfround(g * sigmoidf_(g)) * u);
            }
        }
        if constexpr (G == 93) {
#pragma unroll
            for (int j = 0; j < 8; ++j) wdv[j] = *(const LAS u16*)((LAS char*)lds_ + MOE_WD + j * 1024 + tid * 2);
#pragma unroll
            for (int j = 0; j < 4; ++j) { const f32x4 t = *(const LAS f32x4*)(hs + 4 * j); hsv[4 * j] = t[0]; hsv[4 * j + 1] = t[1]; hsv[4 * j + 2] = t[2]; hsv[4 * j + 3] = t[3]; }
        }
        if constexpr (G == 95) {
            float y0 = 0.f, y1 = 0.f;
#pragma unroll
            for (int j = 0; j < 8; ++j) { const float w = lo16(wdv[j]); y0 += hsv[j] * w; y1 += hsv[8 + j] * w; }
            float* yp = (float*)(ws + WS_YP) + (size_t)cg * 1024;
            store_f32_wt(yp + tid, y0); store_f32_wt(yp + 512 + tid, y1);
        }
    };

    const char* Kh = ws + WS_KF + (size_t)h * 64 * 8192;
    const unsigned vlin = lane * 16;
    bf16x8 qf[8];
    {
        lptr xA[4];
#pragma unroll
        for (int ks = 0; ks < 4; ++ks) xA[ks] = lds + (32 * wid + l31) * 128 + (((2 * ks + hi) ^ ((l31 >> 1) & 7)) << 4);
        const int tq = (lane & 15) >> 2, tp = lane & 3, tblk = (lane >> 4) & 1;
        lptr wA[4];
#pragma unroll
        for (int c = 0; c < 4; ++c) wA[c] = lds + 32768 + (8 * hi + tq) * 256 + ((c ^ tq) << 6) + (tp >> 1) * 32 + tblk * 16 + (tp & 1) * 8;
        auto dmaS = [&](int s, int j) { q_dma(Q, P, C, s, j); };
        auto rdW = [&](int s, int i) { const int o = ((s + 2) % 3) * QST + (i >> 2) * 4096; return cat(ldstr(wA[i & 3] + o), ldstr(wA[i & 3] + o + 1024)); };
        auto rdX = [&](int s, int ks) { return ldsr128(xA[ks] + ((s + 2) % 3) * QST); };
        f32x16 acc[4] = {};
        bf16x8 wfr[8], xfr[2];
        unsigned kvf = 0;
        WAIT_BAR(14);
        xfr[0] = rdX(0, 0);
#pragma unroll
        for (int i = 0; i < 4; ++i) wfr[i] = rdW(0, i);
        static_for<128>([&](auto SI) {
            {
                constexpr int s = SI / 16, i = SI % 16;
                if (i == 12 && s < 7) {
                    if (s == 0) { WAIT_BAR(8); } else if (s == 1) { WAIT_BAR(6); } else if (s == 2) { WAIT_BAR(8); } else if (s < 6) { WAIT_BAR(6); } else { WAIT_BAR(0); }
                }
                const int nx = i + 4;
                if (nx < 16) { if ((nx & 3) == 0) xfr[(nx >> 2) & 1] = rdX(s, nx >> 2); wfr[nx & 7] = rdW(s, nx); }
                else if (s < 7) { if (nx == 16) xfr[0] = rdX(s + 1, 0); wfr[nx & 7] = rdW(s + 1, nx - 16); }
                acc[i & 3] = MFMA(wfr[i & 7], xfr[(i >> 2) & 1], acc[i & 3]);
                if (i == 12 || i == 14) { if (s + 3 < 8) dmaS(s + 3, (i - 12) / 2); }
                else if (s == 0) { if (i < 6) dmaS(2, i); }
                else if (i < 12 && i % 3 == 0) { if (s + 2 < 8) dmaS(s + 2, 2 + i / 3); }
                if (s == 6 && grp == 0) {
                    if (i == 12) glds16x4_dev(Kh + rg * 4096, vlin, lds0 + RING0 * 16384 + rg * 4096);
                    if (i == 14) glds16x4_dev(Kh + 16384 + rg * 4096, vlin, lds0 + (RING0 + 2) * 16384 + rg * 4096);
                }
                moe_gap(SI);
                if (s == 1 && i == 13 && tid == 0) flag_store(C.sy + SY_KV + h * 8 + bi, C.tag);
                if (s == 4 && i == 3 && wid == 0)
                    asm volatile("global_load_dword %0, %1, %2 sc1" : "=v"(kvf) : "v"((lane & 7) * 4), "s"(C.sy + SY_KV + h * 8));
                if (s == 5 && i == 13 && wid == 0) {
                    asm volatile("" : "+v"(kvf));
                    if (__builtin_expect(!__all(kvf == C.tag), 0)) wait_flags<1>(C.sy + SY_KV + h * 8, 8, C.tag, lane);
                }
                if (s == 6 && i == 13 && tid == 0) flag_store(C.sy + SY_Y + bid, C.tag);
                SBAR();
            }
        });
#pragma unroll
        for (int c = 0; c < 4; ++c) { qf[2 * c] = pack8(acc[c], 0, QSCALE); qf[2 * c + 1] = pack8(acc[c], 1, QSCALE); }
    }
    const int n1 = 2 * bi + 2;
    auto tile_of = [&](int n, int g) { return n < n1 ? n : n1 + 2 * (n - n1) + g; };
    auto jstar = [&](int tn) { return tn <= 2 * n1 ? tn : tn <= 2 * n1 + 2 ? tn + 1 : 2 * tn - 2 * n1 - 2; };
    auto jg1 = [&](int tn) { return tn == 2 * n1 + 1 ? tn : 2 * tn - 2 * n1 - 1; };
    int tb_d0, tb_d1, tb_kv, tb_rel;
    auto make_job = [&](int tn, int& d0, int& d1) {
        const int n = tn >> 1;
        int a_off, b_off; bool a_ok, b_ok;
        const int kb = (int)WS_KF + h * 64 * 8192 + rg * 4096, vb = (int)WS_VF + h * 64 * 8192 + rg * 4096;
        if (tn & 1) { a_ok = n >= 1 && n <= 17; a_off = vb + tile_of(n - 1, 0) * 16384; b_ok = n >= n1 && n <= 16; b_off = kb + tile_of(n, 1) * 16384; }
        else        { a_ok = n <= 16;           a_off = kb + tile_of(n, 0) * 16384;     b_ok = n - 2 >= n1 && n <= 18; b_off = vb + tile_of(n - 2, 1) * 16384; }
        const int da = a_off | ((jstar(tn) + RING0) & 7) | 8, db = b_off | ((jg1(tn) + RING0) & 7) | 8;
        d0 = a_ok ? da : b_ok ? db : 0; d1 = (a_ok && b_ok) ? db : 0;
    };
    {
        const int n = lane < 19 ? lane : 18;
        make_job(2 * n + grp + 3, tb_d0, tb_d1);
        const int ks = ((n < n1 || grp == 0 ? jstar(2 * n) : jg1(2 * n + 1)) + RING0) & 7;
        const int vs = ((n < n1 || grp == 0 ? jstar(2 * n + 3) : jg1(2 * n + 4)) + RING0) & 7;
        tb_kv = ks | vs << 8;
        tb_rel = 128 * ((grp || n >= n1) ? blkB : blkA) + 32 * rg - 64 * tile_of(n, grp);
    }
    auto job_from = [&](int d0, int d1) -> DmaJob {
        DmaJob d;
        d.has0 = d0 & 8; d.has1 = d1 & 8;
        d.src0 = ws + (unsigned)(d0 & ~4095); d.src1 = ws + (unsigned)(d1 & ~4095);
        d.dst0 = lds0 + (d0 & 7) * 16384 + rg * 4096; d.dst1 = lds0 + (d1 & 7) * 16384 + rg * 4096;
        return d;
    };
    auto job = [&](int n) { return job_from(__builtin_amdgcn_readlane(tb_d0, n), __builtin_amdgcn_readlane(tb_d1, n)); };
    if (grp == 1) {
#pragma unroll
        for (int c = 0; c < 8; ++c) *(LAS bf16x8*)((LAS char*)lds_ + K2_QB + rg * 8192 + c * 1024 + lane * 16) = qf[c];
    }
    f32x16 o[4] = {};
    SMState st; st.mhat = 0.f; st.l = 0.f; st.negm = f32x16{};
    const lptr kp0 = lds + lane * 16;
    char* ctx = ws + WS_CTX;
    const rsrc_t ctxr = make_rsrc(ctx, (unsigned)((size_t)SEQ * NHD * 2));
    auto store_ctx = [&](int blk, int d, const f32x16& v, float inv) {
        const unsigned off = (unsigned)(((4 * blk + rg) * 32 + h) * 8 + 2 * d) * 1024 + lane * 16;
        store_wt(ctxr, off, pack8(v, 0, inv));
        store_wt(ctxr, off + 1024, pack8(v, 1, inv));
    };
    auto half_sum = [&](float v) { auto rr = __builtin_amdgcn_permlane32_swap(__float_as_uint(v), __float_as_uint(v), false, false);
                                   return __uint_as_float(rr[0]) + __uint_as_float(rr[1]); };
    auto kslot = [&](int n) { return kp0 + (__builtin_amdgcn_readlane(tb_kv, n) & 7) * 16384; };
    auto vslot = [&](int n) { return kp0 + ((__builtin_amdgcn_readlane(tb_kv, n) >> 8) & 7) * 16384; };
    WAIT_BAR(0);
    bool first = true, pend = false;
    float invA = 0.f, fpend = 1.f;
    f32x16 sA0, sA1, sB0, sB1; bf16x8 pf[4], vpre[LA], kpre[LA];
#pragma unroll
    for (int i = 0; i < LA; ++i) kpre[i] = ldsr128(kslot(0) + KOFF(i));
    if (grp == 1) { WAIT_BAR(0); }
    const int nsw = grp == 0 ? n1 - 1 : 100;
    auto headB = [&](int n) {
        if (__builtin_expect(n == nsw + 1, 0)) { invA = 1.0f / half_sum(st.l); st.mhat = 0.f; st.l = 0.f; first = true; }
    };
    auto tailB = [&](int n) {
        first = false;
        if (__builtin_expect(pend || (unsigned)(n - nsw) < 3u, 0)) {
            if (n == nsw + 2 && tid == 0) flag_store(C.sy + SY_CTX + blkA * 32 + h, C.tag);
            if (pend) {
#pragma unroll
                for (int d = 0; d < 4; ++d)
#pragma unroll
                    for (int r = 0; r < 16; ++r) o[d][r] *= fpend;
                pend = false;
            }
            if (n == nsw + 1) {
#pragma unroll
                for (int d = 0; d < 4; ++d) store_ctx(blkA, d, o[d], invA);
#pragma unroll
                for (int d = 0; d < 4; ++d) o[d] = f32x16{};
            }
            if (n == nsw) {
#pragma unroll
                for (int c = 0; c < 8; ++c) qf[c] = ldsr128(lds + K2_QB + rg * 8192 + c * 1024 + lane * 16);
                st.negm = f32x16{};
            }
        }
    };
#define BARRIER_L() asm volatile("s_waitcnt lgkmcnt(0)\n\ts_barrier" ::: "memory")
    lptr kcur = kslot(0), vcur = kp0;
    DmaJob dj = job(0);
    int rel = 0;
    auto pinjob = [&]() { asm volatile("" : "+s"(dj.src0), "+s"(dj.src1), "+s"(dj.dst0), "+s"(dj.dst1)); };
    auto midA = [&](int n) { vcur = vslot(n > 0 ? n - 1 : 0); rel = __builtin_amdgcn_readlane(tb_rel, n); asm volatile("" : "+v"(vcur), "+s"(rel)); };
    auto midB = [&](int n) { kcur = kslot(n + 1); dj = job(n + 1); asm volatile("" : "+v"(kcur)); pinjob(); };
#define RELARGS first, rel < 63, rel + l31 - 4 * hi, pend, fpend
    attn_A<true, false>(kcur, vcur, qf, st.negm, kpre, vpre, sA0, sA1, sA0, sA1, pf, st.l, dj, vlin, [&]() { midA(0); });
    BARRIER_L();
    attn_B<false, true>(vcur, kcur, o, pf, vpre, kpre, sA0, sA1, st, RELARGS, [&]() { midB(0); });
    tailB(0);
    WAIT_BAR(0);
#define STEP(n, C0, C1, P0, P1) do { \
        attn_A<true, true>(kcur, vcur, qf, st.negm, kpre, vpre, C0, C1, P0, P1, pf, st.l, dj, vlin, [&]() { midA(n); }); \
        BARRIER_L(); \
        headB(n); \
        attn_B<true, true>(vcur, kcur, o, pf, vpre, kpre, C0, C1, st, RELARGS, [&]() { midB(n); }); \
        tailB(n); \
        if ((n) < 16) { WAIT_BAR(0); } else { BARRIER_L(); }     \
    } while (0)
    unsigned pfd;
    for (int n = 1; n < 17; n += 2) {
        STEP(n, sB0, sB1, sA0, sA1);
        STEP(n + 1, sA0, sA1, sB0, sB1);
    }
#undef STEP
    asm volatile("global_load_dword %0, %1, %2" : "=v"(pfd) : "v"(vlin * 8),
                 "s"((const char*)wo + (size_t)(wid < 2 ? bid * 2 + wid : ((bid & 7) >> 1) * 128 + ((bid >> 3) & 15) * 4 + ((wid - 2) & 3)) * 8192));
    attn_A<false, true>(kcur, vcur, qf, st.negm, kpre, vpre, sB0, sB1, sA0, sA1, pf, st.l, dj, vlin, [&]() { midA(17); });
    BARRIER_L();
    attn_B<true, false>(vcur, kcur, o, pf, vpre, kpre, sB0, sB1, st, false, false, 0, pend, fpend, [&]() {});
    BARRIER_L();
    if (grp == 0) { BARRIER_L(); }
    BARRIER_L();
    {
        const float lsum = half_sum(st.l);
        LAS char* xch = (LAS char*)lds_;
        LAS char* mine = xch + wid * 8192;
        auto put = [&](int dd, const f32x16& v) {
#pragma unroll
            for (int q4 = 0; q4 < 4; ++q4)
                *(LAS f32x4*)(mine + (dd * 4 + q4) * 1024 + lane * 16) = (f32x4){v[4 * q4], v[4 * q4 + 1], v[4 * q4 + 2], v[4 * q4 + 3]};
        };
        if (grp) { put(0, o[0]); put(1, o[1]); } else { put(0, o[2]); put(1, o[3]); }
        LAS float* ml = (LAS float*)(xch + 65536);
        ml[wid * 128 + lane] = st.mhat; ml[wid * 128 + 64 + lane] = lsum;
        BARRIER_L();
        const int ow = wid ^ 4;
        const float m2 = ml[ow * 128 + lane], l2 = ml[ow * 128 + 64 + lane];
        const float mm = fmaxf(st.mhat, m2);
        const float f1 = __builtin_amdgcn_exp2f(st.mhat - mm), f2 = __builtin_amdgcn_exp2f(m2 - mm);
        const float inv = 1.0f / (lsum * f1 + l2 * f2);
        const float c1 = f1 * inv, c2 = f2 * inv;
        const LAS char* theirs = xch + ow * 8192;
        auto fin = [&](int dd, f32x16 v) {
#pragma unroll
            for (int q4 = 0; q4 < 4; ++q4) {
                const f32x4 t = *(const LAS f32x4*)(theirs + (dd * 4 + q4) * 1024 + lane * 16);
#pragma unroll
                for (int e = 0; e < 4; ++e) v[4 * q4 + e] = v[4 * q4 + e] * c1 + t[e] * c2;
            }
            store_ctx(blkB, 2 * grp + dd, v, 1.0f);
        };
        if (grp) { fin(0, o[2]); fin(1, o[3]); } else { fin(0, o[0]); fin(1, o[1]); }
    }
    asm volatile("s_waitcnt vmcnt(0)" : "+v"(pfd));
    BARRIER_L();
    if (tid == 0) { flag_store(C.sy + SY_CTX + blkB * 32 + h, C.tag); flag_store(C.sy + SY_CTX + blkA * 32 + h, C.tag); }
}

constexpr int K3_SLOT = 32768;
DI unsigned take(unsigned src) { unsigned d; asm volatile("v_mov_b32 %0, %1" : "=v"(d) : "v"(src)); return d; }
DI void phase_oproj(const Params& P, const Ctx& C, const Routed& R) {
    char* ws = P.ws;
    const lptr lds = C.lds;
    const unsigned lds0 = C.lds0;
    const int lane = C.lane, l31 = lane & 31, hi = lane >> 5, wid = C.wid;
    const int bid = C.bid, kq = (bid & 7) >> 1, ct = (bid >> 3) & 3, blk = 2 * (bid >> 5) + (bid & 1);
    const int qp = wid >> 2, cb = wid & 3;
    const char* csrc = ws + WS_CTX + (size_t)((4 * blk + (wid >> 1)) * 32 + 8 * kq) * 8192 + (wid & 1) * 2048;
    const char* wsrc = (const char*)P.wo + (size_t)kq * 1024 * 1024 + ct * 256;
    unsigned voffW[2];
#pragma unroll
    for (int j = 0; j < 2; ++j) voffW[j] = (8 * wid + 4 * j + (lane >> 4)) * 1024 + (((lane & 15) ^ ((lane >> 4) << 2)) << 4);
    const unsigned vlin = lane * 16;
    auto dma = [&](int t, int j) {
        const unsigned b = lds0 + (t % 5) * K3_SLOT;
        if (j < 2) { const char* src = csrc + t * 4096 + j * 1024;
            asm volatile("s_mov_b32 m0, %2\n\ts_nop 0\n\tglobal_load_lds_dwordx4 %0, %1 sc1" :: "v"(vlin), "s"(src),
                         "s"(__builtin_amdgcn_readfirstlane(b + (wid >> 1) * 4096 + (wid & 1) * 2048 + j * 1024)) : "memory"); }
        else glds16(wsrc + (size_t)t * 65536, voffW[j - 2], b + 16384 + (8 * wid + 4 * (j - 2)) * 256);
    };
#pragma unroll
    for (int t = 0; t < 5; ++t) { dma(t, 2); dma(t, 3); }
    if (wid == 0) wait_flags<1>(C.sy + SY_CTX + blk * 32 + 8 * kq, 8, C.tag, lane);
    asm volatile("s_barrier" ::: "memory");
#pragma unroll
    for (int t = 0; t < 5; ++t) { dma(t, 0); dma(t, 1); }
    const int tq = (lane & 15) >> 2, tp = lane & 3, tblk = (lane >> 4) & 1;
    const lptr wA = lds + 16384 + (4 * hi + tq) * 256 + ((cb ^ tq) << 6) + tblk * 32 + tp * 8;
    const lptr cA = lds + 2 * qp * 4096 + lane * 16;
    bf16x8 wf[4], cf[4][2];
    auto rd = [&](int G) { const int o = ((G >> 2) % 5) * K3_SLOT, f = G & 3;
        wf[f] = cat(ldstr(wA + o + f * 4096), ldstr(wA + o + f * 4096 + 2048));
        cf[f][0] = ldsr128(cA + o + f * 1024); cf[f][1] = ldsr128(cA + o + 4096 + f * 1024); };
    f32x16 acc[2] = {};
    unsigned yf[4]; u32x4 yv4[4];
    WAIT_BAR(8);
    rd(0); rd(1);
    static_for<64>([&](auto GI) {
        constexpr int G = GI, t = G >> 2, f = G & 3;
        if constexpr (f == 2 && t < 15) {
            if constexpr (t == 0) { WAIT_BAR(6); } else if constexpr (t == 1) { WAIT_BAR(8); } else if constexpr (t == 2) { WAIT_BAR(10); }
            else if constexpr (t <= 11) { WAIT_BAR(12); } else if constexpr (t == 12) { WAIT_BAR(8); } else if constexpr (t == 13) { WAIT_BAR(4); } else { WAIT_BAR(0); }
        }
        if constexpr (G + 2 < 64) rd(G + 2);
        acc[0] = MFMA(wf[f], cf[f][0], acc[0]);
        acc[1] = MFMA(wf[f], cf[f][1], acc[1]);
        if constexpr (f >= 2) { if constexpr (t + 5 < 16) dma(t + 5, f - 2); } else if constexpr (t >= 1 && t + 4 < 16) dma(t + 4, f + 2);
        if (wid == 1) {
            if constexpr (G == 0) {
#pragma unroll
                for (int j = 0; j < 4; ++j) asm volatile("global_load_dword %0, %1, %2 sc1" : "=v"(yf[j]) : "v"(lane * 4), "s"(C.sy + SY_Y + 64 * j));
            }
            if constexpr (G == 15) {
                const bool ok = take(yf[0]) == C.tag && take(yf[1]) == C.tag && take(yf[2]) == C.tag && take(yf[3]) == C.tag;
                if (__builtin_expect(!__all(ok), 0)) wait_flags<4>(C.sy + SY_Y, 256, C.tag, lane);
#pragma unroll
                for (int j = 0; j < 4; ++j) {
                    const int pp = lane + 64 * j < MOE_WGS ? lane + 64 * j : MOE_WGS - 1;
                    asm volatile("global_load_dwordx4 %0, %1, %2 sc1" : "=v"(yv4[j]) : "v"(pp * 4096), "s"(ws + WS_YP + bid * 16));
                }
            }
            if constexpr (G == 31) {
                f32x4 y = {0.f, 0.f, 0.f, 0.f};
#pragma unroll
                for (int j = 0; j < 4; ++j)
#pragma unroll
                    for (int e = 0; e < 4; ++e) { const float v = __uint_as_float(take(yv4[j][e])); y[e] += lane + 64 * j < MOE_WGS ? v : 0.f; }
#pragma unroll
                for (int e = 0; e < 4; ++e) y[e] = wave_sum(y[e], lane);
                if (lane < 4) store_f32_wt((float*)(ws + WS_Y) + bid * 4 + lane, lane == 0 ? y[0] : lane == 1 ? y[1] : lane == 2 ? y[2] : y[3]);
            }
            if constexpr (G == 47) { if (lane == 0) flag_store(C.sy + SY_Y2 + bid, C.tag); }
        }
        SBAR();
    });
    const int tile = blk * 4 + ct;
    const rsrc_t xr = make_rsrc(ws + WS_X, 64u * 16u * 16384u);
#pragma unroll
    for (int a = 0; a < 2; ++a) {
        const int qg = 2 * qp + a;
        if (qg != kq) {
            const unsigned off = (unsigned)(((tile * 4 + qg) * 4 + kq) * 4 + cb) * 4096 + lane * 16;
#pragma unroll
            for (int q4 = 0; q4 < 4; ++q4) {
                const f32x4 v = {acc[a][4 * q4], acc[a][4 * q4 + 1], acc[a][4 * q4 + 2], acc[a][4 * q4 + 3]};
                __builtin_amdgcn_raw_buffer_store_b128(__builtin_bit_cast(u32x4, v), xr, off + q4 * 1024, 0, 16);
            }
        }
    }
    WAIT_BAR(0);
    if (C.tid == 0) flag_store(C.sy + SY_X + tile * 4 + kq, C.tag);
    if (qp != (kq >> 1)) return;
    const f32x16 own = (kq & 1) ? acc[1] : acc[0];
    const int q = (4 * blk + kq) * 32 + l31;
    const float g = __uint_as_float(flag_load((const unsigned*)(ws + WS_G) + q));
    s16x4 xb[4];
#pragma unroll
    for (int q4 = 0; q4 < 4; ++q4) xb[q4] = *(const s16x4*)(P.ax + (size_t)q * DM + ct * 128 + cb * 32 + 8 * q4 + 4 * hi);
    const int i0 = R.sel0, i1 = R.sel1;
    const bool r0 = (q == i0), r1 = (q == i1);
    f32x4 yrow[4] = {};
    if ((i0 >> 5) == 4 * blk + kq || (i1 >> 5) == 4 * blk + kq) {
        wait_flags<1>(C.sy + SY_Y2 + ct * 32, 32, C.tag, lane);
        wait_flags<1>(C.sy + SY_Y2 + 128 + ct * 32, 32, C.tag, lane);
        const rsrc_t yr = make_rsrc(ws + WS_Y, 4096);
#pragma unroll
        for (int q4 = 0; q4 < 4; ++q4)
            yrow[q4] = __builtin_bit_cast(f32x4, __builtin_amdgcn_raw_buffer_load_b128(yr, ((r1 ? 512 : 0) + ct * 128 + cb * 32 + 8 * q4 + 4 * hi) * 4, 0, 16));
    }
    wait_flags<1>(C.sy + SY_X + tile * 4, 4, C.tag, lane);
    f32x4 part[4][4];
#pragma unroll
    for (int k = 0; k < 4; ++k)
#pragma unroll
        for (int q4 = 0; q4 < 4; ++q4)
            part[k][q4] = __builtin_bit_cast(f32x4, __builtin_amdgcn_raw_buffer_load_b128(xr, (k == kq ? 0x7FFFFFF0u : (unsigned)(((tile * 4 + kq) * 4 + k) * 4 + cb) * 4096 + lane * 16 + q4 * 1024), 0, 16));
    {
        const float ysc = r0 ? R.sig0 : r1 ? R.sig1 : 0.f;
        u32x2 w2[4];
#pragma unroll
        for (int q4 = 0; q4 < 4; ++q4) {
            float res[4];
#pragma unroll
            for (int e = 0; e < 4; ++e) {
                float a = 0.f;
#pragma unroll
                for (int k = 0; k < 4; ++k) a += k == kq ? own[4 * q4 + e] : part[k][q4][e];
                float b = bf2f((u16)xb[q4][e]);
                if (r0 || r1) b += yrow[q4][e] * ysc;
                res[e] = g * a + (1.0f - g) * b;
            }
            w2[q4] = (u32x2){cvtpk(res[0], res[1]), cvtpk(res[2], res[3])};
        }
        const rsrc_t outr = make_rsrc(P.out, SEQ * DM * 2);
#pragma unroll
        for (int k = 0; k < 4; k += 2) {
            auto rx = __builtin_amdgcn_permlane32_swap(w2[k][0], w2[k + 1][0], false, false);
            auto ry = __builtin_amdgcn_permlane32_swap(w2[k][1], w2[k + 1][1], false, false);
            const u32x4 v = {rx[0], ry[0], rx[1], ry[1]};
            __builtin_amdgcn_raw_buffer_store_b128(v, outr, (q * DM + ct * 128 + cb * 32 + 8 * k + 8 * hi) * 2, 0, 16);
        }
    }
}

__global__ __launch_bounds__(512, 2)
void mega_kernel(Params Parg) {
    extern __shared__ __attribute__((aligned(16))) char lds_[];
    const int wid = __builtin_amdgcn_readfirstlane(threadIdx.x >> 6);
    unsigned tag;
    Routed R;
    QPre Q;
    {
        const Params P = load_params();
        unsigned tagv;
        asm volatile("global_load_dword %0, %1, %2 sc1" : "=v"(tagv) : "v"(0), "s"((const unsigned*)(P.ws + WS_SYNC) + SY_EPOCH));
        Ctx C = make_ctx(lds_, P.ws, wid, 0u);
        phase_kv(P, C, Q, tagv);
        tag = __builtin_amdgcn_readfirstlane(C.tag);
    }
    { const Params P = load_params(); phase_attn(P, make_ctx(lds_, P.ws, wid, tag), Q, R); }
    {
        const Params P = load_params();
        const Ctx C = make_ctx(lds_, P.ws, wid, tag);
        phase_oproj(P, C, R);
        if (C.tid == 0) __hip_atomic_fetch_add(C.sy + SY_EPOCH, 1u, __ATOMIC_RELAXED, __HIP_MEMORY_SCOPE_AGENT);
    }
}

extern "C" void kernel_launch(void* const* d_in, const int* in_sizes, int n_in, void* d_out, int out_size, void* d_ws, size_t ws_size,
                              hipStream_t stream) {
    const u16* ax = (const u16*)d_in[0]; const u16* ckv = (const u16*)d_in[1]; const u16* wq = (const u16*)d_in[2];
    const u16* wuk = (const u16*)d_in[3]; const u16* wuv = (const u16*)d_in[4]; const u16* wo = (const u16*)d_in[5];
      const u16* wr = (const u16*)d_in[7]; const u16* wg = (const u16*)d_in[8];
    const u16* wu = (const u16*)d_in[9]; const u16* wd = (const u16*)d_in[10]; const u16* gw = (const u16*)d_in[11];
    char* ws = (char*)d_ws;
    (void)hipFuncSetAttribute((const void*)mega_kernel, hipFuncAttributeMaxDynamicSharedMemorySize, 163840);
    Params P{ax, ckv, wq, wuk, wuv, wo, wr, wg, wu, wd, gw, ws, (u16*)d_out};
    mega_kernel<<<256, 512, 163840, stream>>>(P);
}
```
